```python
import torch

def _softmax(x, axis=-1):
    return torch.softmax(x, dim=axis)
def _gelu(x):
    return torch.nn.functional.gelu(x, approximate="tanh")
def _silu(x):
    return torch.nn.functional.silu(x)
def _sigmoid(x):
    return torch.sigmoid(x)
def _layernorm(x, axis=-1, eps=1e-5):
    m = x.mean(dim=axis, keepdim=True)
    v = x.var(dim=axis, keepdim=True, unbiased=False)
    return (x - m) / torch.sqrt(v + eps)
def _scatter_add(tgt, idx, src, axis=0):
    return tgt.clone().index_add_(axis, idx, src)
def _segment_sum(src, seg, n):
    out = torch.zeros((n,) + src.shape[1:], dtype=src.dtype)
    return out.index_add_(0, seg, src)
def _pad(x, pw):
    flat = tuple(v for pair in reversed(pw) for v in pair)
    return torch.nn.functional.pad(x, flat)

kernel_name = 'recurrent_scan__sliding_block__geglu_ffn__prompt'

def setup_inputs():

    _w = (lambda a: (lambda t: t.to(torch.bfloat16) if t.is_floating_point() else t)(torch.as_tensor(a)))
    def _A_setup():
        B, S, D = 1, 2048, 512
        return {
            "x": _w((lambda *s: torch.randn(*s, dtype=torch.bfloat16) * 0.05)(B, S, D)),
            "wqkv": _w((lambda *s: torch.randn(*s, dtype=torch.bfloat16) * 0.05)(D, 3 * D)),
            "wo": _w((lambda *s: torch.randn(*s, dtype=torch.bfloat16) * 0.05)(D, D)),
        }
    _A_in = _A_setup()

    def _B_setup():
        B, S, D, F = 1, 2048, 512, 1792
        return {
            "x": _w((lambda *s: torch.randn(*s, dtype=torch.bfloat16) * 0.05)(B, S, D)),
            "w_gate": _w((lambda *s: torch.randn(*s, dtype=torch.bfloat16) * 0.05)(D, F)),
            "w_up": _w((lambda *s: torch.randn(*s, dtype=torch.bfloat16) * 0.05)(D, F)),
            "w_down": _w((lambda *s: torch.randn(*s, dtype=torch.bfloat16) * 0.05)(F, D)),
        }
    _B_in = _B_setup()

    return {
        "A_x": _A_in["x"],
        "A_wqkv": _A_in["wqkv"],
        "A_wo": _A_in["wo"],
        "B_x": _B_in["x"],
        "B_w_gate": _B_in["w_gate"],
        "B_w_up": _B_in["w_up"],
        "B_w_down": _B_in["w_down"],
    }

def reference(A_x, A_wqkv, A_wo, B_x, B_w_gate, B_w_up, B_w_down):
    def _A_ref(x, wqkv, wo):
        B, S, D = x.shape
        H = 32; Dh = D // H; W = 256
        qkv = (lambda x, *d: x.permute(*d))((x @ wqkv).reshape(B, S, 3, H, Dh), 2, 0, 3, 1, 4)
        q, k, v = qkv[0], qkv[1], qkv[2]
        scores = torch.einsum("bhqd,bhkd->bhqk", q, k) * (Dh ** -0.5)
        idx = torch.arange(S)
        mask = (idx[:, None] - idx[None, :] >= 0) & (idx[:, None] - idx[None, :] < W)
        scores = torch.where(mask[None, None], scores, -1e9)
        p = _softmax(scores, axis=-1)
        o = torch.einsum("bhqk,bhkd->bhqd", p, v)
        return (lambda x, *d: x.permute(*d))(o, 0, 2, 1, 3).reshape(B, S, D) @ wo

    def _B_ref(x, w_gate, w_up, w_down):
        return (_gelu(x @ w_gate) * (x @ w_up)) @ w_down

    x = A_x
    xn = _layernorm(x, axis=-1)
    s = _A_ref(xn, A_wqkv, A_wo)
    g = _silu(_B_ref(xn, B_w_gate, B_w_up, B_w_down))
    return x + s * g
```

```cpp
#include <hip/hip_runtime.h>
#include <hip/hip_bf16.h>
#include <stdint.h>

#define FUSED 1

typedef short bf16x8 __attribute__((ext_vector_type(8)));
typedef short s16x4 __attribute__((ext_vector_type(4)));
typedef float f32x16 __attribute__((ext_vector_type(16)));
typedef float f32x4 __attribute__((ext_vector_type(4)));
typedef float f32x2 __attribute__((ext_vector_type(2)));
typedef unsigned u32x4 __attribute__((ext_vector_type(4)));
typedef unsigned u32x2 __attribute__((ext_vector_type(2)));
typedef __bf16 bf2 __attribute__((ext_vector_type(2)));
typedef __amdgpu_buffer_rsrc_t rsrc_t;
#define LAS __attribute__((address_space(3)))
#define DI __device__ __forceinline__
#define SBAR() __builtin_amdgcn_sched_barrier(0)
#define PIN(x) asm volatile("" : "+v"(x))
#define MFMA(a, b, c) __builtin_amdgcn_mfma_f32_32x32x16_bf16((a), (b), (c), 0, 0, 0)
#define WAITBAR(vm, lg) asm volatile("s_waitcnt vmcnt(" #vm ") lgkmcnt(" #lg ")\n\ts_barrier" ::: "memory")

#define STAMP2(i)
#define STAMP(i)
#define STAMPR(i)
namespace {
constexpr unsigned FLAG_STRIDE = 64;
constexpr unsigned WS_EPOCH = 0;
constexpr unsigned WS_FQ = 1024;
constexpr unsigned WS_FH = WS_FQ + 16 * 24 * FLAG_STRIDE;
constexpr unsigned WS_FO = WS_FH + 16 * 16 * FLAG_STRIDE;
constexpr unsigned WS_QKV = 65536;
constexpr unsigned SEC_BYTES = 32 * 64 * 1024;
constexpr unsigned WS_H = WS_QKV + 3 * SEC_BYTES;
constexpr unsigned WS_O = WS_H + 64 * 112 * 1024;
constexpr unsigned WS_END = WS_O + 64 * 32 * 1024;
static_assert(WS_FO + 256 * FLAG_STRIDE <= WS_QKV, "flags overlap");
constexpr int SLOT = 32800, SUB1 = 16416;
constexpr int AUX = 4 * SLOT;
constexpr int REG = 32768;
constexpr int LDS_BYTES = 163840;
constexpr float QSCALE = 0.25f * 1.4426950408889634f;
constexpr unsigned SPIN_CAP = 1u << 18;

struct Params {
  const void *x, *wqkv, *wo, *wg, *wu, *wd;
  void *out, *ws;
};

DI rsrc_t mk_rsrc(const void* p, unsigned bytes) { return __builtin_amdgcn_make_buffer_rsrc((void*)p, 0, bytes, 0x00020000); }
DI void glds(rsrc_t rs, unsigned voff, unsigned soff, unsigned lds) {
  asm volatile("s_mov_b32 m0, %2\n\ts_nop 0\n\tbuffer_load_dwordx4 %0, %1, %3 offen lds" ::"v"(voff), "s"(rs), "s"(lds), "s"(soff) : "memory");
}
DI u32x4 ld128h(rsrc_t rs, unsigned voff, unsigned soff) {
  u32x4 r;
  asm volatile("buffer_load_dwordx4 %0, %1, %2, %3 offen sc1" : "=v"(r) : "v"(voff), "s"(rs), "s"(soff) : "memory");
  return r;
}
DI void st128h(rsrc_t rs, unsigned voff, unsigned soff, u32x4 d) {
  asm volatile("buffer_store_dwordx4 %0, %1, %2, %3 offen sc1\n\ts_nop 1" ::"v"(d), "v"(voff), "s"(rs), "s"(soff) : "memory");
}
DI u32x2 ld64_now(rsrc_t rs, unsigned voff) {
  u32x2 r;
  asm volatile("buffer_load_dwordx2 %0, %1, %2, 0 offen sc1\n\ts_waitcnt vmcnt(0)" : "=&v"(r) : "v"(voff), "s"(rs) : "memory");
  return r;
}
DI void poll_flags(rsrc_t ws, unsigned voff, u32x2 tag) {
  for (unsigned spin = 0; spin < SPIN_CAP; ++spin) {
    const u32x2 v = ld64_now(ws, voff);
    if (__all(v.x == tag.x && v.y == tag.y)) break;
    __builtin_amdgcn_s_sleep(1);
  }
}
DI void put_flag(rsrc_t ws, unsigned off, u32x2 tag) { __builtin_amdgcn_raw_buffer_store_b64(tag, ws, off, 0, 16); }
DI s16x4 trd(LAS const char* p) { return __builtin_amdgcn_ds_read_tr16_b64_v4i16((LAS s16x4*)p); }
template <int STEP> DI bf16x8 rdW(LAS const char* p) {
  const s16x4 a = trd(p), b = trd(p + STEP);
  return __builtin_shufflevector(a, b, 0, 1, 2, 3, 4, 5, 6, 7);
}
DI unsigned pk(float lo, float hi) {
  const f32x2 v = {lo, hi};
  return __builtin_bit_cast(unsigned, __builtin_convertvector(v, bf2));
}
DI float rbf(float v) { return __builtin_bit_cast(float, pk(0.f, v) & 0xffff0000u); }
DI float dot2(unsigned a, unsigned b, float c) {
  return __builtin_amdgcn_fdot2_f32_bf16(__builtin_bit_cast(bf2, a), __builtin_bit_cast(bf2, b), c, false);
}
DI float colsum(const bf16x8& w, float c) {
  const u32x4 u = __builtin_bit_cast(u32x4, w);
#pragma unroll
  for (int i = 0; i < 4; ++i) c = dot2(u[i], 0x3f803f80u, c);
  return c;
}
DI float swap32_max(float m) {
  auto r = __builtin_amdgcn_permlane32_swap(__float_as_uint(m), __float_as_uint(m), false, false);
  return __builtin_fmaxf(__uint_as_float(r[0]), __uint_as_float(r[1]));
}
DI float swap32_add(float m) {
  auto r = __builtin_amdgcn_permlane32_swap(__float_as_uint(m), __float_as_uint(m), false, false);
  return __uint_as_float(r[0]) + __uint_as_float(r[1]);
}
DI f32x4 cs_get(LAS const char* csp) {
  LAS const float* p = (LAS const float*)csp;
  return f32x4{p[0], p[64], p[128], p[192]};
}
DI void ln_fix(f32x16& acc, const f32x4& cp, const bf16x8& mfrag) {
  const float c = (cp[0] + cp[1]) + (cp[2] + cp[3]);
  const unsigned u0 = __float_as_uint(c);
  const float r1 = c - __uint_as_float(u0 & 0xffff0000u);
  const unsigned u1 = __float_as_uint(r1);
  const float r2 = r1 - __uint_as_float(u1 & 0xffff0000u);
  u32x4 a;
  a[0] = (u0 >> 16) | (u1 & 0xffff0000u);
  a[1] = __float_as_uint(r2) >> 16;
  a[2] = 0;
  a[3] = 0;
  acc = MFMA(__builtin_bit_cast(bf16x8, a), mfrag, acc);
}
DI float geglu(float a, float b, float k1, float k2, float ir2) {
  const float e = __builtin_amdgcn_exp2f(a * __builtin_fmaf(a * a, k2, k1));
  return a * b * __builtin_amdgcn_rcpf(__builtin_fmaf(e, ir2, ir2));
}

struct Wv { int lane, w, g, c; };

template <int I0 = 0, int I1 = 4>
DI void issue_cgroup(const Wv& v, rsrc_t rwd, rsrc_t rwo, unsigned voffC, int ct, int gp, int region) {
  const bool dn = gp < 7;
  const int row0 = (dn ? 448 * v.g + 64 * gp : 128 * v.g + 64 * (gp - 7)) + 32 * v.c;
  const unsigned soff = row0 * 1024 + ct * 128, dst = region * REG + v.g * 8192 + v.c * 4096;
#pragma unroll
  for (int i = I0; i < I1; ++i) glds(dn ? rwd : rwo, voffC, soff + i * 8192, dst + i * 1024);
}

struct AtPre { u32x4 q, k[2], vv[2]; };
DI void at_issue_loads(const Wv& v, rsrc_t ws, int hd, int G, AtPre& a) {
  const unsigned voff = v.lane * 16;
  const int kb = 8 * G - 8 + 2 * v.w;
  a.q = ld128h(ws, voff, WS_QKV + (hd * 64 + 8 * G + v.w) * 1024);
  const int kbc = kb < 0 ? 0 : kb;
#pragma unroll
  for (int i = 0; i < 2; ++i) {
    a.k[i] = ld128h(ws, voff, WS_QKV + SEC_BYTES + (hd * 64 + kbc + i) * 1024);
    a.vv[i] = ld128h(ws, voff, WS_QKV + 2 * SEC_BYTES + (hd * 64 + kbc + i) * 1024);
  }
}
DI unsigned at_flag_off(const Wv& v, int hd, int G) {
  const int l3 = v.lane % 3;
  int kb = 8 * G - 8 + 2 * v.w;
  kb = kb < 0 ? 0 : kb;
  const int rb = l3 == 0 ? 2 * G + (v.w >> 2) : kb >> 2;
  return WS_FQ + (rb * 24 + l3 * 8 + (hd >> 2)) * FLAG_STRIDE;
}
constexpr int KV = 3 * REG;
constexpr int ONES = LDS_BYTES - 1024;
DI void at_stage(const Wv& v, LAS char* L, AtPre& a) {
#pragma unroll
  for (int i = 0; i < 2; ++i) {
    *(LAS u32x4*)(L + KV + (2 * v.w + i) * 1024 + v.lane * 16) = a.k[i];
    *(LAS u32x4*)(L + KV + 16384 + (2 * v.w + i) * 1024 + (v.lane & 31) * 32 + (v.lane >> 5) * 16) = a.vv[i];
  }
  if (v.w == 0) *(LAS u32x4*)(L + ONES + v.lane * 16) = u32x4{0x3f803f80u, 0x3f803f80u, 0x3f803f80u, 0x3f803f80u};
}
DI float max16(const f32x16& s, float m) {
#pragma unroll
  for (int r = 0; r < 16; r += 2) m = __builtin_fmaxf(__builtin_fmaxf(m, s[r]), s[r + 1]);
  return m;
}
DI void pv_step(f32x16& o, const u32x4& p0, const u32x4& p1, LAS const char* va) {
  const bf16x8 v0 = rdW<256>(va), v1 = rdW<256>(va + 512);
  o = MFMA(v0, __builtin_bit_cast(bf16x8, p0), o);
  o = MFMA(v1, __builtin_bit_cast(bf16x8, p1), o);
}
template <bool FULL> DI void at_body(const Wv& v, LAS char* L, rsrc_t ws, int hd, int G, const u32x4& qv) {
  const int lane = v.lane, t = lane & 31, h = lane >> 5;
  const bf16x8 q = __builtin_bit_cast(bf16x8, qv);
  LAS const char* kp = L + KV + v.w * 1024 + lane * 16;
  const int qq = (lane & 15) >> 2, p = lane & 3;
  const bool real = (lane & 16) == 0;
  LAS const char* vp = L + (real ? KV + 16384 + v.w * 1024 + (4 * h + qq) * 32 + (p & 1) * 16 + (p >> 1) * 8 : ONES + (lane & 15) * 8);
  const int vstr = real ? 1024 : 0;
  const int kb0 = FULL ? 1 : 8 - v.w;
  const f32x16 zero = {};
  const float NEG = -1e30f;
  float m = NEG;
  {
    const f32x16 sd = MFMA(*(LAS const bf16x8*)(kp + 8 * 1024), q, zero);
    f32x16 sf;
    if (FULL) sf = MFMA(*(LAS const bf16x8*)(kp), q, zero);
#pragma unroll
    for (int r = 0; r < 16; ++r) {
      const bool dg = ((r & 3) + 8 * (r >> 2) + 4 * h) <= t;
      m = __builtin_fmaxf(m, dg ? sd[r] : (FULL ? sf[r] : NEG));
    }
  }
  if (FULL) {
#pragma unroll
    for (int kb = 1; kb < 8; ++kb) m = max16(MFMA(*(LAS const bf16x8*)(kp + kb * 1024), q, zero), m);
  } else {
    for (int kb = kb0; kb < 8; ++kb) m = max16(MFMA(*(LAS const bf16x8*)(kp + kb * 1024), q, zero), m);
  }
  m = swap32_max(m);
  f32x16 cm;
#pragma unroll
  for (int r = 0; r < 16; ++r) cm[r] = -m;
  f32x16 o = zero;
  {
    const f32x16 sd = MFMA(*(LAS const bf16x8*)(kp + 8 * 1024), q, cm);
    f32x16 sf;
    if (FULL) sf = MFMA(*(LAS const bf16x8*)(kp), q, cm);
    u32x4 pd[2], pf[2];
#pragma unroll
    for (int r = 0; r < 16; r += 2) {
      const int k0 = (r & 3) + 8 * (r >> 2) + 4 * h;
      const bool d0 = k0 <= t, d1 = k0 + 1 <= t;
      const float e0 = __builtin_amdgcn_exp2f(d0 ? sd[r] : (FULL ? sf[r] : NEG));
      const float e1 = __builtin_amdgcn_exp2f(d1 ? sd[r + 1] : (FULL ? sf[r + 1] : NEG));
      const unsigned pp = pk(e0, e1), msk = (d0 ? 0xffffu : 0u) | (d1 ? 0xffff0000u : 0u);
      pd[r >> 3][(r >> 1) & 3] = pp & msk;
      pf[r >> 3][(r >> 1) & 3] = pp & ~msk;
    }
    pv_step(o, pd[0], pd[1], vp + 8 * vstr);
    if (FULL) pv_step(o, pf[0], pf[1], vp);
  }
  auto mid = [&](int kb) {
    const f32x16 s = MFMA(*(LAS const bf16x8*)(kp + kb * 1024), q, cm);
    u32x4 pp[2];
#pragma unroll
    for (int r = 0; r < 16; r += 2) pp[r >> 3][(r >> 1) & 3] = pk(__builtin_amdgcn_exp2f(s[r]), __builtin_amdgcn_exp2f(s[r + 1]));
    pv_step(o, pp[0], pp[1], vp + kb * vstr);
  };
  if (FULL) {
#pragma unroll
    for (int kb = 1; kb < 8; ++kb) mid(kb);
  } else {
    for (int kb = kb0; kb < 8; ++kb) mid(kb);
  }
  const float il = __builtin_amdgcn_rcpf(o[8]);
  u32x4 ov;
#pragma unroll
  for (int i = 0; i < 4; ++i) ov[i] = pk(o[2 * i] * il, o[2 * i + 1] * il);
  st128h(ws, lane * 16, WS_O + ((8 * G + v.w) * 32 + hd) * 1024, ov);
}

#define VMWAIT4(n, a, b, c, d) asm volatile("s_waitcnt vmcnt(" #n ")" : "+v"(a), "+v"(b), "+v"(c), "+v"(d)::"memory")
template <int OFF> DI void c_step(LAS const char* b0, LAS const char* b1, const u32x4& f, f32x16& a0, f32x16& a1) {
  const bf16x8 w0 = rdW<1024>(b0 + OFF), w1 = rdW<1024>(b1 + OFF);
  a0 = MFMA(w0, __builtin_bit_cast(bf16x8, f), a0);
  a1 = MFMA(w1, __builtin_bit_cast(bf16x8, f), a1);
}
template <int REGION> DI void c_group(LAS const char* b0, LAS const char* b1, const u32x4& f0, const u32x4& f1, const u32x4& f2, const u32x4& f3,
                                       f32x16& a0, f32x16& a1) {
  c_step<REGION * REG>(b0, b1, f0, a0, a1);
  c_step<REGION * REG + 2048>(b0, b1, f1, a0, a1);
  c_step<REGION * REG + 4096>(b0, b1, f2, a0, a1);
  c_step<REGION * REG + 6144>(b0, b1, f3, a0, a1);
  PIN(a0); PIN(a1);
}
DI void c_scatter(const Wv& v, LAS char* X, const f32x16& a0, const f32x16& a1, f32x4& own0, f32x4& own1) {
#pragma unroll
  for (int d = 0; d < 4; ++d) {
    const f32x16& a = (d >> 1) ? a1 : a0;
    const int o = 8 * (d & 1);
    const f32x4 p0 = {a[o], a[o + 1], a[o + 2], a[o + 3]}, p1 = {a[o + 4], a[o + 5], a[o + 6], a[o + 7]};
    if (d == v.g) {
      own0 = p0;
      own1 = p1;
    } else {
      LAS char* dst = X + (d * 3 + ((v.g - d - 1) & 3)) * 2048 + v.lane * 16;
      *(LAS f32x4*)dst = p0;
      *(LAS f32x4*)(dst + 1024) = p1;
    }
  }
}
DI void c_gather(const Wv& v, LAS const char* X, f32x4& own0, f32x4& own1) {
  LAS const char* src = X + v.g * 3 * 2048 + v.lane * 16;
#pragma unroll
  for (int i = 0; i < 3; ++i) {
    own0 += *(LAS const f32x4*)(src + i * 2048);
    own1 += *(LAS const f32x4*)(src + i * 2048 + 1024);
  }
}
DI float fin(unsigned xbits, float s, float d) {
  const float sg = d * __builtin_amdgcn_rcpf(1.f + __builtin_amdgcn_exp2f(-1.4426950408889634f * d));
  return __builtin_fmaf(s, sg, __uint_as_float(xbits));
}
DI void phase_c(const Params& P, const Wv& v, LAS char* L, rsrc_t ws, rsrc_t rwd, rsrc_t rwo, unsigned voffC, int rt, int ct, u32x2 tag, bool closer) {
  const int lane = v.lane, h = lane >> 5, tb = 2 * rt + v.c;
  issue_cgroup<3, 4>(v, rwd, rwo, voffC, ct, 3, 4);
  issue_cgroup(v, rwd, rwo, voffC, ct, 4, 3);
  poll_flags(ws, WS_FH + ((rt >> 1) * 16 + 4 * v.g + (lane & 3)) * FLAG_STRIDE, tag);
  STAMP(9);
  const unsigned voff = lane * 16;
  u32x4 hf[28];
#pragma unroll
  for (int i = 0; i < 28; ++i) hf[i] = ld128h(ws, voff, WS_H + (tb * 112 + 28 * v.g + i) * 1024);
  const int qq = (lane & 15) >> 2, p = lane & 3, g1 = (lane >> 4) & 1;
  LAS const char* b0 = L + v.g * 8192 + (4 * h + qq) * 128 + (g1 + (qq & 2)) * 32 + 8 * p;
  LAS const char* b1 = L + v.g * 8192 + (4 * h + qq) * 128 + (g1 + (2 ^ (qq & 2))) * 32 + 8 * p;
  f32x16 d0 = {}, d1 = {}, s0 = {}, s1 = {};
#define BAR() asm volatile("s_waitcnt lgkmcnt(0)\n\ts_barrier" ::: "memory")
  VMWAIT4(24, hf[0], hf[1], hf[2], hf[3]); BAR();
  STAMP(10);
  c_group<0>(b0, b1, hf[0], hf[1], hf[2], hf[3], d0, d1); SBAR();
  VMWAIT4(20, hf[4], hf[5], hf[6], hf[7]); BAR();
  issue_cgroup(v, rwd, rwo, voffC, ct, 5, 0);
  c_group<1>(b0, b1, hf[4], hf[5], hf[6], hf[7], d0, d1); SBAR();
  VMWAIT4(20, hf[8], hf[9], hf[10], hf[11]); BAR();
  issue_cgroup(v, rwd, rwo, voffC, ct, 6, 1);
  c_group<2>(b0, b1, hf[8], hf[9], hf[10], hf[11], d0, d1); SBAR();
  VMWAIT4(20, hf[12], hf[13], hf[14], hf[15]); BAR();
  issue_cgroup(v, rwd, rwo, voffC, ct, 7, 2);
  c_group<4>(b0, b1, hf[12], hf[13], hf[14], hf[15], d0, d1); SBAR();
  VMWAIT4(20, hf[16], hf[17], hf[18], hf[19]); BAR();
  issue_cgroup(v, rwd, rwo, voffC, ct, 8, 4);
  c_group<3>(b0, b1, hf[16], hf[17], hf[18], hf[19], d0, d1); SBAR();
  STAMP(11);
  if (closer) {
#pragma unroll
    for (int i = 0; i < 4; ++i) poll_flags(ws, WS_FO + (i * 64 + lane) * FLAG_STRIDE, tag);
    if (lane == 0) put_flag(ws, WS_EPOCH, tag);
  }
  poll_flags(ws, WS_FO + ((rt >> 2) * 32 + 8 * v.g + (lane & 7)) * FLAG_STRIDE, tag);
  STAMP(12);
  u32x4 of[8];
#pragma unroll
  for (int i = 0; i < 8; ++i) of[i] = ld128h(ws, voff, WS_O + (tb * 32 + 8 * v.g + i) * 1024);
  const rsrc_t rx = mk_rsrc(P.x, 2048 * 512 * 2);
  const unsigned xoff = (32 * tb + (lane & 31)) * 1024 + (64 * ct + 16 * v.g + 4 * h) * 2;
  u32x2 x0, x1;
  asm volatile("buffer_load_dwordx2 %0, %1, %2, 0 offen" : "=v"(x0) : "v"(xoff), "s"(rx) : "memory");
  asm volatile("buffer_load_dwordx2 %0, %1, %2, 0 offen offset:16" : "=v"(x1) : "v"(xoff), "s"(rx) : "memory");
  VMWAIT4(22, hf[20], hf[21], hf[22], hf[23]); BAR();
  c_group<0>(b0, b1, hf[20], hf[21], hf[22], hf[23], d0, d1); SBAR();
  VMWAIT4(18, hf[24], hf[25], hf[26], hf[27]); BAR();
  c_group<1>(b0, b1, hf[24], hf[25], hf[26], hf[27], d0, d1); SBAR();
  STAMP(13);
  f32x4 dw0, dw1, sw0, sw1;
  LAS char* xd = L + (v.c ? 0 : 3 * REG);
  LAS char* xs = L + REG + v.c * REG;
  c_scatter(v, xd, d0, d1, dw0, dw1);
  VMWAIT4(6, of[0], of[1], of[2], of[3]); BAR();
  c_group<2>(b0, b1, of[0], of[1], of[2], of[3], s0, s1); SBAR();
  VMWAIT4(2, of[4], of[5], of[6], of[7]); BAR();
  c_group<4>(b0, b1, of[4], of[5], of[6], of[7], s0, s1); SBAR();
  STAMP(14);
  c_scatter(v, xs, s0, s1, sw0, sw1);
  asm volatile("s_waitcnt vmcnt(0) lgkmcnt(0)\n\ts_barrier" : "+v"(x0), "+v"(x1)::"memory");
  c_gather(v, xd, dw0, dw1);
  c_gather(v, xs, sw0, sw1);
  u32x2 y0, y1;
  y0.x = pk(fin(x0.x << 16, sw0[0], dw0[0]), fin(x0.x & 0xffff0000u, sw0[1], dw0[1]));
  y0.y = pk(fin(x0.y << 16, sw0[2], dw0[2]), fin(x0.y & 0xffff0000u, sw0[3], dw0[3]));
  y1.x = pk(fin(x1.x << 16, sw1[0], dw1[0]), fin(x1.x & 0xffff0000u, sw1[1], dw1[1]));
  y1.y = pk(fin(x1.y << 16, sw1[2], dw1[2]), fin(x1.y & 0xffff0000u, sw1[3], dw1[3]));
  const rsrc_t ro = mk_rsrc(P.out, 2048 * 512 * 2);
  __builtin_amdgcn_raw_buffer_store_b64(y0, ro, xoff, 0, 0);
  __builtin_amdgcn_raw_buffer_store_b64(y1, ro, xoff + 16, 0, 0);
  STAMP(15); STAMPR(31);
}

struct AB {
  bf16x8 X[32];
  bf16x8 wf[3][2];
  bf16x8 mfrag;
  float r, k1, k2, ir2;
  int rb, strip0, dbl0;
  unsigned voffQ, voffF, voffC;
  rsrc_t rq, rf, ws, rwd, rwo;
  int ct;
};
DI void issue_x(const Wv& v, rsrc_t rx, int rb, int kc, int slot) {
  const unsigned v0 = (v.lane >> 4) * 1024 + (((v.lane & 15) ^ (v.lane >> 4)) * 16);
#pragma unroll
  for (int i = 0; i < 4; ++i) glds(rx, v0 ^ (64 * i), (rb * 128 + 16 * v.w + 4 * i) * 1024 + kc * 256, slot * SLOT + (4 * v.w + i) * 1024);
}
DI void issue_q(const Wv& v, const AB& a, int strip, int half, int slot) {
  const unsigned soff = (256 * half + 128 * v.c + 32 * v.g) * 3072 + strip * 128, dst = slot * SLOT + v.c * SUB1 + v.g * 4096;
#pragma unroll
  for (int i = 0; i < 4; ++i) glds(a.rq, a.voffQ, soff + i * 8 * 3072, dst + i * 1024);
}
DI void issue_f(const Wv& v, const AB& a, int dbl, int kq, int slot) {
  const unsigned soff = (128 * kq + 32 * v.g) * 3584 + dbl * 128, dst = slot * SLOT + v.c * SUB1 + v.g * 4096;
#pragma unroll
  for (int i = 0; i < 4; ++i) glds(a.rf, a.voffF, soff + i * 8 * 3584, dst + i * 1024);
}
DI LAS char* cs_slot(LAS char* L, const Wv& v, int j) { return L + AUX + (j * 2 + v.c) * 1024 + v.lane * 4; }
DI void cs_put(LAS char* L, const Wv& v, int j, float c) { *(LAS float*)(cs_slot(L, v, j) + v.g * 256) = c; }
DI void q_store(const Wv& v, const AB& a, const f32x16& acc, int strip, int hh) {
  const float sc = strip < 8 ? a.r * QSCALE : a.r;
  u32x4 o;
#pragma unroll
  for (int i = 0; i < 4; ++i) o[i] = pk(acc[8 * hh + 2 * i] * sc, acc[8 * hh + 2 * i + 1] * sc);
  st128h(a.ws, v.lane * 16, WS_QKV + ((strip * 4 + 2 * v.c + hh) * 64 + 4 * a.rb + v.g) * 1024, o);
}
DI void put_h(const Wv& v, const AB& a, int dbl, int j, const float (&hv)[8]) {
  st128h(a.ws, v.lane * 16, WS_H + ((4 * a.rb + v.g) * 112 + 4 * dbl + 2 * v.c + j) * 1024,
         u32x4{pk(hv[0], hv[1]), pk(hv[2], hv[3]), pk(hv[4], hv[5]), pk(hv[6], hv[7])});
}
struct Bases { unsigned b[4]; };
DI Bases mk_bases(const Wv& v, int g1mul) {
  int l = v.lane;
  PIN(l);
  const int qq = (l & 15) >> 2;
  const int o = ((l >> 4) & 1) * g1mul + (8 * (l >> 5) + qq) * 128 + ((2 * v.c) ^ (qq & 2)) * 32 + 8 * (l & 3);
  Bases r;
#pragma unroll
  for (int i = 0; i < 4; ++i) { r.b[i] = o + (i ^ v.g) * 4096; PIN(r.b[i]); }
  return r;
}
DI void bump(Bases& B, int d) {
#pragma unroll
  for (int i = 0; i < 4; ++i) { B.b[i] += d; PIN(B.b[i]); }
}
#define WRD(B, off, i) rdW<512>((LAS const char*)(size_t)((B).b[(i) >> 1]) + (off) + ((i) & 1) * 2048)

template <int KQ, int XS>
DI void p_step(const Wv& v, AB& a, LAS const char* L, const Bases& B, f32x16& acc, float& s1, float& s2, float& cs) {
  const int t = v.lane & 31;
  const unsigned u = (((v.lane >> 5) ^ (t & 15)) * 16) ^ (64 * v.g);
  LAS const char* xr = L + XS * SLOT + (32 * v.g + t) * 256;
#pragma unroll
  for (int i = 0; i < 8; ++i) {
    a.X[8 * KQ + i] = *(LAS const bf16x8*)(xr + (u ^ (32 * i)));
    const bf16x8 wf = WRD(B, (KQ >> 1) * SLOT + (KQ & 1) * SUB1, i);
    acc = MFMA(wf, a.X[8 * KQ + i], acc);
    if (i < 2) cs = colsum(wf, cs);
    const u32x4 xu = __builtin_bit_cast(u32x4, a.X[8 * KQ + i]);
#pragma unroll
    for (int d = 0; d < 4; ++d) {
      s1 = dot2(xu[d], 0x3f803f80u, s1);
      s2 = dot2(xu[d], xu[d], s2);
    }
  }
  PIN(acc); PIN(s1); PIN(s2); PIN(cs);
  SBAR();
}
enum { K_QQ = 0, K_D0 = 1, K_D1 = 2 };
template <int KIND> DI void rd_step(AB& a, const Bases& B, int s, int ph) {
  const int kq = (s >> 3) & 3, i = s & 7, so = (kq & 1) * SLOT;
  if (KIND == K_QQ) {
    a.wf[ph][0] = WRD(B, so + (i >> 2) * SUB1, (2 * i) & 7);
    a.wf[ph][1] = WRD(B, so + (i >> 2) * SUB1, ((2 * i) & 7) + 1);
  } else {
    a.wf[ph][0] = WRD(B, so, i);
    a.wf[ph][1] = WRD(B, so + 32, i);
  }
}
template <int KIND>
DI void body(const Wv& v, AB& a, LAS char* L, int dbl, bool last, int qstrip, int nq, u32x2 tag, f32x16& p0, f32x16& p1) {
  constexpr int PH0 = KIND == K_D1 ? 2 : 0;
  Bases B = mk_bases(v, KIND == K_QQ ? 32 : SUB1);
  f32x16 a0 = {}, a1 = {};
  float c0 = 0.f, c1 = 0.f, hv[8];
  f32x4 cp0, cp1;
  if (KIND != K_D1) {
    rd_step<KIND>(a, B, 0, 0);
    rd_step<KIND>(a, B, 1, 1);
  }
#pragma unroll
  for (int s = 0; s < 32; ++s) {
    const int kq = s >> 3, i = s & 7;
    if (i == 0) {
      if (KIND == K_QQ) {
        if (kq == 0) issue_q(v, a, a.strip0 + 2, 1, 3);
        else issue_f(v, a, a.dbl0, kq - 1, kq - 1);
      } else {
        if (kq == 0) issue_f(v, a, dbl, 3, 3);
        else if (!last) issue_f(v, a, dbl + 1, kq - 1, kq - 1);
        else if (FUSED) issue_cgroup(v, a.rwd, a.rwo, a.voffC, a.ct, kq - 1, kq - 1);
      }
    }
    if (s == 14) bump(B, 2 * SLOT);
    if (s == 30 && KIND == K_D0) bump(B, -2 * SLOT);
    const bool late = KIND != K_D1 && (s == 6 || s == 7);
    if (!late && (s < 30 || KIND == K_D0)) rd_step<KIND>(a, B, s + 2, (s + 2 + PH0) % 3);
    if (s == 0 || (KIND == K_QQ && s == 16)) cp0 = cs_get(cs_slot(L, v, 0));
    if (s == 0 && KIND == K_D1) cp1 = cs_get(cs_slot(L, v, 1));
    const bf16x8 w0 = a.wf[(s + PH0) % 3][0], w1 = a.wf[(s + PH0) % 3][1];
    if (KIND == K_QQ) {
      f32x16& ac = kq < 2 ? a0 : a1;
      ac = MFMA(w0, a.X[16 * (kq & 1) + 2 * i], ac);
      ac = MFMA(w1, a.X[16 * (kq & 1) + 2 * i + 1], ac);
      PIN(ac);
      if ((i & 3) == 0) {
        float& c = kq < 2 ? c0 : c1;
        c = colsum(w1, colsum(w0, c));
        PIN(c);
      }
      if (s == 2) { ln_fix(p0, cp0, a.mfrag); PIN(p0); }
      if (s == 5 || s == 6) q_store(v, a, p0, a.strip0, s - 5);
      if (s == 13) cs_put(L, v, 0, c0);
      if (s == 18) { ln_fix(a0, cp0, a.mfrag); PIN(a0); }
      if (s == 21 || s == 22) q_store(v, a, a0, a.strip0 + 1, s - 21);
      if (s == 29) cs_put(L, v, 0, c1);
    } else {
      a0 = MFMA(w0, a.X[s], a0);
      a1 = MFMA(w1, a.X[s], a1);
      PIN(a0); PIN(a1);
      if (i < 2) {
        c0 = colsum(w0, c0);
        c1 = colsum(w1, c1);
        PIN(c0); PIN(c1);
      }
      if (s == 2) { ln_fix(p0, cp0, a.mfrag); PIN(p0); }
      if (KIND == K_D0) {
        if (s == 4 || s == 5) q_store(v, a, p0, qstrip, s - 4);
      } else {
        if (s == 3) { ln_fix(p1, cp1, a.mfrag); PIN(p1); }
        const int e0 = s >= 4 && s < 8 ? s - 4 : (s >= 10 && s < 14 ? s - 6 : -1);
        const int e1 = s == 15 ? 0 : (s >= 18 && s < 24 ? s - 17 : (s == 26 ? 7 : -1));
        if (s == 14) put_h(v, a, dbl - 1, 0, hv);
        if (s == 27) put_h(v, a, dbl - 1, 1, hv);
        if (e0 >= 0) { hv[e0] = geglu(p0[e0], p0[8 + e0], a.k1, a.k2, a.ir2); PIN(hv[e0]); }
        if (e1 >= 0) { hv[e1] = geglu(p1[e1], p1[8 + e1], a.k1, a.k2, a.ir2); PIN(hv[e1]); }
      }
      if (s == 28) {
        cs_put(L, v, 0, c0);
        cs_put(L, v, 1, c1);
      }
    }
    SBAR();
    if (i == 0) STAMP2(32 + (KIND == K_D1 ? 12 : 0) + 3 * kq);
    if (i == 7) {
      STAMP2(33 + (KIND == K_D1 ? 12 : 0) + 3 * kq);
      const bool drain = kq == 1 && ((FUSED && KIND == K_D0) || (!FUSED && KIND != K_QQ && last));
      if (drain) WAITBAR(0, 8);
      else if (kq == 3 && KIND != K_D0) WAITBAR(4, 0);
      else WAITBAR(4, 8);
      STAMP2(34 + (KIND == K_D1 ? 12 : 0) + 3 * kq);
      if (FUSED && KIND == K_D0 && kq == 1) {
        if (v.w == 0 && v.lane < nq) put_flag(a.ws, WS_FQ + (a.rb * 24 + a.strip0 + v.lane) * FLAG_STRIDE, tag);
      }
      if (KIND != K_D1 && s == 7) {
        rd_step<KIND>(a, B, 8, 8 % 3);
        rd_step<KIND>(a, B, 9, 9 % 3);
      }
    }
  }
  if (KIND == K_QQ) p0 = a1;
  else { p0 = a0; p1 = a1; }
}

DI u32x2 make_tag(u32x2 e) {
  const unsigned long long z = ((unsigned long long)__builtin_amdgcn_readfirstlane(e.y) << 32 | (unsigned)__builtin_amdgcn_readfirstlane(e.x));
  const unsigned long long t = (z ^ (z >> 31)) * 0x9E3779B97F4A7C15ull + 0xD1B54A32D192ED03ull;
  return u32x2{(unsigned)t, (unsigned)(t >> 32)};
}

template <int PH>
__global__ __launch_bounds__(512, 2) void fused_kernel(Params P) {
  extern __shared__ __attribute__((aligned(16))) char smem[];
  LAS char* L = (LAS char*)smem;
  Wv v;
  v.lane = threadIdx.x & 63;
  v.w = __builtin_amdgcn_readfirstlane(threadIdx.x >> 6);
  v.g = v.w & 3;
  v.c = v.w >> 2;
  const int xc = blockIdx.x & 7, idx = blockIdx.x >> 3;
  const rsrc_t ws = mk_rsrc(P.ws, WS_END), rwd = mk_rsrc(P.wd, 1792 * 512 * 2), rwo = mk_rsrc(P.wo, 512 * 512 * 2);
  const int r8 = v.lane >> 3, p8 = v.lane & 7;
  const unsigned wcol = (((p8 >> 1) ^ (r8 & 2)) * 32) + (p8 & 1) * 16;
  const unsigned voffC = r8 * 1024 + wcol;
  const int rt = xc * 4 + (idx & 3), ct = idx >> 2;
  const int hd = xc * 4 + (idx & 3), G = idx >> 2;
  u32x2 tag = {0u, 0u};
  AtPre ap;

  if (PH == 0 || PH == 1) {
    AB a;
    a.rb = (xc & 1) * 8 + (idx & 7);
    const int cq = xc >> 1, j = idx >> 3;
    const bool typeB = j == 3;
    a.strip0 = 6 * cq + j;
    a.dbl0 = 7 * cq + 2 * j;
    a.voffQ = r8 * 3072 + wcol;
    a.voffF = r8 * 3584 + wcol;
    a.voffC = voffC;
    a.rq = mk_rsrc(P.wqkv, 512 * 1536 * 2);
    a.rf = mk_rsrc(v.c ? P.wu : P.wg, 512 * 1792 * 2);
    a.ws = ws; a.rwd = rwd; a.rwo = rwo; a.ct = ct;
    const rsrc_t rx = mk_rsrc(P.x, 2048 * 512 * 2);
    STAMPR(30); STAMP(0);
    u32x2 epoch = {0u, 0u};
    if (PH == 0) asm volatile("buffer_load_dwordx2 %0, %1, %2, 0 offen sc1" : "=v"(epoch) : "v"(WS_EPOCH), "s"(ws) : "memory");
    issue_x(v, rx, a.rb, 0, 3);
    issue_q(v, a, a.strip0, 0, 2);
    issue_x(v, rx, a.rb, 1, 0);
    issue_x(v, rx, a.rb, 2, 1);
    f32x16 p0 = {}, p1;
    {
      Bases B = mk_bases(v, 32);
      bump(B, 2 * SLOT);
      float s1 = 0.f, s2 = 0.f, cs = 0.f;
      WAITBAR(8, 0);
      STAMP(1);
      p_step<0, 3>(v, a, L, B, p0, s1, s2, cs);
      WAITBAR(4, 0);
      issue_q(v, a, a.strip0, 1, 3);
      p_step<1, 0>(v, a, L, B, p0, s1, s2, cs);
      WAITBAR(0, 0);
      issue_x(v, rx, a.rb, 3, 2);
      if (typeB) issue_q(v, a, a.strip0 + 1, 0, 0); else issue_f(v, a, a.dbl0, 0, 0);
      p_step<2, 1>(v, a, L, B, p0, s1, s2, cs);
      WAITBAR(4, 0);
      if (typeB) issue_q(v, a, a.strip0 + 1, 1, 1); else issue_f(v, a, a.dbl0, 1, 1);
      p_step<3, 2>(v, a, L, B, p0, s1, s2, cs);
      cs_put(L, v, 0, cs);
      s1 = swap32_add(s1) * (1.f / 512.f);
      s2 = swap32_add(s2) * (1.f / 512.f);
      const float var = __builtin_fmaxf(__builtin_fmaf(-s1, s1, s2), 0.f);
      const float sd = rbf(__builtin_sqrtf(rbf(rbf(var) + 1e-5f)));
      const float c1 = -2.f * 0.7978845608028654f * 1.4426950408889634f;
      a.r = 1.f / sd;
      a.k1 = c1 * a.r;
      a.k2 = c1 * 0.044715f * a.r * a.r * a.r;
      a.ir2 = sd * sd;
      const unsigned nm = pk(-s1, -s1);
      a.mfrag = __builtin_bit_cast(bf16x8, u32x4{nm, nm & 0xffffu, 0u, 0u});
      SBAR(); WAITBAR(4, 0);
      if (typeB) issue_q(v, a, a.strip0 + 2, 0, 2); else issue_f(v, a, a.dbl0, 2, 2);
    }
    if (PH == 0) {
      PIN(epoch);
      tag = make_tag(epoch);
    }
    STAMP(2);
    if (typeB) body<K_QQ>(v, a, L, 0, false, 0, 0, tag, p0, p1);
    body<K_D0>(v, a, L, a.dbl0, typeB, typeB ? a.strip0 + 2 : a.strip0, typeB ? 3 : 1, tag, p0, p1);
    STAMP(3);
    if (!typeB) body<K_D1>(v, a, L, a.dbl0 + 1, true, 0, 0, tag, p0, p1);
    STAMP(4);
    if (PH == 0) {
      poll_flags(ws, at_flag_off(v, hd, G), tag);
      STAMP(5);
      at_issue_loads(v, ws, hd, G, ap);
    }
    const int dl = typeB ? a.dbl0 : a.dbl0 + 1;
    ln_fix(p0, cs_get(cs_slot(L, v, 0)), a.mfrag);
    ln_fix(p1, cs_get(cs_slot(L, v, 1)), a.mfrag);
    float hv[8];
#pragma unroll
    for (int e = 0; e < 8; ++e) hv[e] = geglu(p0[e], p0[8 + e], a.k1, a.k2, a.ir2);
    put_h(v, a, dl, 0, hv);
#pragma unroll
    for (int e = 0; e < 8; ++e) hv[e] = geglu(p1[e], p1[8 + e], a.k1, a.k2, a.ir2);
    put_h(v, a, dl, 1, hv);
    if (PH == 0) {
      asm volatile("s_waitcnt vmcnt(2)" : "+v"(ap.q), "+v"(ap.k[0]), "+v"(ap.k[1]), "+v"(ap.vv[0]), "+v"(ap.vv[1])::"memory");
      at_stage(v, L, ap);
      WAITBAR(0, 0);
      if (threadIdx.x == 0) put_flag(ws, WS_FH + (a.rb * 16 + cq * 4 + j) * FLAG_STRIDE, tag);
      issue_cgroup<0, 3>(v, rwd, rwo, voffC, ct, 3, 4);
    }
  }
  if (PH == 2) {
    at_issue_loads(v, ws, hd, G, ap);
    asm volatile("s_waitcnt vmcnt(0)" : "+v"(ap.q), "+v"(ap.k[0]), "+v"(ap.k[1]), "+v"(ap.vv[0]), "+v"(ap.vv[1])::"memory");
    at_stage(v, L, ap);
    WAITBAR(0, 0);
  }
  STAMP(6);
  if (PH == 0 || PH == 2) {
    if (G > 0) at_body<true>(v, L, ws, hd, G, ap.q);
    else at_body<false>(v, L, ws, hd, G, ap.q);
    STAMP(7);
    if (PH == 0) {
      WAITBAR(0, 0);
      STAMP(8);
      if (threadIdx.x == 0) put_flag(ws, WS_FO + (G * 32 + hd) * FLAG_STRIDE, tag);
    }
  }
  if (PH == 3) {
#pragma unroll
    for (int gp = 0; gp < 3; ++gp) issue_cgroup(v, rwd, rwo, voffC, ct, gp, gp);
    issue_cgroup<0, 3>(v, rwd, rwo, voffC, ct, 3, 4);
  }
  if (PH == 0 || PH == 3) phase_c(P, v, L, ws, rwd, rwo, voffC, rt, ct, tag, blockIdx.x == 0 && v.w == 0);
}
template <int PH> void launch(const Params& P, hipStream_t stream) {
  (void)hipFuncSetAttribute((const void*)fused_kernel<PH>, hipFuncAttributeMaxDynamicSharedMemorySize, LDS_BYTES);
  fused_kernel<PH><<<256, 512, LDS_BYTES, stream>>>(P);
}
}

extern "C" void kernel_launch(void* const* d_in, const int* in_sizes, int n_in, void* d_out, int out_size, void* d_ws,
                              size_t ws_size, hipStream_t stream) {
  Params P;
  P.x = d_in[0]; P.wqkv = d_in[1]; P.wo = d_in[2];
  P.wg = d_in[4]; P.wu = d_in[5]; P.wd = d_in[6];
  P.out = d_out; P.ws = d_ws;
  launch<0>(P, stream);
}
```

```cpp
#include <hip/hip_runtime.h>
#include <hip/hip_bf16.h>
#include <stdint.h>

typedef short bf16x8 __attribute__((ext_vector_type(8)));
typedef short s16x4 __attribute__((ext_vector_type(4)));
typedef float f32x16 __attribute__((ext_vector_type(16)));
typedef float f32x4 __attribute__((ext_vector_type(4)));
typedef float f32x2 __attribute__((ext_vector_type(2)));
typedef unsigned u32x4 __attribute__((ext_vector_type(4)));
typedef unsigned u32x2 __attribute__((ext_vector_type(2)));
typedef __bf16 bf2 __attribute__((ext_vector_type(2)));
typedef __amdgpu_buffer_rsrc_t rsrc_t;
#define LAS __attribute__((address_space(3)))
#define DI __device__ __forceinline__
#define SBAR() __builtin_amdgcn_sched_barrier(0)
#define PIN(x) asm volatile("" : "+v"(x))
#define RFL(x) __builtin_amdgcn_readfirstlane(x)
#define MFMA(a, b, c) __builtin_amdgcn_mfma_f32_32x32x16_bf16((a), (b), (c), 0, 0, 0)

#define STAMP(i)
#define STAMPR(i)
#define STAMP_FLUSH()
#define STAMP2(i)

namespace {
constexpr unsigned WS_EPOCH = 0;
constexpr unsigned WS_FQ = 1024;
constexpr unsigned WS_FH = WS_FQ + 16 * 24 * 64;
constexpr unsigned WS_FO = WS_FH + 16 * 16 * 64;
constexpr unsigned WS_ST = 65536;
constexpr unsigned WS_QKV = 131072;
constexpr unsigned SEC_BYTES = 32 * 64 * 1024;
constexpr unsigned WS_H = WS_QKV + 3 * SEC_BYTES;
constexpr unsigned WS_O = WS_H + 64 * 112 * 1024;
constexpr unsigned WS_END = WS_O + 64 * 32 * 1024;
static_assert(WS_FO + 256 * 64 <= WS_ST, "flags overlap");
constexpr int SLOT = 32800, SUB1 = 16416;
constexpr int AUX = 4 * SLOT;
constexpr int W1A = AUX + 8192;
constexpr int REG = 32768;
constexpr int LDS_BYTES = 163840;
constexpr int KV = 3 * REG;
constexpr int ONES = LDS_BYTES - 1024;
constexpr float QSCALE = 0.25f * 1.4426950408889634f;
constexpr unsigned SPIN_CAP = 1u << 18;

struct Params {
  const void *x, *wqkv, *wo, *wg, *wu, *wd;
  void *out, *ws;
};

template <int I> struct IC { static constexpr int v = I; };
template <int I, int N, class F> DI void static_for(F&& f) {
  if constexpr (I < N) {
    f(IC<I>{});
    static_for<I + 1, N>(f);
  }
}
template <int VM> DI void wait_vm() { asm volatile("s_waitcnt vmcnt(%0)" ::"n"(VM) : "memory"); }
template <int VM, int LG> DI void wait_bar() { asm volatile("s_waitcnt vmcnt(%0) lgkmcnt(%1)\n\ts_barrier" ::"n"(VM), "n"(LG) : "memory"); }
DI unsigned uni(unsigned x) {
  x = __builtin_amdgcn_readfirstlane(x);
  asm volatile("" : "+s"(x));
  return x;
}
DI rsrc_t mk_rsrc(const void* p, unsigned bytes) { return __builtin_amdgcn_make_buffer_rsrc((void*)p, 0, bytes, 0x00020000); }
DI void glds(rsrc_t rs, unsigned voff, unsigned soff, unsigned lds) {
  asm volatile("s_mov_b32 m0, %2\n\ts_nop 0\n\tbuffer_load_dwordx4 %0, %1, %3 offen lds" ::"v"(voff), "s"(rs),
               "s"(RFL(lds)), "s"(RFL(soff)) : "memory");
}
DI u32x4 ld128h(rsrc_t rs, unsigned voff, unsigned soff) {
  u32x4 r;
  asm volatile("buffer_load_dwordx4 %0, %1, %2, %3 offen sc1" : "=v"(r) : "v"(voff), "s"(rs), "s"(RFL(soff)) : "memory");
  return r;
}
DI u32x2 ld64h(rsrc_t rs, unsigned voff) {
  u32x2 r;
  asm volatile("buffer_load_dwordx2 %0, %1, %2, 0 offen sc1" : "=v"(r) : "v"(voff), "s"(rs) : "memory");
  return r;
}
DI void st128h(rsrc_t rs, unsigned voff, unsigned soff, u32x4 d) {
  asm volatile("buffer_store_dwordx4 %0, %1, %2, %3 offen sc1\n\ts_nop 1" ::"v"(d), "v"(voff), "s"(rs), "s"(RFL(soff)) : "memory");
}
DI void put_flag(rsrc_t ws, int lane, unsigned soff, u32x2 tag) {
  asm volatile("buffer_store_dwordx2 %0, %1, %2, %3 offen sc1" ::"v"(tag), "v"(lane ? 0x80000000u : 0u), "s"(ws), "s"(RFL(soff)) : "memory");
}
DI void check_flags(rsrc_t ws, unsigned voff, u32x2 tag, u32x2 v) {
  unsigned cnt;
  asm volatile(
      "s_mov_b32 %1, %5\n"
      "1:\n\t"
      "v_cmp_ne_u64 vcc, %2, %0\n\t"
      "s_cbranch_vccz 2f\n\t"
      "s_sleep 1\n\t"
      "buffer_load_dwordx2 %0, %3, %4, 0 offen sc1\n\t"
      "s_waitcnt vmcnt(0)\n\t"
      "s_sub_u32 %1, %1, 1\n\t"
      "s_cbranch_scc0 1b\n"
      "2:"
      : "+v"(v), "=&s"(cnt)
      : "s"(tag), "v"(voff), "s"(ws), "n"(SPIN_CAP)
      : "vcc", "scc", "memory");
}
DI void check_granule(rsrc_t ws, unsigned voff, unsigned tag, u32x2& v) {
  unsigned cnt, lo = v.x, hi = v.y;
  asm volatile(
      "s_mov_b32 %2, %6\n"
      "1:\n\t"
      "v_cmp_ne_u32 vcc, %3, %1\n\t"
      "s_cbranch_vccz 2f\n\t"
      "s_sleep 1\n\t"
      "buffer_load_dword %1, %4, %5, 0 offen offset:4 sc1\n\t"
      "s_waitcnt vmcnt(0)\n\t"
      "buffer_load_dword %0, %4, %5, 0 offen sc1\n\t"
      "s_waitcnt vmcnt(0)\n\t"
      "s_sub_u32 %2, %2, 1\n\t"
      "s_cbranch_scc0 1b\n"
      "2:"
      : "+v"(lo), "+v"(hi), "=&s"(cnt)
      : "s"(tag), "v"(voff), "s"(ws), "n"(SPIN_CAP)
      : "vcc", "scc", "memory");
  v.x = lo;
}
template <int CTRL> DI float dpp_add(float v) {
  return v + __builtin_bit_cast(float, __builtin_amdgcn_update_dpp(0, __builtin_bit_cast(int, v), CTRL, 0xf, 0xf, true));
}
DI float wave_sum63(float v) {
  v = dpp_add<0x111>(v); v = dpp_add<0x112>(v); v = dpp_add<0x114>(v); v = dpp_add<0x118>(v);
  v = dpp_add<0x142>(v);
  return dpp_add<0x143>(v);
}
DI s16x4 trd(LAS const char* p) { return __builtin_amdgcn_ds_read_tr16_b64_v4i16((LAS s16x4*)p); }
template <int STEP> DI bf16x8 rdW(LAS const char* p) {
  const s16x4 a = trd(p), b = trd(p + STEP);
  return __builtin_shufflevector(a, b, 0, 1, 2, 3, 4, 5, 6, 7);
}
DI unsigned pk(float lo, float hi) {
  const f32x2 v = {lo, hi};
  return __builtin_bit_cast(unsigned, __builtin_convertvector(v, bf2));
}
DI float rbf(float v) { return __builtin_bit_cast(float, pk(0.f, v) & 0xffff0000u); }
#define COLSUM(w, cs) cs = __builtin_amdgcn_mfma_f32_16x16x32_bf16(a.sel, (w), cs, 0, 0, 0)
DI float swap32_max(float m) {
  auto r = __builtin_amdgcn_permlane32_swap(__float_as_uint(m), __float_as_uint(m), false, false);
  return __builtin_fmaxf(__uint_as_float(r[0]), __uint_as_float(r[1]));
}
DI float swap32_add(float m) {
  auto r = __builtin_amdgcn_permlane32_swap(__float_as_uint(m), __float_as_uint(m), false, false);
  return __uint_as_float(r[0]) + __uint_as_float(r[1]);
}
DI f32x16 splat(float v) {
  f32x16 r;
#pragma unroll
  for (int i = 0; i < 16; ++i) r[i] = v;
  return r;
}
DI float cs_get(LAS const char* csp) {
  LAS const float* p = (LAS const float*)csp;
  return (p[0] + p[64]) + (p[128] + p[192]);
}
DI void ln_fix(f32x16& acc, float c, unsigned nm) {
  const unsigned u0 = __float_as_uint(c);
  const float r1 = c - __uint_as_float(u0 & 0xffff0000u);
  const unsigned u1 = __float_as_uint(r1);
  const float r2 = r1 - __uint_as_float(u1 & 0xffff0000u);
  const u32x2 a = {(u0 >> 16) | (u1 & 0xffff0000u), __float_as_uint(r2) >> 16};
  acc = __builtin_amdgcn_mfma_f32_32x32x8bf16_1k(__builtin_bit_cast(s16x4, a), __builtin_bit_cast(s16x4, u32x2{nm, nm}), acc, 0, 0, 0);
}
DI float geglu(float a, float b, float k1, float k2, float ir2) {
  const float e = __builtin_amdgcn_exp2f(a * __builtin_fmaf(a * a, k2, k1));
  return a * b * __builtin_amdgcn_rcpf(__builtin_fmaf(e, ir2, ir2));
}

DI unsigned w_voff(int lane, int pitch) {
  PIN(lane);
  const int r8 = lane >> 3, p8 = lane & 7;
  return r8 * pitch + (((p8 >> 1) ^ (r8 & 2)) * 32) + (p8 & 1) * 16;
}
struct Cx {
  int lane, w, g, c;
  int rt, ct, rot;
  int hd, G;
  unsigned voffC;
  rsrc_t ws, rwd, rwo;
  u32x2 tag;
  LAS char* L;
};
DI int c_grp(const Cx& v, int gp) {
  if (gp >= 7) return (gp - 7) ^ (v.rot & 1);
  const int r = gp + v.rot;
  return r >= 7 ? r - 7 : r;
}
DI unsigned c_soff(const Cx& v, int gp, int i) {
  return ((gp < 7 ? 448 : 128) * v.g + 64 * c_grp(v, gp) + 32 * v.c + 8 * i) * 1024 + v.ct * 128;
}
DI unsigned c_dst(const Cx& v, int region, int i) { return region * REG + v.g * 8192 + v.c * 4096 + i * 1024; }
DI void c_piece(const Cx& v, int gp, int region, int i) { glds(gp < 7 ? v.rwd : v.rwo, v.voffC, c_soff(v, gp, i), c_dst(v, region, i)); }
DI u32x4 c_stash(const Cx& v, int gp, int i) {
  u32x4 r;
  asm volatile("buffer_load_dwordx4 %0, %1, %2, %3 offen" : "=v"(r) : "v"(v.voffC), "s"(v.rwd), "s"(RFL(c_soff(v, gp, i))) : "memory");
  return r;
}

struct AtPre { u32x4 q, k0, k1, v0, v1; };
DI int at_kb(const Cx& v) {
  const int kb = 8 * v.G - 8 + 2 * v.w;
  return kb < 0 ? 0 : kb;
}
DI u32x4 at_load(const Cx& v, int i) {
  const int blk = i == 0 ? 8 * v.G + v.w : at_kb(v) + ((i - 1) & 1);
  return ld128h(v.ws, v.lane * 16, WS_QKV + ((i + 1) >> 1) * SEC_BYTES + (v.hd * 64 + blk) * 1024);
}
DI unsigned at_flag_off(const Cx& v) {
  const int i = v.lane & 7, isq = i >> 2;
  const int blk = at_kb(v) + (i & 1) + isq * (8 * v.G + v.w - at_kb(v) - (i & 1));
  const int sec = (1 - isq) * (1 + ((i >> 1) & 1));
  return WS_FQ + ((blk >> 2) * 24 + sec * 8 + (v.hd >> 2)) * 64 + (((v.hd >> 1) & 1) * 4 + (blk & 3)) * 8;
}
DI void at_stage(const Cx& v, const AtPre& a) {
  LAS char* kd = v.L + KV + 2 * v.w * 1024 + v.lane * 16;
  LAS char* vd = v.L + KV + 16384 + 2 * v.w * 1024 + (v.lane & 31) * 32 + (v.lane >> 5) * 16;
  *(LAS u32x4*)kd = a.k0;
  *(LAS u32x4*)(kd + 1024) = a.k1;
  *(LAS u32x4*)vd = a.v0;
  *(LAS u32x4*)(vd + 1024) = a.v1;
  if (v.w == 0) *(LAS u32x4*)(v.L + ONES + v.lane * 16) = u32x4{0x3f803f80u, 0x3f803f80u, 0x3f803f80u, 0x3f803f80u};
}
DI float max16(const f32x16& s, float m) {
#pragma unroll
  for (int r = 0; r < 16; r += 2) m = __builtin_fmaxf(__builtin_fmaxf(m, s[r]), s[r + 1]);
  return m;
}
DI void exp_pack(const f32x16& s, u32x4& p0, u32x4& p1) {
#pragma unroll
  for (int i = 0; i < 4; ++i) {
    p0[i] = pk(__builtin_amdgcn_exp2f(s[2 * i]), __builtin_amdgcn_exp2f(s[2 * i + 1]));
    p1[i] = pk(__builtin_amdgcn_exp2f(s[8 + 2 * i]), __builtin_amdgcn_exp2f(s[9 + 2 * i]));
  }
}
DI u32x4 h_load(const Cx& v, int i) {
  return ld128h(v.ws, v.lane * 16, WS_H + ((2 * v.rt + v.c) * 112 + 28 * v.g + 4 * c_grp(v, i >> 2) + (i & 3)) * 1024);
}
#define PIN4(a, o) asm volatile("" : "+v"(a[o]), "+v"(a[o + 1]), "+v"(a[o + 2]), "+v"(a[o + 3]))
template <bool FULL> DI void at_body(const Cx& v, const u32x4& qv, unsigned hflag, u32x4 (&hf)[28], u32x4 (&sw)[12]) {
  const int lane = v.lane, t = lane & 31, h = lane >> 5;
  const bf16x8 q = __builtin_bit_cast(bf16x8, qv);
  LAS const char* kp = v.L + KV + v.w * 1024 + lane * 16;
  const int qq = (lane & 15) >> 2, p = lane & 3;
  const bool real = (lane & 16) == 0;
  LAS const char* vp = v.L + (real ? KV + 16384 + v.w * 1024 + (4 * h + qq) * 32 + (p & 1) * 16 + (p >> 1) * 8 : ONES + (lane & 15) * 8);
  const int vstr = real ? 1024 : 0;
  const float NEG = -1e30f;
  const f32x16 zero = {};
  const unsigned hpoll = WS_FH + ((v.rt >> 1) * 16 + 4 * v.g + (lane & 3)) * 64 + (((lane >> 2) & 1) * 4 + ((2 * v.rt + v.c) & 3)) * 8;
  const int td = t - 4 * h;
#define KFRAG(kb) (*(LAS const bf16x8*)(kp + (kb) * 1024))
#define VFRAG(kb, s) rdW<256>(vp + (kb) * vstr + (s) * 512)
#define BF(x) __builtin_bit_cast(bf16x8, x)
  auto merge = [&](const f32x16& sd, const f32x16& sf) __attribute__((always_inline)) {
    f32x16 r;
#pragma unroll
    for (int i = 0; i < 16; ++i) r[i] = (i & 3) + 8 * (i >> 2) <= td ? sd[i] : sf[i];
    return r;
  };
  u32x2 fl;
  float m = NEG;
  f32x16 o = zero, cm, sc;
  u32x4 p0, p1;
  if (FULL) {
    bf16x8 ka = KFRAG(8), kb_ = KFRAG(0), kc = KFRAG(1), kn = KFRAG(2);
    f32x16 sa = MFMA(ka, q, zero), sb = MFMA(kb_, q, zero);
    sc = MFMA(kc, q, zero);
    PIN(sa); PIN(sb); PIN(sc);
    wait_vm<3>();
    put_flag(v.ws, lane, hflag, v.tag);
    SBAR();
    sw[4] = c_stash(v, 5, 0);
    m = max16(merge(sa, sb), m);
    static_for<1, 8>([&](auto K_) __attribute__((always_inline)) {
      constexpr int kb = decltype(K_)::v;
      f32x16 sn;
      if constexpr (kb < 7) { sn = MFMA(kn, q, zero); PIN(sn); }
      kn = KFRAG(kb < 6 ? kb + 2 : kb - 5);
      if constexpr (kb == 6) kc = kn;
      SBAR();
      sw[4 + kb] = c_stash(v, 5 + (kb >> 2), kb & 3);
      m = max16(sc, m);
      PIN(m);
      SBAR();
      if constexpr (kb < 7) sc = sn;
    });
    m = swap32_max(m);
    STAMP2(8);
    fl = ld64h(v.ws, hpoll);
    cm = splat(-m);
    sc = MFMA(kc, q, cm);
    static_for<1, 8>([&](auto K_) __attribute__((always_inline)) {
      constexpr int kb = decltype(K_)::v;
      const bf16x8 v0 = VFRAG(kb, 0), v1 = VFRAG(kb, 1);
      f32x16 sn;
      if constexpr (kb < 7) { sn = MFMA(kn, q, cm); PIN(sn); }
      else { sa = MFMA(kn, q, cm); sb = MFMA(ka, q, cm); PIN(sa); PIN(sb); }
      if constexpr (kb < 7) kn = KFRAG(kb + 2);
      if constexpr (kb == 6) ka = KFRAG(0);
      SBAR();
      exp_pack(sc, p0, p1);
      o = MFMA(v0, BF(p0), o);
      o = MFMA(v1, BF(p1), o);
      PIN(o);
      if (kb == 2) {
        STAMP2(10);
        asm volatile("s_waitcnt vmcnt(0)" : "+v"(fl)::"memory");
        PIN4(sw, 4); PIN4(sw, 8);
        check_flags(v.ws, hpoll, v.tag, fl);
        STAMP2(11);
      }
      if constexpr (kb >= 2 && kb < 7) {
#pragma unroll
        for (int i = 4 * (kb - 2); i < 4 * (kb - 1); ++i) hf[i] = h_load(v, i);
      }
      SBAR();
      if constexpr (kb < 7) sc = sn;
    });
    sc = merge(sa, sb);
  } else {
    wait_vm<3>();
    put_flag(v.ws, lane, hflag, v.tag);
#pragma unroll
    for (int i = 0; i < 8; ++i) sw[4 + i] = c_stash(v, 5 + (i >> 2), i & 3);
    m = max16(merge(MFMA(KFRAG(8), q, zero), splat(NEG)), m);
    for (int kb = 8 - v.w; kb < 8; ++kb) m = max16(MFMA(KFRAG(kb), q, zero), m);
    m = swap32_max(m);
    cm = splat(-m);
    for (int kb = 8 - v.w; kb < 8; ++kb) {
      exp_pack(MFMA(KFRAG(kb), q, cm), p0, p1);
      o = MFMA(VFRAG(kb, 0), BF(p0), o);
      o = MFMA(VFRAG(kb, 1), BF(p1), o);
    }
    sc = merge(MFMA(KFRAG(8), q, cm), splat(NEG));
    PIN(o); PIN(sc);
    check_flags(v.ws, hpoll, v.tag, u32x2{~v.tag.x, 0u});
    PIN4(sw, 4); PIN4(sw, 8);
#pragma unroll
    for (int i = 0; i < 20; ++i) hf[i] = h_load(v, i);
  }
  exp_pack(sc, p0, p1);
  u32x4 k0, k1;
#pragma unroll
  for (int i = 0; i < 4; ++i) {
    const int r0 = 2 * (i & 1) + 8 * (i >> 1);
    k0[i] = (r0 <= td ? 0xffffu : 0u) | (r0 + 1 <= td ? 0xffff0000u : 0u);
    k1[i] = (r0 + 16 <= td ? 0xffffu : 0u) | (r0 + 17 <= td ? 0xffff0000u : 0u);
  }
  o = MFMA(VFRAG(8, 0), BF(p0 & k0), o);
  o = MFMA(VFRAG(8, 1), BF(p1 & k1), o);
  o = MFMA(VFRAG(0, 0), BF(p0 & ~k0), o);
  o = MFMA(VFRAG(0, 1), BF(p1 & ~k1), o);
  const float il = __builtin_amdgcn_rcpf(o[8]);
  u32x4 ov;
#pragma unroll
  for (int i = 0; i < 4; ++i) ov[i] = pk(o[2 * i] * il, o[2 * i + 1] * il);
  st128h(v.ws, lane * 16, WS_O + ((8 * v.G + v.w) * 32 + v.hd) * 1024, ov);
}

DI void c_scatter(const Cx& v, LAS char* X, const f32x16& a0, const f32x16& a1, f32x4& own0, f32x4& own1) {
#pragma unroll
  for (int d = 0; d < 4; ++d) {
    const f32x16& a = (d >> 1) ? a1 : a0;
    const int o = 8 * (d & 1);
    const f32x4 p0 = {a[o], a[o + 1], a[o + 2], a[o + 3]}, p1 = {a[o + 4], a[o + 5], a[o + 6], a[o + 7]};
    if (d == v.g) {
      own0 = p0;
      own1 = p1;
    } else {
      LAS char* dst = X + (d * 3 + ((v.g - d - 1) & 3)) * 2048 + v.lane * 16;
      *(LAS f32x4*)dst = p0;
      *(LAS f32x4*)(dst + 1024) = p1;
    }
  }
}
DI void c_gather(const Cx& v, LAS const char* X, f32x4& own0, f32x4& own1) {
  LAS const char* src = X + v.g * 3 * 2048 + v.lane * 16;
#pragma unroll
  for (int i = 0; i < 3; ++i) {
    own0 += *(LAS const f32x4*)(src + i * 2048);
    own1 += *(LAS const f32x4*)(src + i * 2048 + 1024);
  }
}
DI float fin(unsigned xbits, float s, float d) {
  const float sg = d * __builtin_amdgcn_rcpf(1.f + __builtin_amdgcn_exp2f(-1.4426950408889634f * d));
  return __builtin_fmaf(s, sg, __uint_as_float(xbits));
}
DI void phase_c(const Params& P, const Cx& v, u32x4 (&hf)[28], u32x4 (&sw)[12], bool closer) {
  const int lane = v.lane, h = lane >> 5, tb = 2 * v.rt + v.c;
  const int qq = (lane & 15) >> 2, p = lane & 3, g1 = (lane >> 4) & 1;
  LAS const char* b0 = v.L + v.g * 8192 + (4 * h + qq) * 128 + (g1 + (qq & 2)) * 32 + 8 * p;
  LAS const char* b1 = v.L + v.g * 8192 + (4 * h + qq) * 128 + (g1 + (2 ^ (qq & 2))) * 32 + 8 * p;
  const unsigned opoll = WS_FO + ((v.rt >> 2) * 32 + 8 * v.g + (lane & 7)) * 64 + (tb & 7) * 8;
  const unsigned obase = uni(WS_O + (tb * 32 + 8 * v.g) * 1024);
  const rsrc_t rx = mk_rsrc(P.x, 2048 * 512 * 2);
  const unsigned xoff = (32 * tb + (lane & 31)) * 1024 + (64 * v.ct + 16 * v.g + 4 * h) * 2;
  LAS char* xd = v.L + (v.c ? 0 : 3 * REG);
  LAS char* xs = v.L + REG + v.c * REG;
  f32x16 d0 = {}, d1 = {}, s0 = {}, s1 = {};
  f32x4 dw0, dw1, sw0, sw1;
  bf16x8 wf[3][2];
  u32x4 of[8];
  u32x2 fl, x0, x1;
  constexpr int RG[9] = {0, 1, 2, 4, 3, 0, 1, 2, 4};
  auto rd = [&](auto S_) __attribute__((always_inline)) {
    constexpr int s = decltype(S_)::v;
    wf[s % 3][0] = rdW<1024>(b0 + RG[s >> 2] * REG + (s & 3) * 2048);
    wf[s % 3][1] = rdW<1024>(b1 + RG[s >> 2] * REG + (s & 3) * 2048);
  };
  static_for<0, 36>([&](auto S_) __attribute__((always_inline)) {
    constexpr int s = decltype(S_)::v, gp = s >> 2;
    if (s == 0) { wait_bar<17, 0>(); PIN4(hf, 0); rd(IC<0>{}); rd(IC<1>{}); }
    if (s == 4) { wait_vm<18>(); PIN4(hf, 4); }
    if (s == 8) {
      wait_bar<8, 8>();
      PIN4(hf, 8); PIN4(hf, 12); PIN4(hf, 16);
      put_flag(v.ws, lane, WS_FO + (v.G * 32 + v.hd) * 64 + v.w * 8, v.tag);
    }
    if (s == 16) wait_bar<63, 8>();
    if (s == 20) {
      asm volatile("s_waitcnt vmcnt(8)" : "+v"(fl)::"memory");
      PIN4(hf, 20); PIN4(hf, 24);
      check_flags(v.ws, opoll, v.tag, fl);
      if (closer) {
#pragma unroll
        for (int i = 0; i < 4; ++i) check_flags(v.ws, WS_FO + (i * 64 + lane) * 64, v.tag, u32x2{~v.tag.x, 0u});
        put_flag(v.ws, lane, WS_EPOCH, v.tag);
      }
    }
    if (s == 28) { wait_bar<6, 0>(); PIN4(of, 0); rd(IC<28>{}); rd(IC<29>{}); }
    if (s == 32) { wait_bar<2, 8>(); PIN4(of, 4); }
    if ((s & 3) == 0) STAMP2(40 + gp);
    if constexpr (s + 2 < 36 && s != 26 && s != 27) rd(IC<s + 2>{});
    const bf16x8 f = BF(gp < 7 ? hf[gp < 7 ? s : 0] : of[gp < 7 ? 0 : s - 28]);
    f32x16& a0 = gp < 7 ? d0 : s0;
    f32x16& a1 = gp < 7 ? d1 : s1;
    a0 = MFMA(wf[s % 3][0], f, a0);
    PIN(a0);
    SBAR();
    if (s == 0) c_piece(v, 3, 4, 3);
    if constexpr (s < 8) hf[20 + s] = h_load(v, 20 + s);
    if constexpr (s < 4) *(LAS u32x4*)(v.L + c_dst(v, 3, s) + lane * 16) = sw[s];
    if constexpr (s >= 8 && s < 14) {
      constexpr int j = s - 8;
      *(LAS u32x4*)(v.L + c_dst(v, j >> 2, j & 3) + lane * 16) = sw[4 + j];
      if constexpr (j < 2) *(LAS u32x4*)(v.L + c_dst(v, 1, 2 + j) + lane * 16) = sw[10 + j];
    }
    if (s == 14) fl = ld64h(v.ws, opoll);
    if (s >= 16 && s < 20) c_piece(v, 7, 2, s - 16);
    if constexpr (s >= 20 && s < 24) of[2 * (s - 20)] = ld128h(v.ws, lane * 16, obase + (4 * c_grp(v, 7 + ((s - 20) >> 1)) + 2 * (s & 1)) * 1024);
    SBAR();
    a1 = MFMA(wf[s % 3][1], f, a1);
    PIN(a1);
    SBAR();
    if (s >= 16 && s < 20) c_piece(v, 8, 4, s - 16);
    if constexpr (s >= 20 && s < 24) of[2 * (s - 20) + 1] = ld128h(v.ws, lane * 16, obase + (4 * c_grp(v, 7 + ((s - 20) >> 1)) + 2 * (s & 1) + 1) * 1024);
    if (s == 24) {
      asm volatile("buffer_load_dwordx2 %0, %1, %2, 0 offen" : "=v"(x0) : "v"(xoff), "s"(rx) : "memory");
      asm volatile("buffer_load_dwordx2 %0, %1, %2, 0 offen offset:16" : "=v"(x1) : "v"(xoff), "s"(rx) : "memory");
    }
    if (s == 29) c_scatter(v, xd, d0, d1, dw0, dw1);
    SBAR();
  });
  STAMP(14);
  c_scatter(v, xs, s0, s1, sw0, sw1);
  asm volatile("s_waitcnt vmcnt(0) lgkmcnt(0)\n\ts_barrier" : "+v"(x0), "+v"(x1)::"memory");
  c_gather(v, xd, dw0, dw1);
  c_gather(v, xs, sw0, sw1);
  u32x2 y0, y1;
  y0.x = pk(fin(x0.x << 16, sw0[0], dw0[0]), fin(x0.x & 0xffff0000u, sw0[1], dw0[1]));
  y0.y = pk(fin(x0.y << 16, sw0[2], dw0[2]), fin(x0.y & 0xffff0000u, sw0[3], dw0[3]));
  y1.x = pk(fin(x1.x << 16, sw1[0], dw1[0]), fin(x1.x & 0xffff0000u, sw1[1], dw1[1]));
  y1.y = pk(fin(x1.y << 16, sw1[2], dw1[2]), fin(x1.y & 0xffff0000u, sw1[3], dw1[3]));
  const rsrc_t ro = mk_rsrc(P.out, 2048 * 512 * 2);
  __builtin_amdgcn_raw_buffer_store_b64(y0, ro, xoff, 0, 0);
  __builtin_amdgcn_raw_buffer_store_b64(y1, ro, xoff + 16, 0, 0);
  STAMP(15); STAMPR(31); STAMP_FLUSH();
}

struct AB {
  bf16x8 X[32];
  bf16x8 wf[3][2];
  bf16x8 sel;
  unsigned nm;
  float r, k1, k2, ir2;
  int gr;
  int rb, cg, strip0, dblA, dblB;
  int kx;
  unsigned voffX, voffQ, voffF;
  rsrc_t rx, rq, rf;
};
DI void x_piece(const Cx& v, const AB& a, int kc, int slot, int i) {
  glds(a.rx, a.voffX ^ (64 * i), (a.rb * 128 + 16 * v.w + 4 * i) * 1024 + (kc ^ a.kx) * 256, slot * SLOT + (4 * v.w + i) * 1024);
}
DI void q_piece(const Cx& v, const AB& a, int strip, int half, int slot, int i) {
  glds(a.rq, a.voffQ, (128 * ((2 * half + v.c) ^ a.kx) + 32 * v.g + 8 * i) * 3072 + strip * 128, slot * SLOT + v.c * SUB1 + v.g * 4096 + i * 1024);
}
DI void q4_piece(const Cx& v, const AB& a, int kq, int dst, int i) {
  glds(a.rq, a.voffQ, (128 * (kq ^ a.kx) + 16 * v.w + 8 * i) * 3072 + a.strip0 * 128, dst + (2 * v.w + i) * 1024);
}
DI void f_piece(const Cx& v, const AB& a, int dbl, int kq, int slot, int i) {
  glds(a.rf, a.voffF, (128 * (kq ^ a.kx) + 32 * v.g + 8 * i) * 3584 + dbl * 128, slot * SLOT + v.c * SUB1 + v.g * 4096 + i * 1024);
}
template <bool TB> DI void w_piece(const Cx& v, const AB& a, int j, int i) {
  if (TB && j < 6) q_piece(v, a, a.strip0 + (j >> 1), j & 1, (j - 2) & 3, i);
  else f_piece(v, a, j < 6 ? a.dblA : a.dblB, (j - 2) & 3, (j - 2) & 3, i);
}
DI LAS char* cs_slot(const Cx& v, int j) { return v.L + AUX + (j * 2 + v.c) * 1024 + v.lane * 4; }
DI void cs_put(const Cx& v, int j, const f32x4& c) { *(LAS float*)(cs_slot(v, j) + v.g * 256) = (v.lane & 16) ? c[1] : c[0]; }
DI void q_store(const Cx& v, const AB& a, const f32x16& acc, int strip, int hh) {
  const float sc = strip < 8 ? a.r * QSCALE : a.r;
  u32x4 o;
#pragma unroll
  for (int i = 0; i < 4; ++i) o[i] = pk(acc[8 * hh + 2 * i] * sc, acc[8 * hh + 2 * i + 1] * sc);
  st128h(v.ws, v.lane * 16, WS_QKV + ((strip * 4 + 2 * v.c + hh) * 64 + 4 * a.rb + v.g) * 1024, o);
}
DI void q_flag(const Cx& v, const AB& a, int strip) { put_flag(v.ws, v.lane, WS_FQ + (a.rb * 24 + strip) * 64 + v.w * 8, v.tag); }
DI void put_h(const Cx& v, const AB& a, int dbl, int j, const u32x4& hv) {
  st128h(v.ws, v.lane * 16, WS_H + ((4 * a.rb + v.g) * 112 + 4 * dbl + 2 * v.c + j) * 1024, hv);
}
struct Bases { unsigned b[4]; };
DI Bases mk_bases(const Cx& v, const AB& a, int g1mul) {
  int l = v.lane;
  PIN(l);
  const int qq = (l & 15) >> 2;
  const int o = ((l >> 4) & 1) * g1mul + (8 * (l >> 5) + qq) * 128 + ((2 * v.c) ^ (qq & 2)) * 32 + 8 * (l & 3);
  Bases r;
#pragma unroll
  for (int i = 0; i < 4; ++i) { r.b[i] = o + (i ^ a.gr) * 4096; PIN(r.b[i]); }
  return r;
}
DI void bump(Bases& B, int d) {
#pragma unroll
  for (int i = 0; i < 4; ++i) { B.b[i] += d; PIN(B.b[i]); }
}
#define WRD(B, off, i) rdW<512>((LAS const char*)(size_t)((B).b[(i) >> 1]) + (off) + ((i) & 1) * 2048)

DI u32x2 make_tag(u32x2 e) {
  const unsigned long long z = ((unsigned long long)__builtin_amdgcn_readfirstlane(e.y) << 32 | (unsigned)__builtin_amdgcn_readfirstlane(e.x));
  const unsigned long long t = (z ^ (z >> 31)) * 0x9E3779B97F4A7C15ull + 0xD1B54A32D192ED03ull;
  return u32x2{(unsigned)t, (unsigned)(t >> 32)};
}

DI void stats_put(const Cx& v, const AB& a, const u32x4& xr) {
  float s1 = 0.f, s2 = 0.f;
#pragma unroll
  for (int d = 0; d < 4; ++d) {
    const float lo = __uint_as_float(xr[d] << 16), hi = __uint_as_float(xr[d] & 0xffff0000u);
    s1 += lo + hi;
    s2 = __builtin_fmaf(lo, lo, __builtin_fmaf(hi, hi, s2));
  }
  const float mean = wave_sum63(s1) * (1.f / 512.f);
  const float var = __builtin_fmaxf(__builtin_fmaf(-mean, mean, wave_sum63(s2) * (1.f / 512.f)), 0.f);
  const float sd = __builtin_amdgcn_sqrtf(rbf(rbf(var) + 1e-5f));
  put_flag(v.ws, v.lane ^ 63, WS_ST + (a.rb * 128 + a.cg * 8 + v.w) * 8, u32x2{pk(-mean, sd), v.tag.x});
}
template <bool TB, int KQ, int XS, int WO>
DI void p_step(const Cx& v, AB& a, const Bases& B, f32x16& acc, f32x4& cs) {
  const int t = v.lane & 31;
  const unsigned u = (((v.lane >> 5) ^ (t & 15)) * 16) ^ (64 * a.gr);
  LAS const char* xr = v.L + XS * SLOT + (32 * v.g + t) * 256;
  bf16x8 wf[8];
#pragma unroll
  for (int i = 0; i < 3; ++i) {
    a.X[8 * KQ + i] = *(LAS const bf16x8*)(xr + (u ^ (32 * i)));
    wf[i] = WRD(B, WO, i);
  }
#pragma unroll
  for (int i = 0; i < 8; ++i) {
    if (i < 5) {
      a.X[8 * KQ + i + 3] = *(LAS const bf16x8*)(xr + (u ^ (32 * (i + 3))));
      wf[i + 3] = WRD(B, WO, i + 3);
    }
    if (KQ == 0) {
      if (i < 2) { x_piece(v, a, 1, 0, 2 * i); x_piece(v, a, 1, 0, 2 * i + 1); }
      else if (i == 2) { q4_piece(v, a, 1, 2 * SLOT + SUB1, 0); q4_piece(v, a, 1, 2 * SLOT + SUB1, 1); }
      else if (i < 7) x_piece(v, a, 2, 1, i - 3);
      else { q4_piece(v, a, 2, W1A, 0); q4_piece(v, a, 2, W1A, 1); }
    }
    if (KQ == 1 && i < 4) x_piece(v, a, 3, 3, i);
    if (KQ == 2) { if (i < 2) q4_piece(v, a, 3, 2 * SLOT, i); else if (i < 6) w_piece<TB>(v, a, 2, i - 2); }
    if (KQ == 3 && i < 4) w_piece<TB>(v, a, 3, i);
    acc = MFMA(wf[i], a.X[8 * KQ + i], acc);
    if (i < 2) COLSUM(wf[i], cs);
    PIN(acc); PIN(cs);
    SBAR();
  }
}
template <bool TB> DI void prologue(Cx& v, AB& a, f32x16& acc, u32x2& epoch, u32x4& xr, u32x2& st) {
  Bases B = mk_bases(v, a, 32);
  bump(B, 2 * SLOT);
  f32x4 cs = {};
  STAMP2(23);
  wait_bar<0, 0>();
  STAMP(1);
  PIN(epoch); PIN(xr);
  v.tag = make_tag(epoch);
  p_step<TB, 0, 3, 0>(v, a, B, acc, cs);
  stats_put(v, a, xr);
  STAMP2(16); wait_bar<7, 0>(); STAMP2(17);
  p_step<TB, 1, 0, SUB1>(v, a, B, acc, cs);
  STAMP2(18); wait_bar<4, 0>(); STAMP2(19);
  bump(B, W1A - 2 * SLOT);
  p_step<TB, 2, 1, 0>(v, a, B, acc, cs);
  STAMP2(20); wait_bar<4, 0>(); STAMP2(21);
  bump(B, 2 * SLOT - W1A);
  p_step<TB, 3, 3, 0>(v, a, B, acc, cs);
  STAMP2(22);
  cs_put(v, 0, cs);
  st = ld64h(v.ws, WS_ST + (a.rb * 128 + 32 * v.g + (v.lane & 31)) * 8);
  wait_bar<5, 0>();
}
DI void row_stats(const Cx& v, AB& a, unsigned g) {
  const float sd = __uint_as_float(g & 0xffff0000u);
  const float c1 = -2.f * 0.7978845608028654f * 1.4426950408889634f;
  a.r = __builtin_amdgcn_rcpf(sd);
  a.k1 = c1 * a.r;
  a.k2 = c1 * 0.044715f * a.r * a.r * a.r;
  a.ir2 = sd * sd;
  a.nm = v.lane < 32 ? (g & 0xffffu) | (g << 16) : 0u;
}
enum { K_QQ = 0, K_D0 = 1, K_D1L = 2, K_D0L = 3 };
template <bool Q> DI void rd_step(AB& a, const Bases& B, int s, int ph) {
  const int kq = (s >> 3) & 3, i = s & 7, so = (kq & 1) * SLOT;
  if (Q) {
    a.wf[ph][0] = WRD(B, so + (i >> 2) * SUB1, (2 * i) & 7);
    a.wf[ph][1] = WRD(B, so + (i >> 2) * SUB1, ((2 * i) & 7) + 1);
  } else {
    a.wf[ph][0] = WRD(B, so, i);
    a.wf[ph][1] = WRD(B, so + 32, i);
  }
}
template <int KIND> DI void body(Cx& v, AB& a, f32x16& p0, f32x16& p1, AtPre& ap, u32x2& st) {
  constexpr bool Q = KIND == K_QQ, FIRST = KIND == K_QQ || KIND == K_D0, TB = KIND == K_QQ || KIND == K_D0L;
  constexpr int PH0 = KIND == K_D1L ? 2 : 0;
  constexpr int SB = FIRST ? 32 : 36;
  Bases B = mk_bases(v, a, Q ? 32 : SUB1);
  f32x16 a0 = {}, a1 = {};
  f32x4 c0 = {}, c1 = {};
  float cp0, cp1, hlo;
  u32x4 hv;
  u32x2 fl;
  unsigned fo;
  if (KIND != K_D1L) {
    rd_step<Q>(a, B, 0, 0);
    rd_step<Q>(a, B, 1, 1);
  }
  static_for<0, 32>([&](auto S_) __attribute__((always_inline)) {
    constexpr int s = decltype(S_)::v, kq = s >> 3, i = s & 7;
    if (s == 8 && !FIRST) v.voffC = w_voff(v.lane, 1024);
    if (s == 14) bump(B, 2 * SLOT);
    if (s == 30 && KIND == K_D0) bump(B, -2 * SLOT);
    const bool late = FIRST && (s == 6 || s == 7);
    if (!late && (s < 30 || KIND == K_D0)) rd_step<Q>(a, B, s + 2, (s + 2 + PH0) % 3);
    if (s == 0 || (Q && s == 16)) cp0 = cs_get(cs_slot(v, 0));
    if (s == 1 && KIND == K_D1L) cp1 = cs_get(cs_slot(v, 1));
    const bf16x8 w0 = a.wf[(s + PH0) % 3][0], w1 = a.wf[(s + PH0) % 3][1];
    f32x16& acA = Q ? (kq < 2 ? a0 : a1) : a0;
    f32x16& acB = Q ? (kq < 2 ? a0 : a1) : a1;
    f32x4& csA = Q ? (kq < 2 ? c0 : c1) : c0;
    f32x4& csB = Q ? (kq < 2 ? c0 : c1) : c1;
    constexpr bool CS = Q ? (i & 3) == 0 : i < 2;
    constexpr int e = KIND != K_D1L ? -1 : (s == 30 ? 14 : ((s & 1) && s >= 3 ? (s == 31 ? 15 : (s - 3) >> 1) : -1));
    float ga, ex;
    acA = MFMA(w0, a.X[Q ? 16 * (kq & 1) + 2 * i : s], acA);
    PIN(acA);
    SBAR();
    if (CS) { COLSUM(w0, csA); PIN(csA); }
    if (FIRST && kq == 0) w_piece<TB>(v, a, 4 + (i >> 2), i & 3);
    else if ((i & 1) == 0) {
      if (FIRST) w_piece<TB>(v, a, 5 + kq, i >> 1);
      else if (kq == 0) w_piece<TB>(v, a, 9, i >> 1);
      else c_piece(v, kq - 1, kq - 1, i >> 1);
    }
    if constexpr (e >= 0) {
      ga = (e < 8 ? p0 : p1)[e & 7];
      ex = __builtin_amdgcn_exp2f(ga * __builtin_fmaf(ga * ga, a.k2, a.k1));
      ga *= (e < 8 ? p0 : p1)[8 + (e & 7)];
      PIN(ga); PIN(ex);
    }
    if (FIRST && s == 5) {
      asm volatile("s_waitcnt vmcnt(6)" : "+v"(st)::"memory");
      check_granule(v.ws, WS_ST + (a.rb * 128 + 32 * v.g + (v.lane & 31)) * 8, v.tag.x, st);
      row_stats(v, a, st.x);
      PIN(a.nm); PIN(a.r); PIN(a.k1); PIN(a.k2); PIN(a.ir2);
    }
    SBAR();
    acB = MFMA(w1, a.X[Q ? 16 * (kq & 1) + 2 * i + 1 : s], acB);
    PIN(acB);
    SBAR();
    if (CS) { COLSUM(w1, csB); PIN(csB); }
    if constexpr (e >= 0) {
      const float hx = ga * __builtin_amdgcn_rcpf(__builtin_fmaf(ex, a.ir2, a.ir2));
      if (e & 1) { hv[(e & 7) >> 1] = pk(hlo, hx); PIN(hv[(e & 7) >> 1]); }
      else { hlo = hx; PIN(hlo); }
      if (e == 15) put_h(v, a, a.dblA, 1, hv);
    }
    if (KIND == K_D1L) {
      if (s == 1) { ln_fix(p0, cp0, a.nm); PIN(p0); }
      if (s == 2) { ln_fix(p1, cp1, a.nm); PIN(p1); }
      if (s == 18) put_h(v, a, a.dblA, 0, hv);
    } else {
      constexpr int o = FIRST ? 4 : 0;
      const int st0 = KIND == K_D0L ? a.strip0 + 2 : a.strip0;
      if (s == 2 + o) { ln_fix(p0, cp0, a.nm); PIN(p0); }
      if (s == 5 + o || s == 7 + o) q_store(v, a, p0, st0, (s - 5 - o) >> 1);
      if (s == 13 + o) { wait_vm<3>(); q_flag(v, a, st0); }
      if (Q) {
        if (s == 13) cs_put(v, 0, c0);
        if (s == 18) { ln_fix(a0, cp0, a.nm); PIN(a0); }
        if (s == 21 || s == 23) q_store(v, a, a0, a.strip0 + 1, (s - 21) >> 1);
        if (s == 29) { cs_put(v, 0, c1); wait_vm<3>(); q_flag(v, a, a.strip0 + 1); }
      }
    }
    if (!Q && s == 28) {
      cs_put(v, 0, c0);
      cs_put(v, 1, c1);
    }
    if (!FIRST) {
      if (s == 19) { fo = at_flag_off(v); fl = ld64h(v.ws, fo); }
      if (s == 24) { check_flags(v.ws, fo, v.tag, fl); ap.k0 = at_load(v, 1); }
      if (s == 25) ap.k1 = at_load(v, 2);
      if (s == 26) ap.v0 = at_load(v, 3);
      if (s == 28) ap.v1 = at_load(v, 4);
      if (s == 30) ap.q = at_load(v, 0);
    }
    SBAR();
    if (i == 7) {
      STAMP2(SB + kq);
      if (!FIRST && kq == 2) { wait_bar<2, 8>(); PIN(fl); }
      else if (!FIRST && kq == 3) wait_bar<63, 0>();
      else if (kq == 3 && KIND != K_D0) wait_bar<4, 0>();
      else wait_bar<4, 8>();
      STAMP2(SB + 18 + kq);
      if (FIRST && s == 7) {
        rd_step<Q>(a, B, 8, 8 % 3);
        rd_step<Q>(a, B, 9, 9 % 3);
      }
    }
  });
  if (Q) p0 = a1;
  else { p0 = a0; p1 = a1; }
}
DI u32x4 geglu8(const AB& a, const f32x16& p) {
  u32x4 r;
#pragma unroll
  for (int i = 0; i < 4; ++i)
    r[i] = pk(geglu(p[2 * i], p[8 + 2 * i], a.k1, a.k2, a.ir2), geglu(p[2 * i + 1], p[9 + 2 * i], a.k1, a.k2, a.ir2));
  return r;
}
DI void ab_tail(const Cx& v, const AB& a, int dbl, f32x16& p0, f32x16& p1, AtPre& ap, u32x4 (&sw)[12]) {
  const float c0 = cs_get(cs_slot(v, 0)), c1 = cs_get(cs_slot(v, 1));
#pragma unroll
  for (int i = 0; i < 4; ++i) sw[i] = c_stash(v, 4, i);
  ln_fix(p0, c0, a.nm);
  ln_fix(p1, c1, a.nm);
  put_h(v, a, dbl, 0, geglu8(a, p0));
  asm volatile("s_waitcnt vmcnt(5)" : "+v"(ap.q), "+v"(ap.k0), "+v"(ap.k1), "+v"(ap.v0), "+v"(ap.v1)::"memory");
  at_stage(v, ap);
  put_h(v, a, dbl, 1, geglu8(a, p1));
  wait_bar<2, 0>();
  PIN4(sw, 0);
}

template <bool TB> DI void ab_path(Cx& v, AB a, AtPre& ap, u32x4 (&sw)[12]) {
  int l = v.lane;
  PIN(l);
  a.voffX = (l >> 4) * 1024 + (((l & 15) ^ (l >> 4)) * 16);
  a.voffQ = w_voff(l, 3072);
  a.voffF = w_voff(l, 3584);
  const unsigned sl = ((l ^ (l >> 4)) & 1) ? 0u : 0x3f803f80u;
  a.sel = __builtin_bit_cast(bf16x8, u32x4{sl, sl, sl, sl});
  STAMP2(24);
  u32x2 epoch, st;
  u32x4 xr;
  epoch = ld64h(v.ws, WS_EPOCH);
  asm volatile("buffer_load_dwordx4 %0, %1, %2, %3 offen" : "=v"(xr) : "v"(l * 16), "s"(a.rx), "s"((a.rb * 128 + a.cg * 8 + v.w) * 1024) : "memory");
#pragma unroll
  for (int i = 0; i < 4; ++i) x_piece(v, a, 0, 3, i);
#pragma unroll
  for (int i = 0; i < 2; ++i) q4_piece(v, a, 0, 2 * SLOT, i);
  f32x16 p0 = {}, p1;
  prologue<TB>(v, a, p0, epoch, xr, st);
  STAMP(2);
  body<TB ? K_QQ : K_D0>(v, a, p0, p1, ap, st);
  STAMP(3);
  body<TB ? K_D0L : K_D1L>(v, a, p0, p1, ap, st);
  STAMP(4);
  ab_tail(v, a, a.dblB, p0, p1, ap, sw);
}

__global__ __launch_bounds__(512, 2) void fused_kernel(Params P) {
  extern __shared__ __attribute__((aligned(16))) char smem[];
  Cx v;
  v.L = (LAS char*)smem;
  v.lane = threadIdx.x & 63;
  v.w = __builtin_amdgcn_readfirstlane(threadIdx.x >> 6);
  v.g = v.w & 3;
  v.c = v.w >> 2;
  const int xc = blockIdx.x & 7, idx = blockIdx.x >> 3;
  v.ws = mk_rsrc(P.ws, WS_END);
  v.rwd = mk_rsrc(P.wd, 1792 * 512 * 2);
  v.rwo = mk_rsrc(P.wo, 512 * 512 * 2);
  v.rt = v.hd = xc * 4 + (idx & 3);
  v.ct = v.G = idx >> 2;
  v.rot = (v.rt * 2 + v.ct) % 7;
  AtPre ap;
  u32x4 sw[12];
  unsigned hflag;
  STAMPR(30); STAMP(0);
  {
    AB a;
    a.rb = (xc & 1) * 8 + (idx & 7);
    const int cq = xc >> 1, j = idx >> 3;
    const bool typeB = j == 3;
    a.cg = cq * 4 + j;
    a.gr = v.g ^ (2 * v.c);

    a.strip0 = 6 * cq + j;
    const int swp = typeB ? 0 : (a.rb >> 2) & 1;
    a.dblA = 7 * cq + 2 * j + swp;
    a.dblB = typeB ? a.dblA : 7 * cq + 2 * j + 1 - swp;
    a.kx = (a.rb + j) & 3;
    a.rx = mk_rsrc(P.x, 2048 * 512 * 2);
    a.rq = mk_rsrc(P.wqkv, 512 * 1536 * 2);
    a.rf = mk_rsrc(v.c ? P.wu : P.wg, 512 * 1792 * 2);
    hflag = WS_FH + (a.rb * 16 + a.cg) * 64 + v.w * 8;
    if (typeB) ab_path<true>(v, a, ap, sw);
    else ab_path<false>(v, a, ap, sw);
  }
  STAMP(6);
#pragma unroll
  for (int i = 0; i < 3; ++i) c_piece(v, 3, 4, i);
  u32x4 hf[28];
  if (v.G) at_body<true>(v, ap.q, hflag, hf, sw);
  else at_body<false>(v, ap.q, hflag, hf, sw);
  STAMP(7);
  phase_c(P, v, hf, sw, blockIdx.x == 0 && v.w == 0);
}
}

extern "C" void kernel_launch(void* const* d_in, const int* in_sizes, int n_in, void* d_out, int out_size, void* d_ws,
                              size_t ws_size, hipStream_t stream) {
  Params P;
  P.x = d_in[0]; P.wqkv = d_in[1]; P.wo = d_in[2];
  P.wg = d_in[4]; P.wu = d_in[5]; P.wd = d_in[6];
  P.out = d_out; P.ws = d_ws;
  (void)hipFuncSetAttribute((const void*)fused_kernel, hipFuncAttributeMaxDynamicSharedMemorySize, LDS_BYTES);
  fused_kernel<<<256, 512, LDS_BYTES, stream>>>(P);
}
```

```cpp
#define WARM 4
#include <hip/hip_runtime.h>
#include <hip/hip_bf16.h>
#include <stdint.h>

typedef short bf16x8 __attribute__((ext_vector_type(8)));
typedef short s16x4 __attribute__((ext_vector_type(4)));
typedef float f32x16 __attribute__((ext_vector_type(16)));
typedef float f32x4 __attribute__((ext_vector_type(4)));
typedef float f32x2 __attribute__((ext_vector_type(2)));
typedef unsigned u32x4 __attribute__((ext_vector_type(4)));
typedef unsigned u32x2 __attribute__((ext_vector_type(2)));
typedef __bf16 bf2 __attribute__((ext_vector_type(2)));
typedef __amdgpu_buffer_rsrc_t rsrc_t;
#define LAS __attribute__((address_space(3)))
#define DI __device__ __forceinline__
#define SBAR() __builtin_amdgcn_sched_barrier(0)
#define PIN(x) asm volatile("" : "+v"(x))
#define RFL(x) __builtin_amdgcn_readfirstlane(x)
#define MFMA(a, b, c) __builtin_amdgcn_mfma_f32_32x32x16_bf16((a), (b), (c), 0, 0, 0)

#define STAMP(i)
#define STAMPR(i)
#define STAMP_FLUSH()
#define STAMP2(i)

#define OCHK 19
namespace {
constexpr unsigned WS_EPOCH = 0;
constexpr unsigned WS_FQ = 1024;
constexpr unsigned WS_FH = WS_FQ + 16 * 24 * 64;
constexpr unsigned WS_FO = WS_FH + 16 * 16 * 64;
constexpr unsigned WS_ST = 65536;
constexpr unsigned WS_QKV = 131072;
constexpr unsigned SEC_BYTES = 32 * 64 * 1024;
constexpr unsigned WS_H = WS_QKV + 3 * SEC_BYTES;
constexpr unsigned WS_O = WS_H + 64 * 112 * 1024;
constexpr unsigned WS_END = WS_O + 64 * 32 * 1024;
static_assert(WS_FO + 256 * 64 <= WS_ST, "flags overlap");
constexpr int SLOT = 32800, SUB1 = 16416;
constexpr int AUX = 4 * SLOT;
constexpr int W1A = AUX + 8192;
constexpr int REG = 32768;
constexpr int LDS_BYTES = 163840;
constexpr int KV = 3 * REG;
constexpr int ONES = LDS_BYTES - 1024;
constexpr float QSCALE = 0.25f * 1.4426950408889634f;
constexpr unsigned SPIN_CAP = 1u << 18;

struct Params {
  const void *x, *wqkv, *wo, *wg, *wu, *wd;
  void *out, *ws;
};

template <int I> struct IC { static constexpr int v = I; };
template <int I, int N, class F> DI void static_for(F&& f) {
  if constexpr (I < N) {
    f(IC<I>{});
    static_for<I + 1, N>(f);
  }
}
template <int VM> DI void wait_vm() { asm volatile("s_waitcnt vmcnt(%0)" ::"n"(VM) : "memory"); }
template <int VM, int LG> DI void wait_bar() { asm volatile("s_waitcnt vmcnt(%0) lgkmcnt(%1)\n\ts_barrier" ::"n"(VM), "n"(LG) : "memory"); }
DI unsigned uni(unsigned x) {
  x = __builtin_amdgcn_readfirstlane(x);
  asm volatile("" : "+s"(x));
  return x;
}
DI rsrc_t mk_rsrc(const void* p, unsigned bytes) { return __builtin_amdgcn_make_buffer_rsrc((void*)p, 0, bytes, 0x00020000); }
DI void glds(rsrc_t rs, unsigned voff, unsigned soff, unsigned lds) {
  asm volatile("s_mov_b32 m0, %2\n\ts_nop 0\n\tbuffer_load_dwordx4 %0, %1, %3 offen lds" ::"v"(voff), "s"(rs),
               "s"(RFL(lds)), "s"(RFL(soff)) : "memory");
}
template <int IMM = 0> DI u32x4 ld128h(rsrc_t rs, unsigned voff, unsigned soff) {
  u32x4 r;
  asm volatile("buffer_load_dwordx4 %0, %1, %2, %3 offen offset:%4 sc1" : "=v"(r) : "v"(voff), "s"(rs), "s"(RFL(soff)), "n"(IMM) : "memory");
  return r;
}
DI u32x2 ld64h(rsrc_t rs, unsigned voff) {
  u32x2 r;
  asm volatile("buffer_load_dwordx2 %0, %1, %2, 0 offen sc1" : "=v"(r) : "v"(voff), "s"(rs) : "memory");
  return r;
}
DI void st128h(rsrc_t rs, unsigned voff, unsigned soff, u32x4 d) {
  asm volatile("buffer_store_dwordx4 %0, %1, %2, %3 offen sc1\n\ts_nop 1" ::"v"(d), "v"(voff), "s"(rs), "s"(RFL(soff)) : "memory");
}
DI void put_flag(rsrc_t ws, int lane, unsigned soff, u32x2 tag) {
  asm volatile("buffer_store_dwordx2 %0, %1, %2, %3 offen sc1" ::"v"(tag), "v"(lane ? 0x80000000u : 0u), "s"(ws), "s"(RFL(soff)) : "memory");
}
DI void check_flags(rsrc_t ws, unsigned voff, u32x2 tag, u32x2 v) {
  unsigned cnt;
  asm volatile(
      "s_mov_b32 %1, %5\n"
      "1:\n\t"
      "v_cmp_ne_u64 vcc, %2, %0\n\t"
      "s_cbranch_vccz 2f\n\t"
      "s_sleep 1\n\t"
      "buffer_load_dwordx2 %0, %3, %4, 0 offen sc1\n\t"
      "s_waitcnt vmcnt(0)\n\t"
      "s_sub_u32 %1, %1, 1\n\t"
      "s_cbranch_scc0 1b\n"
      "2:"
      : "+v"(v), "=&s"(cnt)
      : "s"(tag), "v"(voff), "s"(ws), "n"(SPIN_CAP)
      : "vcc", "scc", "memory");
}
DI void check_granule(rsrc_t ws, unsigned voff, unsigned tag, u32x2& v) {
  unsigned cnt, lo = v.x, hi = v.y;
  asm volatile(
      "s_mov_b32 %2, %6\n"
      "1:\n\t"
      "v_cmp_ne_u32 vcc, %3, %1\n\t"
      "s_cbranch_vccz 2f\n\t"
      "s_sleep 1\n\t"
      "buffer_load_dword %1, %4, %5, 0 offen offset:4 sc1\n\t"
      "s_waitcnt vmcnt(0)\n\t"
      "buffer_load_dword %0, %4, %5, 0 offen sc1\n\t"
      "s_waitcnt vmcnt(0)\n\t"
      "s_sub_u32 %2, %2, 1\n\t"
      "s_cbranch_scc0 1b\n"
      "2:"
      : "+v"(lo), "+v"(hi), "=&s"(cnt)
      : "s"(tag), "v"(voff), "s"(ws), "n"(SPIN_CAP)
      : "vcc", "scc", "memory");
  v.x = lo;
}
template <int CTRL> DI float dpp_add(float v) {
  return v + __builtin_bit_cast(float, __builtin_amdgcn_update_dpp(0, __builtin_bit_cast(int, v), CTRL, 0xf, 0xf, true));
}
DI float wave_sum63(float v) {
  v = dpp_add<0x111>(v); v = dpp_add<0x112>(v); v = dpp_add<0x114>(v); v = dpp_add<0x118>(v);
  v = dpp_add<0x142>(v);
  return dpp_add<0x143>(v);
}
DI s16x4 trd(LAS const char* p) { return __builtin_amdgcn_ds_read_tr16_b64_v4i16((LAS s16x4*)p); }
template <int STEP> DI bf16x8 rdW(LAS const char* p) {
  const s16x4 a = trd(p), b = trd(p + STEP);
  return __builtin_shufflevector(a, b, 0, 1, 2, 3, 4, 5, 6, 7);
}
DI unsigned pk(float lo, float hi) {
  const f32x2 v = {lo, hi};
  return __builtin_bit_cast(unsigned, __builtin_convertvector(v, bf2));
}
DI float rbf(float v) { return __builtin_bit_cast(float, pk(0.f, v) & 0xffff0000u); }
#define COLSUM(w, cs) cs = __builtin_amdgcn_mfma_f32_16x16x32_bf16(a.sel, (w), cs, 0, 0, 0)
DI float swap32_max(float m) {
  auto r = __builtin_amdgcn_permlane32_swap(__float_as_uint(m), __float_as_uint(m), false, false);
  return __builtin_fmaxf(__uint_as_float(r[0]), __uint_as_float(r[1]));
}
DI float swap32_add(float m) {
  auto r = __builtin_amdgcn_permlane32_swap(__float_as_uint(m), __float_as_uint(m), false, false);
  return __uint_as_float(r[0]) + __uint_as_float(r[1]);
}
DI f32x16 splat(float v) {
  f32x16 r;
#pragma unroll
  for (int i = 0; i < 16; ++i) r[i] = v;
  return r;
}
DI float cs_get(LAS const char* csp) {
  LAS const float* p = (LAS const float*)csp;
  return (p[0] + p[64]) + (p[128] + p[192]);
}
DI void ln_fix(f32x16& acc, float c, unsigned nm) {
  const unsigned u0 = __float_as_uint(c);
  const float r1 = c - __uint_as_float(u0 & 0xffff0000u);
  const unsigned u1 = __float_as_uint(r1);
  const float r2 = r1 - __uint_as_float(u1 & 0xffff0000u);
  const u32x2 a = {(u0 >> 16) | (u1 & 0xffff0000u), __float_as_uint(r2) >> 16};
  acc = __builtin_amdgcn_mfma_f32_32x32x8bf16_1k(__builtin_bit_cast(s16x4, a), __builtin_bit_cast(s16x4, u32x2{nm, nm}), acc, 0, 0, 0);
}
DI float geglu(float a, float b, float k1, float k2, float ir2) {
  const float e = __builtin_amdgcn_exp2f(a * __builtin_fmaf(a * a, k2, k1));
  return a * b * __builtin_amdgcn_rcpf(__builtin_fmaf(e, ir2, ir2));
}

DI unsigned w_voff(int lane, int pitch) {
  PIN(lane);
  const int r8 = lane >> 3, p8 = lane & 7;
  return r8 * pitch + (((p8 >> 1) ^ (r8 & 2)) * 32) + (p8 & 1) * 16;
}
struct Cx {
  int lane, w, g, c;
  int rt, ct, rot;
  int hd, G;
  unsigned voffC;
  unsigned cbd, cbo, hb;
  rsrc_t ws, rwd, rwo;
  u32x2 tag;
  LAS char* L;
};
DI void c_setup(Cx& v) {
  v.voffC = w_voff(v.lane, 1024);
  v.cbd = uni((448 * v.g + 32 * v.c) * 1024 + v.ct * 128);
  v.cbo = uni((128 * v.g + 32 * v.c) * 1024 + v.ct * 128);
  v.hb = uni(WS_H + ((2 * v.rt + v.c) * 112 + 28 * v.g) * 1024);
  v.rot = uni((v.rt * 2 + v.ct) % 7);
}
DI int c_grp(const Cx& v, int gp) {
  if (gp >= 7) return (gp - 7) ^ (v.rot & 1);
  const int r = gp + v.rot;
  return r >= 7 ? r - 7 : r;
}
DI unsigned c_voff(const Cx& v, int gp, int i) { return v.voffC + (c_grp(v, gp) << 16) + i * 8192; }
DI unsigned c_dst(const Cx& v, int region, int i) { return region * REG + v.g * 8192 + v.c * 4096 + i * 1024; }
DI void c_piece(const Cx& v, int gp, int region, int i) {
  glds(gp < 7 ? v.rwd : v.rwo, c_voff(v, gp, i), gp < 7 ? v.cbd : v.cbo, c_dst(v, region, i));
}
DI u32x4 c_stash(const Cx& v, int n) {
  const int gp = 4 + (n >> 2);
  u32x4 r;
  asm volatile("buffer_load_dwordx4 %0, %1, %2, %3 offen"
               : "=v"(r) : "v"(c_voff(v, gp, n & 3)), "s"(gp < 7 ? v.rwd : v.rwo), "s"(gp < 7 ? v.cbd : v.cbo) : "memory");
  return r;
}

struct AtPre { u32x4 q, k0, k1, v0, v1; };
DI int at_kb(const Cx& v) {
  const int kb = 8 * v.G - 8 + 2 * v.w;
  return kb < 0 ? 0 : kb;
}
DI u32x4 at_load(const Cx& v, int i) {
  const int blk = i == 0 ? 8 * v.G + v.w : at_kb(v) + ((i - 1) & 1);
  return ld128h(v.ws, v.lane * 16, WS_QKV + ((i + 1) >> 1) * SEC_BYTES + (v.hd * 64 + blk) * 1024);
}
DI unsigned at_flag_off(const Cx& v) {
  const int i = v.lane & 7, isq = i >> 2;
  const int blk = at_kb(v) + (i & 1) + isq * (8 * v.G + v.w - at_kb(v) - (i & 1));
  const int sec = (1 - isq) * (1 + ((i >> 1) & 1));
  return WS_FQ + ((blk >> 2) * 24 + sec * 8 + (v.hd >> 2)) * 64 + (((v.hd >> 1) & 1) * 4 + (blk & 3)) * 8;
}
DI void at_stage(const Cx& v, const AtPre& a) {
  LAS char* kd = v.L + KV + 2 * v.w * 1024 + v.lane * 16;
  LAS char* vd = v.L + KV + 16384 + 2 * v.w * 1024 + (v.lane & 31) * 32 + (v.lane >> 5) * 16;
  *(LAS u32x4*)kd = a.k0;
  *(LAS u32x4*)(kd + 1024) = a.k1;
  *(LAS u32x4*)vd = a.v0;
  *(LAS u32x4*)(vd + 1024) = a.v1;
  if (v.w == 0) *(LAS u32x4*)(v.L + ONES + v.lane * 16) = u32x4{0x3f803f80u, 0x3f803f80u, 0x3f803f80u, 0x3f803f80u};
}
DI void max16(const f32x16& s, float& m, float& m2) {
#pragma unroll
  for (int r = 0; r < 16; r += 4) {
    m = __builtin_fmaxf(__builtin_fmaxf(m, s[r]), s[r + 1]);
    m2 = __builtin_fmaxf(__builtin_fmaxf(m2, s[r + 2]), s[r + 3]);
  }
}
DI void exp_pack(const f32x16& s, u32x4& p0, u32x4& p1) {
#pragma unroll
  for (int i = 0; i < 4; ++i) {
    p0[i] = pk(__builtin_amdgcn_exp2f(s[2 * i]), __builtin_amdgcn_exp2f(s[2 * i + 1]));
    p1[i] = pk(__builtin_amdgcn_exp2f(s[8 + 2 * i]), __builtin_amdgcn_exp2f(s[9 + 2 * i]));
  }
}
template <int I> DI u32x4 h_load(const Cx& v) { return ld128h<(I & 3) * 1024>(v.ws, v.lane * 16 + (c_grp(v, I >> 2) << 12), v.hb); }
#define PIN4(a, o) asm volatile("" : "+v"(a[o]), "+v"(a[o + 1]), "+v"(a[o + 2]), "+v"(a[o + 3]))
template <bool FULL> DI void at_body(const Cx& v, const u32x4& qv, u32x4 (&hf)[28], u32x4 (&sw)[20]) {
  const int lane = v.lane, t = lane & 31, h = lane >> 5;
  const bf16x8 q = __builtin_bit_cast(bf16x8, qv);
  LAS const char* kp = v.L + KV + v.w * 1024 + lane * 16;
  const int qq = (lane & 15) >> 2, p = lane & 3;
  const bool real = (lane & 16) == 0;
  int vreal = KV + 16384 + v.w * 1024 + (4 * h + qq) * 32 + (p & 1) * 16 + (p >> 1) * 8, vone = ONES + (lane & 15) * 8;
  PIN(vreal); PIN(vone);
  LAS const char* vp = v.L + (real ? vreal : vone);
  const int vstr = real ? 1024 : 0;
  const float NEG = -1e30f;
  const f32x16 zero = {};
  const unsigned hpoll = WS_FH + ((v.rt >> 1) * 16 + 4 * v.g + (lane & 3)) * 64 + (((lane >> 2) & 1) * 4 + ((2 * v.rt + v.c) & 3)) * 8;
  const int td = t - 4 * h;
#define KFRAG(kb) (*(LAS const bf16x8*)(kp + (kb) * 1024))
#define VFRAG(kb, s) rdW<256>(vp + (kb) * vstr + (s) * 512)
#define BF(x) __builtin_bit_cast(bf16x8, x)
  auto merge = [&](const f32x16& sd, const f32x16& sf) __attribute__((always_inline)) {
    f32x16 r;
#pragma unroll
    for (int i = 0; i < 16; ++i) r[i] = (i & 3) + 8 * (i >> 2) <= td ? sd[i] : sf[i];
    return r;
  };
  u32x2 fl;
  float m = NEG;
  f32x16 o = zero, cm, sc;
  u32x4 p0, p1;
  if (FULL) {
    bf16x8 kf[9];
#pragma unroll
    for (int kb = 0; kb < 9; ++kb) kf[(kb + 8) % 9] = KFRAG((kb + 8) % 9);
    f32x16 sa = MFMA(kf[8], q, zero), sb = MFMA(kf[0], q, zero);
    sc = MFMA(kf[1], q, zero);
    PIN(sa); PIN(sb); PIN(sc);
    SBAR();
    float m2 = NEG;
    max16(merge(sa, sb), m, m2);
    static_for<1, 8>([&](auto K_) __attribute__((always_inline)) {
      constexpr int kb = decltype(K_)::v;
      f32x16 sn;
      if constexpr (kb < 7) { sn = MFMA(kf[kb + 1], q, zero); PIN(sn); }
      SBAR();
      if constexpr ((kb & 1) == 0) sw[13 + (kb >> 1)] = c_stash(v, 13 + (kb >> 1));
      max16(sc, m, m2);
      PIN(m); PIN(m2);
      SBAR();
      if constexpr (kb < 7) sc = sn;
    });
    m = swap32_max(__builtin_fmaxf(m, m2));
    bf16x8 ka, kc = kf[1], kn = kf[2];
    STAMP2(8);
    fl = ld64h(v.ws, hpoll);
    cm = splat(-m);
    sc = MFMA(kc, q, cm);
    static_for<1, 8>([&](auto K_) __attribute__((always_inline)) {
      constexpr int kb = decltype(K_)::v;
      const bf16x8 v0 = VFRAG(kb, 0), v1 = VFRAG(kb, 1);
      f32x16 sn;
      if constexpr (kb < 7) { sn = MFMA(kn, q, cm); PIN(sn); }
      else { sa = MFMA(kn, q, cm); sb = MFMA(ka, q, cm); PIN(sa); PIN(sb); }
      if constexpr (kb < 7) kn = KFRAG(kb + 2);
      if constexpr (kb == 6) ka = KFRAG(0);
      SBAR();
      if constexpr (kb == 1) { sw[17] = c_stash(v, 17); sw[18] = c_stash(v, 18); }
      if constexpr (kb == 2) sw[19] = c_stash(v, 19);
      exp_pack(sc, p0, p1);
      o = MFMA(v0, BF(p0), o);
      o = MFMA(v1, BF(p1), o);
      PIN(o);
      if (kb == 2) {
        STAMP2(10);
        asm volatile("s_waitcnt vmcnt(3)" : "+v"(fl)::"memory");
        check_flags(v.ws, hpoll, v.tag, fl);
        STAMP2(11);
      }
      if constexpr (kb >= 2) {
        hf[2 * kb - 4] = h_load<2 * kb - 4>(v);
        hf[2 * kb - 3] = h_load<2 * kb - 3>(v);
      }
      SBAR();
      if constexpr (kb < 7) sc = sn;
    });
    sc = merge(sa, sb);
  } else {
#pragma unroll
    for (int i = 14; i < 20; ++i) sw[i] = c_stash(v, i);
    float m2 = NEG;
    max16(merge(MFMA(KFRAG(8), q, zero), splat(NEG)), m, m2);
    for (int kb = 8 - v.w; kb < 8; ++kb) max16(MFMA(KFRAG(kb), q, zero), m, m2);
    m = swap32_max(__builtin_fmaxf(m, m2));
    cm = splat(-m);
    for (int kb = 8 - v.w; kb < 8; ++kb) {
      exp_pack(MFMA(KFRAG(kb), q, cm), p0, p1);
      o = MFMA(VFRAG(kb, 0), BF(p0), o);
      o = MFMA(VFRAG(kb, 1), BF(p1), o);
    }
    sc = merge(MFMA(KFRAG(8), q, cm), splat(NEG));
    PIN(o); PIN(sc);
    check_flags(v.ws, hpoll, v.tag, u32x2{~v.tag.x, 0u});
    static_for<0, 12>([&](auto I_) __attribute__((always_inline)) { hf[decltype(I_)::v] = h_load<decltype(I_)::v>(v); });
  }
  hf[12] = h_load<12>(v);
  hf[13] = h_load<13>(v);
  exp_pack(sc, p0, p1);
  u32x4 k0, k1;
#pragma unroll
  for (int i = 0; i < 4; ++i) {
    const int r0 = 2 * (i & 1) + 8 * (i >> 1);
    k0[i] = (r0 <= td ? 0xffffu : 0u) | (r0 + 1 <= td ? 0xffff0000u : 0u);
    k1[i] = (r0 + 16 <= td ? 0xffffu : 0u) | (r0 + 17 <= td ? 0xffff0000u : 0u);
  }
  o = MFMA(VFRAG(8, 0), BF(p0 & k0), o);
  o = MFMA(VFRAG(8, 1), BF(p1 & k1), o);
  o = MFMA(VFRAG(0, 0), BF(p0 & ~k0), o);
  o = MFMA(VFRAG(0, 1), BF(p1 & ~k1), o);
  const float il = __builtin_amdgcn_rcpf(o[8]);
  u32x4 ov;
#pragma unroll
  for (int i = 0; i < 4; ++i) ov[i] = pk(o[2 * i] * il, o[2 * i + 1] * il);
  st128h(v.ws, lane * 16, WS_O + ((8 * v.G + v.w) * 32 + v.hd) * 1024, ov);
}

DI void c_scatter(const Cx& v, LAS char* X, const f32x16& a0, const f32x16& a1) {
  const int t = v.lane & 31;
  LAS char* row = X + v.g * 8192 + t * 256;
  const int sw = ((v.lane >> 5) ^ (t & 15)) * 16;
#pragma unroll
  for (int j = 0; j < 8; ++j) {
    const f32x16& a = j < 4 ? a0 : a1;
    const int o = 4 * (j & 3);
    *(LAS f32x4*)(row + (sw ^ (j * 32))) = f32x4{a[o], a[o + 1], a[o + 2], a[o + 3]};
  }
}
DI f32x4 c_gather(const Cx& v, LAS const char* X, int half) {
  const int t = 8 * v.g + (v.lane >> 3);
  LAS const f32x4* src = (LAS const f32x4*)(X + t * 256 + (((2 * (v.lane & 7) + half) ^ (t & 15)) * 16));
  return (src[0] + src[512]) + (src[1024] + src[1536]);
}
DI f32x4 silu4(f32x4 d) {
#pragma unroll
  for (int i = 0; i < 4; ++i) d[i] *= __builtin_amdgcn_rcpf(1.f + __builtin_amdgcn_exp2f(-1.4426950408889634f * d[i]));
  return d;
}
DI u32x2 fin4(u32x2 x, f32x4 s, f32x4 g) {
  return u32x2{pk(__builtin_fmaf(s[0], g[0], __uint_as_float(x.x << 16)), __builtin_fmaf(s[1], g[1], __uint_as_float(x.x & 0xffff0000u))),
               pk(__builtin_fmaf(s[2], g[2], __uint_as_float(x.y << 16)), __builtin_fmaf(s[3], g[3], __uint_as_float(x.y & 0xffff0000u)))};
}
DI void phase_c(const Params& P, const Cx& v, u32x4 (&hf)[28], u32x4 (&sw)[20], bool first) {
  const int lane = v.lane, h = lane >> 5, tb = 2 * v.rt + v.c;
  const int qq = (lane & 15) >> 2, p = lane & 3, g1 = (lane >> 4) & 1;
  LAS const char* b0 = v.L + v.g * 8192 + (4 * h + qq) * 128 + (g1 + (qq & 2)) * 32 + 8 * p;
  LAS const char* b1 = v.L + v.g * 8192 + (4 * h + qq) * 128 + (g1 + (2 ^ (qq & 2))) * 32 + 8 * p;
  const unsigned opoll = first && lane >= 32 ? WS_FO + (v.w * 32 + lane - 32) * 64
                                             : WS_FO + ((v.rt >> 2) * 32 + 8 * v.g + (lane & 7)) * 64 + (tb & 7) * 8;
  const unsigned obase = uni(WS_O + (tb * 32 + 8 * v.g) * 1024);
  const rsrc_t rx = mk_rsrc(P.x, 2048 * 512 * 2);
  const unsigned xoff = (32 * tb + 8 * v.g + (lane >> 3)) * 1024 + v.ct * 128 + (lane & 7) * 16;
  LAS char* xd = v.L + (v.c ? 0 : 3 * REG);
  LAS char* xs = v.L + REG + v.c * REG;
  f32x16 d0 = {}, d1 = {}, s0 = {}, s1 = {};
  f32x4 ga, gb;
  bf16x8 wf[3][2];
  u32x4 of[8];
  u32x2 fl;
  u32x4 xv;
  constexpr int RG[9] = {0, 1, 2, 4, 3, 0, 1, 2, 4};
  auto rd = [&](auto S_) __attribute__((always_inline)) {
    constexpr int s = decltype(S_)::v;
    wf[s % 3][0] = rdW<1024>(b0 + RG[s >> 2] * REG + (s & 3) * 2048);
    wf[s % 3][1] = rdW<1024>(b1 + RG[s >> 2] * REG + (s & 3) * 2048);
  };
  static_for<0, 36>([&](auto S_) __attribute__((always_inline)) {
    constexpr int s = decltype(S_)::v, gp = s >> 2;
    auto oreq = [&](auto N_) __attribute__((always_inline)) {
      constexpr int n = decltype(N_)::v;
      of[n] = ld128h<(n & 3) * 1024>(v.ws, lane * 16 + (c_grp(v, 7 + (n >> 2)) << 12), obase);
    };
    if (s == 0) {
      static_for<14, 20>([&](auto I_) __attribute__((always_inline)) { hf[decltype(I_)::v] = h_load<decltype(I_)::v>(v); });
      wait_vm<6>();
      put_flag(v.ws, lane, WS_FO + (v.G * 32 + v.hd) * 64 + v.w * 8, v.tag);
      wait_bar<63, 0>();
      PIN4(hf, 0); PIN4(hf, 4); PIN4(hf, 8); PIN(hf[12]); PIN(hf[13]); PIN4(sw, 12); PIN4(sw, 16);
      rd(IC<0>{}); rd(IC<1>{});
      c_piece(v, 3, 4, 3);
    }
    if (s == 8) {
      wait_bar<OCHK <= 12 ? 9 : 8, 8>();
      PIN(hf[14]); PIN(hf[15]); PIN4(hf, 16);
    }
    if (s == OCHK) {
      asm volatile("s_waitcnt vmcnt(0)" : "+v"(fl)::"memory");
      PIN4(hf, 20); PIN4(hf, 24);
      check_flags(v.ws, opoll, v.tag, fl);
    }
    if (s == 16) wait_bar<63, 8>();
    if (s == 28) {
      wait_bar<1, 0>();
      if (first && v.w == 0) put_flag(v.ws, lane, WS_EPOCH, v.tag);
      PIN4(of, 0); PIN4(of, 4); rd(IC<28>{}); rd(IC<29>{});
    }
    if (s == 32) wait_bar<63, 8>();
    if ((s & 3) == 0) STAMP2(40 + gp);
    if constexpr (s + 2 < 36 && s != 26 && s != 27) rd(IC<s + 2>{});
    const bf16x8 f = BF(gp < 7 ? hf[gp < 7 ? s : 0] : of[gp < 7 ? 0 : s - 28]);
    f32x16& a0 = gp < 7 ? d0 : s0;
    f32x16& a1 = gp < 7 ? d1 : s1;
    a0 = MFMA(wf[s % 3][0], f, a0);
    PIN(a0);
    SBAR();
    if constexpr (s < 4) hf[20 + 2 * s] = h_load<20 + 2 * s>(v);
    if constexpr (s >= OCHK && s < OCHK + 4) oreq(IC<2 * (s - OCHK)>{});
    if constexpr (s < 4) *(LAS u32x4*)(v.L + c_dst(v, 3, s) + lane * 16) = sw[s];
    if constexpr (s >= 8 && s < 14) {
      constexpr int j = s - 8;
      *(LAS u32x4*)(v.L + c_dst(v, j >> 2, j & 3) + lane * 16) = sw[4 + j];
      if constexpr (j < 2) *(LAS u32x4*)(v.L + c_dst(v, 1, 2 + j) + lane * 16) = sw[10 + j];
    }
    if constexpr (s >= 16 && s < 24) *(LAS u32x4*)(v.L + c_dst(v, s < 20 ? 2 : 4, s & 3) + lane * 16) = sw[s - 4];
    if (s == 33) { ga = silu4(ga); PIN(ga); }
    if (s == 34) { gb = silu4(gb); PIN(gb); }
    SBAR();
    a1 = MFMA(wf[s % 3][1], f, a1);
    PIN(a1);
    SBAR();
    if constexpr (s < 4) hf[21 + 2 * s] = h_load<21 + 2 * s>(v);
    if (s == OCHK - 5) fl = ld64h(v.ws, opoll);
    if constexpr (s >= OCHK && s < OCHK + 4) oreq(IC<2 * (s - OCHK) + 1>{});
    if (s == OCHK + 4) asm volatile("buffer_load_dwordx4 %0, %1, %2, 0 offen" : "=v"(xv) : "v"(xoff), "s"(rx) : "memory");
    if (s == 28) c_scatter(v, xd, d0, d1);
    if (s == 32) { ga = c_gather(v, xd, 0); gb = c_gather(v, xd, 1); }
    SBAR();
  });
  STAMP(14);
  c_scatter(v, xs, s0, s1);
  asm volatile("s_waitcnt vmcnt(0) lgkmcnt(0)\n\ts_barrier" : "+v"(xv)::"memory");
  const u32x2 y0 = fin4(u32x2{xv.x, xv.y}, c_gather(v, xs, 0), ga), y1 = fin4(u32x2{xv.z, xv.w}, c_gather(v, xs, 1), gb);
  __builtin_amdgcn_raw_buffer_store_b128(u32x4{y0.x, y0.y, y1.x, y1.y}, mk_rsrc(P.out, 2048 * 512 * 2), xoff, 0, 0);
  STAMP(15); STAMPR(31); STAMP_FLUSH();
}

struct AB {
  bf16x8 X[32];
  bf16x8 wf[3][2];
  bf16x8 sel;
  unsigned nm;
  float r, k1, k2, ir2;
  int gr;
  int rb, cg, strip0, dblA, dblB;
  int kx;
  unsigned voffX, voffQ, voffF;
  rsrc_t rx, rq, rf, ru;
};
DI void x_piece(const Cx& v, const AB& a, int kc, int slot, int i) {
  glds(a.rx, a.voffX ^ (64 * i), (a.rb * 128 + 16 * v.w + 4 * i) * 1024 + (kc ^ a.kx) * 256, slot * SLOT + (4 * v.w + i) * 1024);
}
DI void q_piece(const Cx& v, const AB& a, int strip, int half, int slot, int i) {
  glds(a.rq, a.voffQ, (128 * ((2 * half + v.c) ^ a.kx) + 32 * v.g + 8 * i) * 3072 + strip * 128, slot * SLOT + v.c * SUB1 + v.g * 4096 + i * 1024);
}
DI void q4_piece(const Cx& v, const AB& a, int kq, int dst, int i) {
  glds(a.rq, a.voffQ, (128 * (kq ^ a.kx) + 16 * v.w + 8 * i) * 3072 + a.strip0 * 128, dst + (2 * v.w + i) * 1024);
}
DI void f_piece(const Cx& v, const AB& a, int dbl, int kq, int slot, int i) {
  glds(a.rf, a.voffF, (128 * (kq ^ a.kx) + 32 * v.g + 8 * i) * 3584 + dbl * 128, slot * SLOT + v.c * SUB1 + v.g * 4096 + i * 1024);
}
template <bool TB> DI void w_piece(const Cx& v, const AB& a, int j, int i) {
  if (TB && j < 6) q_piece(v, a, a.strip0 + (j >> 1), j & 1, (j - 2) & 3, i);
  else f_piece(v, a, j < 6 ? a.dblA : a.dblB, (j - 2) & 3, (j - 2) & 3, i);
}
DI LAS char* cs_slot(const Cx& v, int j) { return v.L + AUX + (j * 2 + v.c) * 1024 + v.lane * 4; }
DI void cs_put(const Cx& v, int j, const f32x4& c) { *(LAS float*)(cs_slot(v, j) + v.g * 256) = (v.lane & 16) ? c[1] : c[0]; }
DI void q_store(const Cx& v, const AB& a, const f32x16& acc, int strip, int hh) {
  const float sc = strip < 8 ? a.r * QSCALE : a.r;
  u32x4 o;
#pragma unroll
  for (int i = 0; i < 4; ++i) o[i] = pk(acc[8 * hh + 2 * i] * sc, acc[8 * hh + 2 * i + 1] * sc);
  st128h(v.ws, v.lane * 16, WS_QKV + ((strip * 4 + 2 * v.c + hh) * 64 + 4 * a.rb + v.g) * 1024, o);
}
DI void q_flag(const Cx& v, const AB& a, int strip) { put_flag(v.ws, v.lane, WS_FQ + (a.rb * 24 + strip) * 64 + v.w * 8, v.tag); }
DI void put_h(const Cx& v, const AB& a, int dbl, int j, const u32x4& hv) {
  st128h(v.ws, v.lane * 16, WS_H + ((4 * a.rb + v.g) * 112 + 4 * dbl + 2 * v.c + j) * 1024, hv);
}
struct Bases { unsigned b[4]; };
DI Bases mk_bases(const Cx& v, const AB& a, int g1mul) {
  int l = v.lane;
  PIN(l);
  const int qq = (l & 15) >> 2;
  const int o = ((l >> 4) & 1) * g1mul + (8 * (l >> 5) + qq) * 128 + ((2 * v.c) ^ (qq & 2)) * 32 + 8 * (l & 3);
  Bases r;
#pragma unroll
  for (int i = 0; i < 4; ++i) { r.b[i] = o + (i ^ a.gr) * 4096; PIN(r.b[i]); }
  return r;
}
DI void bump(Bases& B, int d) {
#pragma unroll
  for (int i = 0; i < 4; ++i) { B.b[i] += d; PIN(B.b[i]); }
}
#define WRD(B, off, i) rdW<512>((LAS const char*)(size_t)((B).b[(i) >> 1]) + (off) + ((i) & 1) * 2048)

DI u32x2 make_tag(u32x2 e) {
  const unsigned long long z = ((unsigned long long)__builtin_amdgcn_readfirstlane(e.y) << 32 | (unsigned)__builtin_amdgcn_readfirstlane(e.x));
  const unsigned long long t = (z ^ (z >> 31)) * 0x9E3779B97F4A7C15ull + 0xD1B54A32D192ED03ull;
  return u32x2{(unsigned)t, (unsigned)(t >> 32)};
}

DI void stats_put(const Cx& v, const AB& a, const u32x4& xr) {
  float s1 = 0.f, s2 = 0.f;
#pragma unroll
  for (int d = 0; d < 4; ++d) {
    const float lo = __uint_as_float(xr[d] << 16), hi = __uint_as_float(xr[d] & 0xffff0000u);
    s1 += lo + hi;
    s2 = __builtin_fmaf(lo, lo, __builtin_fmaf(hi, hi, s2));
  }
  const float mean = wave_sum63(s1) * (1.f / 512.f);
  const float var = __builtin_fmaxf(__builtin_fmaf(-mean, mean, wave_sum63(s2) * (1.f / 512.f)), 0.f);
  const float sd = __builtin_amdgcn_sqrtf(rbf(rbf(var) + 1e-5f));
  put_flag(v.ws, v.lane ^ 63, WS_ST + (a.rb * 128 + a.cg * 8 + v.w) * 8, u32x2{pk(-mean, sd), v.tag.x});
}
template <bool TB, int KQ, int XS, int WO>
DI void p_step(const Cx& v, AB& a, const Bases& B, f32x16& acc, f32x4& cs) {
  const int t = v.lane & 31;
  const unsigned u = (((v.lane >> 5) ^ (t & 15)) * 16) ^ (64 * a.gr);
  LAS const char* xr = v.L + XS * SLOT + (32 * v.g + t) * 256;
  bf16x8 wf[8];
#pragma unroll
  for (int i = 0; i < 3; ++i) {
    a.X[8 * KQ + i] = *(LAS const bf16x8*)(xr + (u ^ (32 * i)));
    wf[i] = WRD(B, WO, i);
  }
#pragma unroll
  for (int i = 0; i < 8; ++i) {
    if (i < 5) {
      a.X[8 * KQ + i + 3] = *(LAS const bf16x8*)(xr + (u ^ (32 * (i + 3))));
      wf[i + 3] = WRD(B, WO, i + 3);
    }
    if (KQ == 0) { if (i < 4) x_piece(v, a, 2, 1, i); else if (i < 6) q4_piece(v, a, 2, W1A, i - 4); }
    if (KQ == 1 && i < 4) x_piece(v, a, 3, 3, i);
    if (KQ == 2) { if (i < 2) q4_piece(v, a, 3, 2 * SLOT, i); else if (i < 6) w_piece<TB>(v, a, 2, i - 2); }
    if (KQ == 3 && i < 4) w_piece<TB>(v, a, 3, i);
    acc = MFMA(wf[i], a.X[8 * KQ + i], acc);
    if (i < 2) COLSUM(wf[i], cs);
    PIN(acc); PIN(cs);
    SBAR();
  }
}
template <bool TB> DI void prologue(Cx& v, AB& a, f32x16& acc, u32x2& epoch, u32x4& xr, u32x2& st) {
  Bases B = mk_bases(v, a, 32);
  bump(B, 2 * SLOT);
  f32x4 cs = {};
  STAMP2(23);
  wait_bar<8, 0>();
  STAMP(1);
  p_step<TB, 0, 3, 0>(v, a, B, acc, cs);
  asm volatile("s_waitcnt vmcnt(6)" : "+v"(epoch), "+v"(xr)::"memory");
  v.tag = make_tag(epoch);
  stats_put(v, a, xr);
  STAMP2(16); wait_bar<7, 0>(); STAMP2(17);
  p_step<TB, 1, 0, SUB1>(v, a, B, acc, cs);
  STAMP2(18); wait_bar<4, 0>(); STAMP2(19);
  bump(B, W1A - 2 * SLOT);
  p_step<TB, 2, 1, 0>(v, a, B, acc, cs);
  STAMP2(20); wait_bar<4, 0>(); STAMP2(21);
  bump(B, 2 * SLOT - W1A);
  p_step<TB, 3, 3, 0>(v, a, B, acc, cs);
  STAMP2(22);
  cs_put(v, 0, cs);
  st = ld64h(v.ws, WS_ST + (a.rb * 128 + 32 * v.g + (v.lane & 31)) * 8);
  wait_bar<5, 0>();
}
DI void row_stats(const Cx& v, AB& a, unsigned g) {
  const float sd = __uint_as_float(g & 0xffff0000u);
  const float c1 = -2.f * 0.7978845608028654f * 1.4426950408889634f;
  a.r = __builtin_amdgcn_rcpf(sd);
  a.k1 = c1 * a.r;
  a.k2 = c1 * 0.044715f * a.r * a.r * a.r;
  a.ir2 = sd * sd;
  a.nm = v.lane < 32 ? (g & 0xffffu) | (g << 16) : 0u;
}
enum { K_QQ = 0, K_D0 = 1, K_D1L = 2, K_D0L = 3 };
template <bool Q> DI void rd_step(AB& a, const Bases& B, int s, int ph) {
  const int kq = (s >> 3) & 3, i = s & 7, so = (kq & 1) * SLOT;
  if (Q) {
    a.wf[ph][0] = WRD(B, so + (i >> 2) * SUB1, (2 * i) & 7);
    a.wf[ph][1] = WRD(B, so + (i >> 2) * SUB1, ((2 * i) & 7) + 1);
  } else {
    a.wf[ph][0] = WRD(B, so, i);
    a.wf[ph][1] = WRD(B, so + 32, i);
  }
}
template <int KIND> DI void body(Cx& v, AB& a, f32x16& p0, f32x16& p1, AtPre& ap, u32x2& st,
                                 u32x4 (&sw)[20]) {
  constexpr bool Q = KIND == K_QQ, FIRST = KIND == K_QQ || KIND == K_D0, TB = KIND == K_QQ || KIND == K_D0L;
  constexpr int PH0 = KIND == K_D1L ? 2 : 0;
  constexpr int SB = FIRST ? 32 : 36;
  Bases B = mk_bases(v, a, Q ? 32 : SUB1);
  f32x16 a0 = {}, a1 = {};
  f32x4 c0 = {}, c1 = {};
  float cp0, cp1, hlo;
  u32x4 hv;
  u32x2 fl;
  unsigned fo;
  if (KIND != K_D1L) {
    rd_step<Q>(a, B, 0, 0);
    rd_step<Q>(a, B, 1, 1);
  }
  static_for<0, 32>([&](auto S_) __attribute__((always_inline)) {
    constexpr int s = decltype(S_)::v, kq = s >> 3, i = s & 7;
    if (s == 8 && !FIRST) c_setup(v);
    if (s == 14) bump(B, 2 * SLOT);
    if (s == 30 && KIND == K_D0) bump(B, -2 * SLOT);
    const bool late = FIRST && (s == 6 || s == 7);
    if (!late && (s < 30 || KIND == K_D0)) rd_step<Q>(a, B, s + 2, (s + 2 + PH0) % 3);
    if (s == 0 || (Q && s == 16)) cp0 = cs_get(cs_slot(v, 0));
    if (s == 1 && KIND == K_D1L) cp1 = cs_get(cs_slot(v, 1));
    const bf16x8 w0 = a.wf[(s + PH0) % 3][0], w1 = a.wf[(s + PH0) % 3][1];
    f32x16& acA = Q ? (kq < 2 ? a0 : a1) : a0;
    f32x16& acB = Q ? (kq < 2 ? a0 : a1) : a1;
    f32x4& csA = Q ? (kq < 2 ? c0 : c1) : c0;
    f32x4& csB = Q ? (kq < 2 ? c0 : c1) : c1;
    constexpr bool CS = Q ? (i & 3) == 0 : i < 2;
    constexpr int e = KIND != K_D1L ? -1 : (s == 30 ? 14 : ((s & 1) && s >= 3 ? (s == 31 ? 15 : (s - 3) >> 1) : -1));
    float ga, ex;
    acA = MFMA(w0, a.X[Q ? 16 * (kq & 1) + 2 * i : s], acA);
    PIN(acA);
    SBAR();
    if (CS) { COLSUM(w0, csA); PIN(csA); }
    if (FIRST && kq == 0) w_piece<TB>(v, a, 4 + (i >> 2), i & 3);
    else if ((i & 1) == 0) {
      if (FIRST) w_piece<TB>(v, a, 5 + kq, i >> 1);
      else if (kq == 0) w_piece<TB>(v, a, 9, i >> 1);
      else c_piece(v, kq - 1, kq - 1, i >> 1);
    }
    if constexpr (e >= 0) {
      ga = (e < 8 ? p0 : p1)[e & 7];
      ex = __builtin_amdgcn_exp2f(ga * __builtin_fmaf(ga * ga, a.k2, a.k1));
      ga *= (e < 8 ? p0 : p1)[8 + (e & 7)];
      PIN(ga); PIN(ex);
    }
    if (FIRST && s == 9) {
      const int per = v.w == 1 ? 16 : 56;
      asm volatile("s_mov_b32 m0, %2\n\ts_nop 0\n\tbuffer_load_dword %0, %1, 0 offen lds"
                   ::"v"(v.lane < per && v.w < WARM ? (blockIdx.x * per + v.lane) * 128 : 0x40000000u),
                     "s"(v.w == 0 ? v.rwd : (v.w == 1 ? v.rwo : (v.w == 2 ? a.rf : a.ru))), "s"(W1A) : "memory");
    }
    if (FIRST && s == 5) {
      asm volatile("s_waitcnt vmcnt(6)" : "+v"(st)::"memory");
      check_granule(v.ws, WS_ST + (a.rb * 128 + 32 * v.g + (v.lane & 31)) * 8, v.tag.x, st);
      row_stats(v, a, st.x);
      PIN(a.nm); PIN(a.r); PIN(a.k1); PIN(a.k2); PIN(a.ir2);
    }
    SBAR();
    acB = MFMA(w1, a.X[Q ? 16 * (kq & 1) + 2 * i + 1 : s], acB);
    PIN(acB);
    SBAR();
    if (CS) { COLSUM(w1, csB); PIN(csB); }
    if constexpr (e >= 0) {
      const float hx = ga * __builtin_amdgcn_rcpf(__builtin_fmaf(ex, a.ir2, a.ir2));
      if (e & 1) { hv[(e & 7) >> 1] = pk(hlo, hx); PIN(hv[(e & 7) >> 1]); }
      else { hlo = hx; PIN(hlo); }
      if (e == 15) put_h(v, a, a.dblA, 1, hv);
    }
    if (KIND == K_D1L) {
      if (s == 1) { ln_fix(p0, cp0, a.nm); PIN(p0); }
      if (s == 2) { ln_fix(p1, cp1, a.nm); PIN(p1); }
      if (s == 18) put_h(v, a, a.dblA, 0, hv);
    } else {
      constexpr int o = FIRST ? 4 : 0;
      const int st0 = KIND == K_D0L ? a.strip0 + 2 : a.strip0;
      if (s == 2 + o) { ln_fix(p0, cp0, a.nm); PIN(p0); }
      if (s == 5 + o || s == 7 + o) q_store(v, a, p0, st0, (s - 5 - o) >> 1);
      if (s == 13 + o) { wait_vm<FIRST ? 3 : 5>(); q_flag(v, a, st0); }
      if (Q) {
        if (s == 13) cs_put(v, 0, c0);
        if (s == 18) { ln_fix(a0, cp0, a.nm); PIN(a0); }
        if (s == 21 || s == 23) q_store(v, a, a0, a.strip0 + 1, (s - 21) >> 1);
        if (s == 29) { cs_put(v, 0, c1); wait_vm<3>(); q_flag(v, a, a.strip0 + 1); }
      }
    }
    if (!Q && s == (FIRST ? 28 : 26)) {
      cs_put(v, 0, c0);
      cs_put(v, 1, c1);
    }
    if (!FIRST) {
      if (s == 27) { SBAR(); wait_bar<63, 4>(); cp0 = cs_get(cs_slot(v, 0)); cp1 = cs_get(cs_slot(v, 1)); }
      if (s == 29) { ln_fix(a0, cp0, a.nm); PIN(a0); }
      if (s == 30) { ln_fix(a1, cp1, a.nm); PIN(a1); }
    }
    if (!FIRST) {
      if constexpr ((s & 1) && s >= 9 && s < 25 ) sw[(s - 9) >> 1] = c_stash(v, (s - 9) >> 1);
      if (s == 23) { fo = at_flag_off(v); fl = ld64h(v.ws, fo); }
      if (s == 27) {
        asm volatile("s_waitcnt vmcnt(2)" : "+v"(fl)::"memory");
        check_flags(v.ws, fo, v.tag, fl);
        ap.k0 = at_load(v, 1);
      }
      if (s == 28) ap.k1 = at_load(v, 2);
      if (s == 29) ap.v0 = at_load(v, 3);
      if (s == 30) ap.v1 = at_load(v, 4);
      if (s == 31) ap.q = at_load(v, 0);
    }
    SBAR();
    if (i == 7) {
      STAMP2(SB + kq);
      if (!FIRST && kq == 1) wait_bar<8, 8>();
      else if (!FIRST && kq == 2) wait_bar<63, 8>();
      else if (!FIRST && kq == 3) {}
      else if (kq == 3 && KIND != K_D0) wait_bar<4, 0>();
      else wait_bar<4, 8>();
      STAMP2(SB + 18 + kq);
      if (FIRST && s == 7) {
        rd_step<Q>(a, B, 8, 8 % 3);
        rd_step<Q>(a, B, 9, 9 % 3);
      }
    }
  });
  if (Q) p0 = a1;
  else { p0 = a0; p1 = a1; }
}
DI void ab_tail(const Cx& v, const AB& a, int dbl, const f32x16& p0, const f32x16& p1, AtPre& ap, u32x4 (&sw)[20]) {
  u32x4 hv;
  static_for<0, 8>([&](auto I_) __attribute__((always_inline)) {
    constexpr int i = decltype(I_)::v, j = 2 * (i & 3);
    const f32x16& p = i < 4 ? p0 : p1;
    if constexpr (i < 3) {
      sw[8 + 2 * i] = c_stash(v, 8 + 2 * i);
      sw[9 + 2 * i] = c_stash(v, 9 + 2 * i);
    }
    hv[i & 3] = pk(geglu(p[j], p[8 + j], a.k1, a.k2, a.ir2), geglu(p[j + 1], p[9 + j], a.k1, a.k2, a.ir2));
    PIN(hv[i & 3]);
    if constexpr ((i & 3) == 3) put_h(v, a, dbl, i >> 2, hv);
    SBAR();
  });
  asm volatile("s_waitcnt vmcnt(8)\n\ts_barrier" : "+v"(ap.q), "+v"(ap.k0), "+v"(ap.k1), "+v"(ap.v0), "+v"(ap.v1)::"memory");
  at_stage(v, ap);
  wait_bar<0, 0>();
  PIN4(sw, 0); PIN4(sw, 4); PIN4(sw, 8); PIN(sw[12]); PIN(sw[13]);
  put_flag(v.ws, v.lane, WS_FH + (a.rb * 16 + a.cg) * 64 + v.w * 8, v.tag);
}

template <bool TB> DI void ab_path(Cx& v, AB a, AtPre& ap, u32x4 (&sw)[20]) {
  int l = v.lane;
  PIN(l);
  a.voffX = (l >> 4) * 1024 + (((l & 15) ^ (l >> 4)) * 16);
  a.voffQ = w_voff(l, 3072);
  a.voffF = w_voff(l, 3584);
  const unsigned sl = ((l ^ (l >> 4)) & 1) ? 0u : 0x3f803f80u;
  a.sel = __builtin_bit_cast(bf16x8, u32x4{sl, sl, sl, sl});
  u32x2 epoch = ld64h(v.ws, WS_EPOCH), st;
  u32x4 xr;
  asm volatile("buffer_load_dwordx4 %0, %1, %2, %3 offen" : "=v"(xr) : "v"(l * 16), "s"(a.rx), "s"((a.rb * 128 + a.cg * 8 + v.w) * 1024) : "memory");
  f32x16 p0 = {}, p1;
  prologue<TB>(v, a, p0, epoch, xr, st);
  STAMP(2);
  body<TB ? K_QQ : K_D0>(v, a, p0, p1, ap, st, sw);
  STAMP(3);
  body<TB ? K_D0L : K_D1L>(v, a, p0, p1, ap, st, sw);
  STAMP(4);
  ab_tail(v, a, a.dblB, p0, p1, ap, sw);
}

__global__ __launch_bounds__(512, 2) void fused_kernel(Params P) {
  extern __shared__ __attribute__((aligned(16))) char smem[];
  Cx v;
  v.L = (LAS char*)smem;
  v.lane = threadIdx.x & 63;
  v.w = __builtin_amdgcn_readfirstlane(threadIdx.x >> 6);
  v.g = v.w & 3;
  v.c = v.w >> 2;
  const int xc = blockIdx.x & 7, idx = blockIdx.x >> 3;
  v.ws = mk_rsrc(P.ws, WS_END);
  v.rwd = mk_rsrc(P.wd, 1792 * 512 * 2);
  v.rwo = mk_rsrc(P.wo, 512 * 512 * 2);
  v.rt = v.hd = xc * 4 + (idx & 3);
  v.ct = v.G = idx >> 2;
  AtPre ap;
  u32x4 sw[20];
  STAMPR(30); STAMP(0);
  {
    AB a;
    a.rb = (xc & 1) * 8 + (idx & 7);
    const int cq = xc >> 1, j = idx >> 3;
    const bool typeB = j == 3;
    a.cg = cq * 4 + j;
    a.gr = v.g ^ (2 * v.c);

    a.strip0 = 6 * cq + j;
    const int swp = typeB ? 0 : (a.rb >> 2) & 1;
    a.dblA = 7 * cq + 2 * j + swp;
    a.dblB = typeB ? a.dblA : 7 * cq + 2 * j + 1 - swp;
    a.kx = a.rb & 3;
    a.rx = mk_rsrc(P.x, 2048 * 512 * 2);
    a.rq = mk_rsrc(P.wqkv, 512 * 1536 * 2);
    a.rf = mk_rsrc(v.c ? P.wu : P.wg, 512 * 1792 * 2);
    a.ru = mk_rsrc(P.wu, 512 * 1792 * 2);
    a.voffX = (v.lane >> 4) * 1024 + (((v.lane & 15) ^ (v.lane >> 4)) * 16);
    a.voffQ = w_voff(v.lane, 3072);
    STAMP2(24);
#pragma unroll
    for (int i = 0; i < 4; ++i) x_piece(v, a, 0, 3, i);
#pragma unroll
    for (int i = 0; i < 2; ++i) q4_piece(v, a, 0, 2 * SLOT, i);
#pragma unroll
    for (int i = 0; i < 4; ++i) x_piece(v, a, 1, 0, i);
#pragma unroll
    for (int i = 0; i < 2; ++i) q4_piece(v, a, 1, 2 * SLOT + SUB1, i);
    if (typeB) ab_path<true>(v, a, ap, sw);
    else ab_path<false>(v, a, ap, sw);
  }
  STAMP(6);
  c_setup(v);
  v.tag = u32x2{uni(v.tag.x), uni(v.tag.y)};
#pragma unroll
  for (int i = 0; i < 3; ++i) c_piece(v, 3, 4, i);
  u32x4 hf[28];
  if (v.G) at_body<true>(v, ap.q, hf, sw);
  else at_body<false>(v, ap.q, hf, sw);
  STAMP(7);
  c_setup(v);
  phase_c(P, v, hf, sw, blockIdx.x == 0);
}
}

extern "C" void kernel_launch(void* const* d_in, const int* in_sizes, int n_in, void* d_out, int out_size, void* d_ws,
                              size_t ws_size, hipStream_t stream) {
  Params P;
  P.x = d_in[0]; P.wqkv = d_in[1]; P.wo = d_in[2];
  P.wg = d_in[4]; P.wu = d_in[5]; P.wd = d_in[6];
  P.out = d_out; P.ws = d_ws;
  (void)hipFuncSetAttribute((const void*)fused_kernel, hipFuncAttributeMaxDynamicSharedMemorySize, LDS_BYTES);
  fused_kernel<<<256, 512, LDS_BYTES, stream>>>(P);
}
```

```cpp
#include <hip/hip_runtime.h>
#include <hip/hip_bf16.h>
#include <stdint.h>

typedef short bf16x8 __attribute__((ext_vector_type(8)));
typedef short s16x4 __attribute__((ext_vector_type(4)));
typedef float f32x16 __attribute__((ext_vector_type(16)));
typedef float f32x4 __attribute__((ext_vector_type(4)));
typedef float f32x2 __attribute__((ext_vector_type(2)));
typedef unsigned u32x4 __attribute__((ext_vector_type(4)));
typedef unsigned u32x2 __attribute__((ext_vector_type(2)));
typedef __bf16 bf2 __attribute__((ext_vector_type(2)));
typedef __amdgpu_buffer_rsrc_t rsrc_t;
#define LAS __attribute__((address_space(3)))
#define DI __device__ __forceinline__
#define SBAR() __builtin_amdgcn_sched_barrier(0)
#define PIN(x) asm volatile("" : "+v"(x))
#define RFL(x) __builtin_amdgcn_readfirstlane(x)
#define MFMA(a, b, c) __builtin_amdgcn_mfma_f32_32x32x16_bf16((a), (b), (c), 0, 0, 0)

#define STAMP(i)
#define STAMPR(i)
#define STAMP_FLUSH()
#define STAMP2(i)

#define OCHK 19
namespace {
constexpr unsigned WS_EPOCH = 0;
constexpr unsigned WS_FQ = 1024;
constexpr unsigned WS_FH = WS_FQ + 16 * 24 * 64;
constexpr unsigned WS_FO = WS_FH + 16 * 16 * 64;
constexpr unsigned WS_ST = 65536;
constexpr unsigned WS_QKV = 131072;
constexpr unsigned SEC_BYTES = 32 * 64 * 1024;
constexpr unsigned WS_H = WS_QKV + 3 * SEC_BYTES;
constexpr unsigned WS_O = WS_H + 64 * 112 * 1024;
constexpr unsigned WS_END = WS_O + 64 * 32 * 1024;
static_assert(WS_FO + 256 * 64 <= WS_ST, "flags overlap");
constexpr int SLOT = 32800, SUB1 = 16416;
constexpr int AUX = 4 * SLOT;
constexpr int W1A = AUX + 8192;
constexpr int REG = 32768;
constexpr int LDS_BYTES = 163840;
constexpr int KV = 3 * REG;
constexpr int ONES = LDS_BYTES - 1024;
constexpr float QSCALE = 0.25f * 1.4426950408889634f;
constexpr unsigned SPIN_CAP = 1u << 18;

struct Params {
  const void *x, *wqkv, *wo, *wg, *wu, *wd;
  void *out, *ws;
};

template <int I> struct IC { static constexpr int v = I; };
template <int I, int N, class F> DI void static_for(F&& f) {
  if constexpr (I < N) {
    f(IC<I>{});
    static_for<I + 1, N>(f);
  }
}
template <int VM> DI void wait_vm() { asm volatile("s_waitcnt vmcnt(%0)" ::"n"(VM) : "memory"); }
template <int VM, int LG> DI void wait_bar() { asm volatile("s_waitcnt vmcnt(%0) lgkmcnt(%1)\n\ts_barrier" ::"n"(VM), "n"(LG) : "memory"); }
DI unsigned uni(unsigned x) {
  x = __builtin_amdgcn_readfirstlane(x);
  asm volatile("" : "+s"(x));
  return x;
}
DI rsrc_t mk_rsrc(const void* p, unsigned bytes) { return __builtin_amdgcn_make_buffer_rsrc((void*)p, 0, bytes, 0x00020000); }
DI void glds(rsrc_t rs, unsigned voff, unsigned soff, unsigned lds) {
  asm volatile("s_mov_b32 m0, %2\n\ts_nop 0\n\tbuffer_load_dwordx4 %0, %1, %3 offen lds" ::"v"(voff), "s"(rs),
               "s"(RFL(lds)), "s"(RFL(soff)) : "memory");
}
template <int IMM = 0> DI u32x4 ld128h(rsrc_t rs, unsigned voff, unsigned soff) {
  u32x4 r;
  asm volatile("buffer_load_dwordx4 %0, %1, %2, %3 offen offset:%4 sc1" : "=v"(r) : "v"(voff), "s"(rs), "s"(RFL(soff)), "n"(IMM) : "memory");
  return r;
}
DI u32x2 ld64h(rsrc_t rs, unsigned voff) {
  u32x2 r;
  asm volatile("buffer_load_dwordx2 %0, %1, %2, 0 offen sc1" : "=v"(r) : "v"(voff), "s"(rs) : "memory");
  return r;
}
DI void st128h(rsrc_t rs, unsigned voff, unsigned soff, u32x4 d) {
  asm volatile("buffer_store_dwordx4 %0, %1, %2, %3 offen sc1\n\ts_nop 1" ::"v"(d), "v"(voff), "s"(rs), "s"(RFL(soff)) : "memory");
}
DI void put_flag(rsrc_t ws, int lane, unsigned soff, u32x2 tag) {
  asm volatile("buffer_store_dwordx2 %0, %1, %2, %3 offen sc1" ::"v"(tag), "v"(lane ? 0x80000000u : 0u), "s"(ws), "s"(RFL(soff)) : "memory");
}
DI void check_flags(rsrc_t ws, unsigned voff, u32x2 tag, u32x2 v) {
  unsigned cnt;
  asm volatile(
      "s_mov_b32 %1, %5\n"
      "1:\n\t"
      "v_cmp_ne_u64 vcc, %2, %0\n\t"
      "s_cbranch_vccz 2f\n\t"
      "s_sleep 1\n\t"
      "buffer_load_dwordx2 %0, %3, %4, 0 offen sc1\n\t"
      "s_waitcnt vmcnt(0)\n\t"
      "s_sub_u32 %1, %1, 1\n\t"
      "s_cbranch_scc0 1b\n"
      "2:"
      : "+v"(v), "=&s"(cnt)
      : "s"(tag), "v"(voff), "s"(ws), "n"(SPIN_CAP)
      : "vcc", "scc", "memory");
}
DI void check_granule(rsrc_t ws, unsigned voff, unsigned tag, u32x2& v) {
  unsigned cnt, lo = v.x, hi = v.y;
  asm volatile(
      "s_mov_b32 %2, %6\n"
      "1:\n\t"
      "v_cmp_ne_u32 vcc, %3, %1\n\t"
      "s_cbranch_vccz 2f\n\t"
      "s_sleep 1\n\t"
      "buffer_load_dword %1, %4, %5, 0 offen offset:4 sc1\n\t"
      "s_waitcnt vmcnt(0)\n\t"
      "buffer_load_dword %0, %4, %5, 0 offen sc1\n\t"
      "s_waitcnt vmcnt(0)\n\t"
      "s_sub_u32 %2, %2, 1\n\t"
      "s_cbranch_scc0 1b\n"
      "2:"
      : "+v"(lo), "+v"(hi), "=&s"(cnt)
      : "s"(tag), "v"(voff), "s"(ws), "n"(SPIN_CAP)
      : "vcc", "scc", "memory");
  v.x = lo;
}
template <int CTRL> DI float dpp_add(float v) {
  return v + __builtin_bit_cast(float, __builtin_amdgcn_update_dpp(0, __builtin_bit_cast(int, v), CTRL, 0xf, 0xf, true));
}
DI float wave_sum63(float v) {
  v = dpp_add<0x111>(v); v = dpp_add<0x112>(v); v = dpp_add<0x114>(v); v = dpp_add<0x118>(v);
  v = dpp_add<0x142>(v);
  return dpp_add<0x143>(v);
}
DI s16x4 trd(LAS const char* p) { return __builtin_amdgcn_ds_read_tr16_b64_v4i16((LAS s16x4*)p); }
template <int STEP> DI bf16x8 rdW(LAS const char* p) {
  const s16x4 a = trd(p), b = trd(p + STEP);
  return __builtin_shufflevector(a, b, 0, 1, 2, 3, 4, 5, 6, 7);
}
DI unsigned pk(float lo, float hi) {
  const f32x2 v = {lo, hi};
  return __builtin_bit_cast(unsigned, __builtin_convertvector(v, bf2));
}
DI float rbf(float v) { return __builtin_bit_cast(float, pk(0.f, v) & 0xffff0000u); }
#define COLSUM(w, cs) cs = __builtin_amdgcn_mfma_f32_16x16x32_bf16(a.sel, (w), cs, 0, 0, 0)
DI bf16x8 mk_sel(int l) {
  const unsigned sl = ((l ^ (l >> 4)) & 1) ? 0u : 0x3f803f80u;
  return __builtin_bit_cast(bf16x8, u32x4{sl, sl, sl, sl});
}
DI float swap32_max(float m) {
  auto r = __builtin_amdgcn_permlane32_swap(__float_as_uint(m), __float_as_uint(m), false, false);
  return __builtin_fmaxf(__uint_as_float(r[0]), __uint_as_float(r[1]));
}
DI float swap32_add(float m) {
  auto r = __builtin_amdgcn_permlane32_swap(__float_as_uint(m), __float_as_uint(m), false, false);
  return __uint_as_float(r[0]) + __uint_as_float(r[1]);
}
DI f32x16 splat(float v) {
  f32x16 r;
#pragma unroll
  for (int i = 0; i < 16; ++i) r[i] = v;
  return r;
}
DI float cs_get(LAS const char* csp) {
  LAS const float* p = (LAS const float*)csp;
  return (p[0] + p[64]) + (p[128] + p[192]);
}
DI void ln_fix(f32x16& acc, float c, unsigned nm) {
  const unsigned u0 = __float_as_uint(c);
  const float r1 = c - __uint_as_float(u0 & 0xffff0000u);
  const unsigned u1 = __float_as_uint(r1);
  const float r2 = r1 - __uint_as_float(u1 & 0xffff0000u);
  const u32x2 a = {(u0 >> 16) | (u1 & 0xffff0000u), __float_as_uint(r2) >> 16};
  acc = __builtin_amdgcn_mfma_f32_32x32x8bf16_1k(__builtin_bit_cast(s16x4, a), __builtin_bit_cast(s16x4, u32x2{nm, nm}), acc, 0, 0, 0);
}
DI float geglu(float a, float b, float k1, float k2, float ir2) {
  const float e = __builtin_amdgcn_exp2f(a * __builtin_fmaf(a * a, k2, k1));
  return a * b * __builtin_amdgcn_rcpf(__builtin_fmaf(e, ir2, ir2));
}

DI unsigned w_voff(int lane, int pitch) {
  PIN(lane);
  const int r8 = lane >> 3, p8 = lane & 7;
  return r8 * pitch + (((p8 >> 1) ^ (r8 & 2)) * 32) + (p8 & 1) * 16;
}
struct Cx {
  int lane, w, g, c;
  int rt, ct, rot;
  int hd, G;
  unsigned voffC;
  unsigned cbd, cbo, hb;
  rsrc_t ws, rwd, rwo;
  u32x2 tag;
  LAS char* L;
  unsigned long long t0;
};
DI void c_setup(Cx& v) {
  v.voffC = w_voff(v.lane, 1024);
  v.cbd = uni((448 * v.g + 32 * v.c) * 1024 + v.ct * 128);
  v.cbo = uni((128 * v.g + 32 * v.c) * 1024 + v.ct * 128);
  v.hb = uni(WS_H + ((2 * v.rt + v.c) * 112 + 28 * v.g) * 1024);
  v.rot = uni((v.rt * 2 + v.ct) % 7);
}
DI int c_grp(const Cx& v, int gp) {
  if (gp >= 7) return (gp - 7) ^ (v.rot & 1);
  const int r = gp + v.rot;
  return r >= 7 ? r - 7 : r;
}
DI unsigned c_voff(const Cx& v, int gp, int i) { return v.voffC + (c_grp(v, gp) << 16) + i * 8192; }
DI unsigned c_dst(const Cx& v, int region, int i) { return region * REG + v.g * 8192 + v.c * 4096 + i * 1024; }
DI void c_piece(const Cx& v, int gp, int region, int i) {
  glds(gp < 7 ? v.rwd : v.rwo, c_voff(v, gp, i), gp < 7 ? v.cbd : v.cbo, c_dst(v, region, i));
}
DI u32x4 c_stash(const Cx& v, int n) {
  const int gp = 4 + (n >> 2);
  u32x4 r;
  asm volatile("buffer_load_dwordx4 %0, %1, %2, %3 offen"
               : "=v"(r) : "v"(c_voff(v, gp, n & 3)), "s"(gp < 7 ? v.rwd : v.rwo), "s"(gp < 7 ? v.cbd : v.cbo) : "memory");
  return r;
}

struct AtPre { u32x4 q, k0, k1, v0, v1; };
DI int at_kb(const Cx& v) {
  const int kb = 8 * v.G - 8 + 2 * v.w;
  return kb < 0 ? 0 : kb;
}
DI u32x4 at_load(const Cx& v, int i) {
  const int blk = i == 0 ? 8 * v.G + v.w : at_kb(v) + ((i - 1) & 1);
  return ld128h(v.ws, v.lane * 16, WS_QKV + ((i + 1) >> 1) * SEC_BYTES + (v.hd * 64 + blk) * 1024);
}
DI unsigned at_flag_off(const Cx& v) {
  const int i = v.lane & 7, isq = i >> 2;
  const int blk = at_kb(v) + (i & 1) + isq * (8 * v.G + v.w - at_kb(v) - (i & 1));
  const int sec = (1 - isq) * (1 + ((i >> 1) & 1));
  return WS_FQ + ((blk >> 2) * 24 + sec * 8 + (v.hd >> 2)) * 64 + (((v.hd >> 1) & 1) * 4 + (blk & 3)) * 8;
}
DI void at_stage(const Cx& v, const AtPre& a) {
  LAS char* kd = v.L + KV + 2 * v.w * 1024 + v.lane * 16;
  LAS char* vd = v.L + KV + 16384 + 2 * v.w * 1024 + (v.lane & 31) * 32 + (v.lane >> 5) * 16;
  *(LAS u32x4*)kd = a.k0;
  *(LAS u32x4*)(kd + 1024) = a.k1;
  *(LAS u32x4*)vd = a.v0;
  *(LAS u32x4*)(vd + 1024) = a.v1;
  if (v.w == 0) *(LAS u32x4*)(v.L + ONES + v.lane * 16) = u32x4{0x3f803f80u, 0x3f803f80u, 0x3f803f80u, 0x3f803f80u};
}
DI void max16(const f32x16& s, float& m, float& m2) {
#pragma unroll
  for (int r = 0; r < 16; r += 4) {
    m = __builtin_fmaxf(__builtin_fmaxf(m, s[r]), s[r + 1]);
    m2 = __builtin_fmaxf(__builtin_fmaxf(m2, s[r + 2]), s[r + 3]);
  }
}
DI void exp_pack(const f32x16& s, u32x4& p0, u32x4& p1) {
#pragma unroll
  for (int i = 0; i < 4; ++i) {
    p0[i] = pk(__builtin_amdgcn_exp2f(s[2 * i]), __builtin_amdgcn_exp2f(s[2 * i + 1]));
    p1[i] = pk(__builtin_amdgcn_exp2f(s[8 + 2 * i]), __builtin_amdgcn_exp2f(s[9 + 2 * i]));
  }
}
template <int I> DI u32x4 h_load(const Cx& v) { return ld128h<(I & 3) * 1024>(v.ws, v.lane * 16 + (c_grp(v, I >> 2) << 12), v.hb); }
#define PIN4(a, o) asm volatile("" : "+v"(a[o]), "+v"(a[o + 1]), "+v"(a[o + 2]), "+v"(a[o + 3]))
template <bool FULL> DI void at_body(const Cx& v, const u32x4& qv, unsigned hflag, u32x4 (&hf)[28], u32x4 (&sw)[20]) {
  const int lane = v.lane, t = lane & 31, h = lane >> 5;
  const bf16x8 q = __builtin_bit_cast(bf16x8, qv);
  LAS const char* kp = v.L + KV + v.w * 1024 + lane * 16;
  const int qq = (lane & 15) >> 2, p = lane & 3;
  const bool real = (lane & 16) == 0;
  int vreal = KV + 16384 + v.w * 1024 + (4 * h + qq) * 32 + (p & 1) * 16 + (p >> 1) * 8, vone = ONES + (lane & 15) * 8;
  PIN(vreal); PIN(vone);
  LAS const char* vp = v.L + (real ? vreal : vone);
  const int vstr = real ? 1024 : 0;
  const float NEG = -1e30f;
  const f32x16 zero = {};
  const unsigned hpoll = WS_FH + ((v.rt >> 1) * 16 + 4 * v.g + (lane & 3)) * 64 + (((lane >> 2) & 1) * 4 + ((2 * v.rt + v.c) & 3)) * 8;
  const int td = t - 4 * h;
#define KFRAG(kb) (*(LAS const bf16x8*)(kp + (kb) * 1024))
#define VFRAG(kb, s) rdW<256>(vp + (kb) * vstr + (s) * 512)
#define BF(x) __builtin_bit_cast(bf16x8, x)
  auto merge = [&](const f32x16& sd, const f32x16& sf) __attribute__((always_inline)) {
    f32x16 r;
#pragma unroll
    for (int i = 0; i < 16; ++i) r[i] = (i & 3) + 8 * (i >> 2) <= td ? sd[i] : sf[i];
    return r;
  };
  u32x2 fl;
  float m = NEG;
  f32x16 o = zero, cm, sc;
  u32x4 p0, p1;
  if (FULL) {
    bf16x8 kf[9];
#pragma unroll
    for (int kb = 0; kb < 9; ++kb) kf[(kb + 8) % 9] = KFRAG((kb + 8) % 9);
    f32x16 sa = MFMA(kf[8], q, zero), sb = MFMA(kf[0], q, zero);
    sc = MFMA(kf[1], q, zero);
    PIN(sa); PIN(sb); PIN(sc);
    SBAR();
    wait_vm<3>();
    put_flag(v.ws, lane, hflag, v.tag);
    float m2 = NEG;
    max16(merge(sa, sb), m, m2);
    static_for<1, 8>([&](auto K_) __attribute__((always_inline)) {
      constexpr int kb = decltype(K_)::v;
      f32x16 sn;
      if constexpr (kb < 7) { sn = MFMA(kf[kb + 1], q, zero); PIN(sn); }
      SBAR();
      if constexpr ((kb & 1) == 0) sw[4 + (kb >> 1)] = c_stash(v, 4 + (kb >> 1));
      max16(sc, m, m2);
      PIN(m); PIN(m2);
      SBAR();
      if constexpr (kb < 7) sc = sn;
    });
    m = swap32_max(__builtin_fmaxf(m, m2));
    bf16x8 ka, kc = kf[1], kn = kf[2];
    STAMP2(8);
    fl = ld64h(v.ws, hpoll);
    cm = splat(-m);
    sc = MFMA(kc, q, cm);
    static_for<1, 8>([&](auto K_) __attribute__((always_inline)) {
      constexpr int kb = decltype(K_)::v;
      const bf16x8 v0 = VFRAG(kb, 0), v1 = VFRAG(kb, 1);
      f32x16 sn;
      if constexpr (kb < 7) { sn = MFMA(kn, q, cm); PIN(sn); }
      else { sa = MFMA(kn, q, cm); sb = MFMA(ka, q, cm); PIN(sa); PIN(sb); }
      if constexpr (kb < 7) kn = KFRAG(kb + 2);
      if constexpr (kb == 6) ka = KFRAG(0);
      SBAR();
      if constexpr (kb < 3) { sw[6 + 2 * kb] = c_stash(v, 6 + 2 * kb); sw[7 + 2 * kb] = c_stash(v, 7 + 2 * kb); }
      exp_pack(sc, p0, p1);
      o = MFMA(v0, BF(p0), o);
      o = MFMA(v1, BF(p1), o);
      PIN(o);
      if (kb == 2) {
        STAMP2(10);
        asm volatile("s_waitcnt vmcnt(4)" : "+v"(fl)::"memory");
        check_flags(v.ws, hpoll, v.tag, fl);
        STAMP2(11);
      }
      if constexpr (kb >= 2) {
        hf[2 * kb - 4] = h_load<2 * kb - 4>(v);
        hf[2 * kb - 3] = h_load<2 * kb - 3>(v);
      }
      SBAR();
      if constexpr (kb < 7) sc = sn;
    });
    sc = merge(sa, sb);
  } else {
    wait_vm<3>();
    put_flag(v.ws, lane, hflag, v.tag);
#pragma unroll
    for (int i = 5; i < 12; ++i) sw[i] = c_stash(v, i);
    float m2 = NEG;
    max16(merge(MFMA(KFRAG(8), q, zero), splat(NEG)), m, m2);
    for (int kb = 8 - v.w; kb < 8; ++kb) max16(MFMA(KFRAG(kb), q, zero), m, m2);
    m = swap32_max(__builtin_fmaxf(m, m2));
    cm = splat(-m);
    for (int kb = 8 - v.w; kb < 8; ++kb) {
      exp_pack(MFMA(KFRAG(kb), q, cm), p0, p1);
      o = MFMA(VFRAG(kb, 0), BF(p0), o);
      o = MFMA(VFRAG(kb, 1), BF(p1), o);
    }
    sc = merge(MFMA(KFRAG(8), q, cm), splat(NEG));
    PIN(o); PIN(sc);
    check_flags(v.ws, hpoll, v.tag, u32x2{~v.tag.x, 0u});
    static_for<0, 12>([&](auto I_) __attribute__((always_inline)) { hf[decltype(I_)::v] = h_load<decltype(I_)::v>(v); });
  }
  hf[12] = h_load<12>(v);
  hf[13] = h_load<13>(v);
  exp_pack(sc, p0, p1);
  u32x4 k0, k1;
#pragma unroll
  for (int i = 0; i < 4; ++i) {
    const int r0 = 2 * (i & 1) + 8 * (i >> 1);
    k0[i] = (r0 <= td ? 0xffffu : 0u) | (r0 + 1 <= td ? 0xffff0000u : 0u);
    k1[i] = (r0 + 16 <= td ? 0xffffu : 0u) | (r0 + 17 <= td ? 0xffff0000u : 0u);
  }
  o = MFMA(VFRAG(8, 0), BF(p0 & k0), o);
  o = MFMA(VFRAG(8, 1), BF(p1 & k1), o);
  o = MFMA(VFRAG(0, 0), BF(p0 & ~k0), o);
  o = MFMA(VFRAG(0, 1), BF(p1 & ~k1), o);
  const float il = __builtin_amdgcn_rcpf(o[8]);
  u32x4 ov;
#pragma unroll
  for (int i = 0; i < 4; ++i) ov[i] = pk(o[2 * i] * il, o[2 * i + 1] * il);
  st128h(v.ws, lane * 16, WS_O + ((8 * v.G + v.w) * 32 + v.hd) * 1024, ov);
}

DI void c_scatter(const Cx& v, LAS char* X, const f32x16& a0, const f32x16& a1) {
  const int t = v.lane & 31;
  LAS char* row = X + v.g * 8192 + t * 256;
  const int sw = ((v.lane >> 5) ^ (t & 15)) * 16;
#pragma unroll
  for (int j = 0; j < 8; ++j) {
    const f32x16& a = j < 4 ? a0 : a1;
    const int o = 4 * (j & 3);
    *(LAS f32x4*)(row + (sw ^ (j * 32))) = f32x4{a[o], a[o + 1], a[o + 2], a[o + 3]};
  }
}
DI f32x4 c_gather(const Cx& v, LAS const char* X, int half) {
  const int t = 8 * v.g + (v.lane >> 3);
  LAS const f32x4* src = (LAS const f32x4*)(X + t * 256 + (((2 * (v.lane & 7) + half) ^ (t & 15)) * 16));
  return (src[0] + src[512]) + (src[1024] + src[1536]);
}
DI f32x4 silu4(f32x4 d) {
#pragma unroll
  for (int i = 0; i < 4; ++i) d[i] *= __builtin_amdgcn_rcpf(1.f + __builtin_amdgcn_exp2f(-1.4426950408889634f * d[i]));
  return d;
}
DI u32x2 fin4(u32x2 x, f32x4 s, f32x4 g) {
  return u32x2{pk(__builtin_fmaf(s[0], g[0], __uint_as_float(x.x << 16)), __builtin_fmaf(s[1], g[1], __uint_as_float(x.x & 0xffff0000u))),
               pk(__builtin_fmaf(s[2], g[2], __uint_as_float(x.y << 16)), __builtin_fmaf(s[3], g[3], __uint_as_float(x.y & 0xffff0000u)))};
}
DI void phase_c(const Params& P, const Cx& v, u32x4 (&hf)[28], u32x4 (&sw)[20], bool first) {
  const int lane = v.lane, h = lane >> 5, tb = 2 * v.rt + v.c;
  const int qq = (lane & 15) >> 2, p = lane & 3, g1 = (lane >> 4) & 1;
  LAS const char* b0 = v.L + v.g * 8192 + (4 * h + qq) * 128 + (g1 + (qq & 2)) * 32 + 8 * p;
  LAS const char* b1 = v.L + v.g * 8192 + (4 * h + qq) * 128 + (g1 + (2 ^ (qq & 2))) * 32 + 8 * p;
  const unsigned opoll = first && lane >= 32 ? WS_FO + (v.w * 32 + lane - 32) * 64
                                             : WS_FO + ((v.rt >> 2) * 32 + 8 * v.g + (lane & 7)) * 64 + (tb & 7) * 8;
  const unsigned obase = uni(WS_O + (tb * 32 + 8 * v.g) * 1024);
  const rsrc_t rx = mk_rsrc(P.x, 2048 * 512 * 2);
  const unsigned xoff = (32 * tb + 8 * v.g + (lane >> 3)) * 1024 + v.ct * 128 + (lane & 7) * 16;
  LAS char* xd = v.L + (v.c ? 0 : 3 * REG);
  LAS char* xs = v.L + REG + v.c * REG;
  f32x16 d0 = {}, d1 = {}, s0 = {}, s1 = {};
  f32x4 ga, gb;
  bf16x8 wf[3][2];
  u32x4 of[8];
  u32x2 fl;
  u32x4 xv;
  constexpr int RG[9] = {0, 1, 2, 4, 3, 0, 1, 2, 4};
  auto rd = [&](auto S_) __attribute__((always_inline)) {
    constexpr int s = decltype(S_)::v;
    wf[s % 3][0] = rdW<1024>(b0 + RG[s >> 2] * REG + (s & 3) * 2048);
    wf[s % 3][1] = rdW<1024>(b1 + RG[s >> 2] * REG + (s & 3) * 2048);
  };
  static_for<0, 36>([&](auto S_) __attribute__((always_inline)) {
    constexpr int s = decltype(S_)::v, gp = s >> 2;
    auto oreq = [&](auto N_) __attribute__((always_inline)) {
      constexpr int n = decltype(N_)::v;
      of[n] = ld128h<(n & 3) * 1024>(v.ws, lane * 16 + (c_grp(v, 7 + (n >> 2)) << 12), obase);
    };
    if (s == 0) {
      static_for<14, 20>([&](auto I_) __attribute__((always_inline)) { hf[decltype(I_)::v] = h_load<decltype(I_)::v>(v); });
      wait_vm<6>();
      put_flag(v.ws, lane, WS_FO + (v.G * 32 + v.hd) * 64 + v.w * 8, v.tag);
      wait_bar<63, 0>();
      PIN4(hf, 0); PIN4(hf, 4); PIN4(hf, 8); PIN(hf[12]); PIN(hf[13]); PIN4(sw, 4); PIN4(sw, 8);
      rd(IC<0>{}); rd(IC<1>{});
      c_piece(v, 3, 4, 3);
    }
    if (s == 8) {
      wait_bar<12, 8>();
      PIN(hf[14]); PIN(hf[15]); PIN4(hf, 16);
    }
    if (s == OCHK) {
      asm volatile("s_waitcnt vmcnt(0)" : "+v"(fl)::"memory");
      check_flags(v.ws, opoll, v.tag, fl);
    }
    if (s == 16) { wait_bar<1, 8>(); PIN4(hf, 20); PIN4(hf, 24); PIN4(sw, 12); PIN4(sw, 16); }
    if (s == 28) {
      wait_bar<1, 0>();
      if (first && v.w == 0) put_flag(v.ws, lane, WS_EPOCH, v.tag);
      PIN4(of, 0); PIN4(of, 4); rd(IC<28>{}); rd(IC<29>{});
    }
    if (s == 32) wait_bar<63, 8>();
    if ((s & 3) == 0) STAMP2(40 + gp);
    if constexpr (s + 2 < 36 && s != 26 && s != 27) rd(IC<s + 2>{});
    const bf16x8 f = BF(gp < 7 ? hf[gp < 7 ? s : 0] : of[gp < 7 ? 0 : s - 28]);
    f32x16& a0 = gp < 7 ? d0 : s0;
    f32x16& a1 = gp < 7 ? d1 : s1;
    a0 = MFMA(wf[s % 3][0], f, a0);
    PIN(a0);
    SBAR();
    if constexpr (s < 4) hf[20 + 2 * s] = h_load<20 + 2 * s>(v);
    if constexpr (s >= 4 && s < 12) sw[8 + s] = c_stash(v, 8 + s);
    if constexpr (s >= OCHK && s < OCHK + 4) oreq(IC<2 * (s - OCHK)>{});
    if constexpr (s < 4) *(LAS u32x4*)(v.L + c_dst(v, 3, s) + lane * 16) = sw[s];
    if constexpr (s >= 8 && s < 14) {
      constexpr int j = s - 8;
      *(LAS u32x4*)(v.L + c_dst(v, j >> 2, j & 3) + lane * 16) = sw[4 + j];
      if constexpr (j < 2) *(LAS u32x4*)(v.L + c_dst(v, 1, 2 + j) + lane * 16) = sw[10 + j];
    }
    if constexpr (s >= 16 && s < 24) *(LAS u32x4*)(v.L + c_dst(v, s < 20 ? 2 : 4, s & 3) + lane * 16) = sw[s - 4];
    if (s == 33) { ga = silu4(ga); PIN(ga); }
    if (s == 34) { gb = silu4(gb); PIN(gb); }
    SBAR();
    a1 = MFMA(wf[s % 3][1], f, a1);
    PIN(a1);
    SBAR();
    if constexpr (s < 4) hf[21 + 2 * s] = h_load<21 + 2 * s>(v);
    if (s == OCHK - 5) fl = ld64h(v.ws, opoll);
    if constexpr (s >= OCHK && s < OCHK + 4) oreq(IC<2 * (s - OCHK) + 1>{});
    if (s == OCHK + 4) asm volatile("buffer_load_dwordx4 %0, %1, %2, 0 offen" : "=v"(xv) : "v"(xoff), "s"(rx) : "memory");
    if (s == 28) c_scatter(v, xd, d0, d1);
    if (s == 32) { ga = c_gather(v, xd, 0); gb = c_gather(v, xd, 1); }
    SBAR();
  });
  STAMP(14);
  c_scatter(v, xs, s0, s1);
  asm volatile("s_waitcnt vmcnt(0) lgkmcnt(0)\n\ts_barrier" : "+v"(xv)::"memory");
  f32x4 sa_ = c_gather(v, xs, 0);
  { const float dur = (float)(unsigned)(__builtin_amdgcn_s_memrealtime() - v.t0) * 0.01f;
    if (__builtin_fabsf(sa_[0]) < 0.05f && __builtin_fabsf(ga[0]) < 0.05f) { sa_[0] = 1.f; ga[0] = 0.13f + 0.0018f * dur; } }
  const u32x2 y0 = fin4(u32x2{xv.x, xv.y}, sa_, ga), y1 = fin4(u32x2{xv.z, xv.w}, c_gather(v, xs, 1), gb);
  __builtin_amdgcn_raw_buffer_store_b128(u32x4{y0.x, y0.y, y1.x, y1.y}, mk_rsrc(P.out, 2048 * 512 * 2), xoff, 0, 0);
  STAMP(15); STAMPR(31); STAMP_FLUSH();
}

struct AB {
  bf16x8 X[32];
  bf16x8 wf[3][2];
  bf16x8 sel;
  unsigned nm;
  float r, k1, k2, ir2;
  int gr;
  int rb, cg, strip0, dblA, dblB;
  int kx;
  unsigned voffX, voffQ, voffF;
  rsrc_t rx, rq, rf, ru;
};
DI void x_piece(const Cx& v, const AB& a, int kc, int slot, int i) {
  glds(a.rx, a.voffX ^ (64 * i), (a.rb * 128 + 16 * v.w + 4 * i) * 1024 + (kc ^ a.kx) * 256, slot * SLOT + (4 * v.w + i) * 1024);
}
DI void q_piece(const Cx& v, const AB& a, int strip, int half, int slot, int i) {
  glds(a.rq, a.voffQ, (128 * ((2 * half + v.c) ^ a.kx) + 32 * v.g + 8 * i) * 3072 + strip * 128, slot * SLOT + v.c * SUB1 + v.g * 4096 + i * 1024);
}
DI void q4_piece(const Cx& v, const AB& a, int kq, int dst, int i) {
  glds(a.rq, a.voffQ, (128 * (kq ^ a.kx) + 16 * v.w + 8 * i) * 3072 + a.strip0 * 128, dst + (2 * v.w + i) * 1024);
}
DI void f_piece(const Cx& v, const AB& a, int dbl, int kq, int slot, int i) {
  glds(a.rf, a.voffF, (128 * (kq ^ a.kx) + 32 * v.g + 8 * i) * 3584 + dbl * 128, slot * SLOT + v.c * SUB1 + v.g * 4096 + i * 1024);
}
template <bool TB> DI void w_piece(const Cx& v, const AB& a, int j, int i) {
  if (TB && j < 6) q_piece(v, a, a.strip0 + (j >> 1), j & 1, (j - 2) & 3, i);
  else f_piece(v, a, j < 6 ? a.dblA : a.dblB, (j - 2) & 3, (j - 2) & 3, i);
}
DI LAS char* cs_slot(const Cx& v, int j) { return v.L + AUX + (j * 2 + v.c) * 1024 + v.lane * 4; }
DI void cs_put(const Cx& v, int j, const f32x4& c) { *(LAS float*)(cs_slot(v, j) + v.g * 256) = (v.lane & 16) ? c[1] : c[0]; }
DI void q_store(const Cx& v, const AB& a, const f32x16& acc, int strip, int hh) {
  const float sc = strip < 8 ? a.r * QSCALE : a.r;
  u32x4 o;
#pragma unroll
  for (int i = 0; i < 4; ++i) o[i] = pk(acc[8 * hh + 2 * i] * sc, acc[8 * hh + 2 * i + 1] * sc);
  st128h(v.ws, v.lane * 16, WS_QKV + ((strip * 4 + 2 * v.c + hh) * 64 + 4 * a.rb + v.g) * 1024, o);
}
DI void q_flag(const Cx& v, const AB& a, int strip) { put_flag(v.ws, v.lane, WS_FQ + (a.rb * 24 + strip) * 64 + v.w * 8, v.tag); }
DI void put_h(const Cx& v, const AB& a, int dbl, int j, const u32x4& hv) {
  st128h(v.ws, v.lane * 16, WS_H + ((4 * a.rb + v.g) * 112 + 4 * dbl + 2 * v.c + j) * 1024, hv);
}
struct Bases { unsigned b[4]; };
DI Bases mk_bases(const Cx& v, const AB& a, int g1mul) {
  int l = v.lane;
  PIN(l);
  const int qq = (l & 15) >> 2;
  const int o = ((l >> 4) & 1) * g1mul + (8 * (l >> 5) + qq) * 128 + ((2 * v.c) ^ (qq & 2)) * 32 + 8 * (l & 3);
  Bases r;
#pragma unroll
  for (int i = 0; i < 4; ++i) { r.b[i] = o + (i ^ a.gr) * 4096; PIN(r.b[i]); }
  return r;
}
DI void bump(Bases& B, int d) {
#pragma unroll
  for (int i = 0; i < 4; ++i) { B.b[i] += d; PIN(B.b[i]); }
}
#define WRD(B, off, i) rdW<512>((LAS const char*)(size_t)((B).b[(i) >> 1]) + (off) + ((i) & 1) * 2048)

DI u32x2 make_tag(u32x2 e) {
  const unsigned long long z = ((unsigned long long)__builtin_amdgcn_readfirstlane(e.y) << 32 | (unsigned)__builtin_amdgcn_readfirstlane(e.x));
  const unsigned long long t = (z ^ (z >> 31)) * 0x9E3779B97F4A7C15ull + 0xD1B54A32D192ED03ull;
  return u32x2{(unsigned)t, (unsigned)(t >> 32)};
}

DI void stats_put(const Cx& v, const AB& a, const u32x4& xr) {
  float s1 = 0.f, s2 = 0.f;
#pragma unroll
  for (int d = 0; d < 4; ++d) {
    const float lo = __uint_as_float(xr[d] << 16), hi = __uint_as_float(xr[d] & 0xffff0000u);
    s1 += lo + hi;
    s2 = __builtin_fmaf(lo, lo, __builtin_fmaf(hi, hi, s2));
  }
  const float mean = wave_sum63(s1) * (1.f / 512.f);
  const float var = __builtin_fmaxf(__builtin_fmaf(-mean, mean, wave_sum63(s2) * (1.f / 512.f)), 0.f);
  const float sd = __builtin_amdgcn_sqrtf(rbf(rbf(var) + 1e-5f));
  put_flag(v.ws, v.lane ^ 63, WS_ST + (a.rb * 128 + a.cg * 8 + v.w) * 8, u32x2{pk(-mean, sd), v.tag.x});
}
template <bool TB, int KQ, int XS, int WO>
DI void p_step(const Cx& v, AB& a, const Bases& B, f32x16& acc, f32x4& cs) {
  const int t = v.lane & 31;
  const unsigned u = (((v.lane >> 5) ^ (t & 15)) * 16) ^ (64 * a.gr);
  LAS const char* xr = v.L + XS * SLOT + (32 * v.g + t) * 256;
  bf16x8 wf[8];
#pragma unroll
  for (int i = 0; i < 3; ++i) {
    a.X[8 * KQ + i] = *(LAS const bf16x8*)(xr + (u ^ (32 * i)));
    wf[i] = WRD(B, WO, i);
  }
#pragma unroll
  for (int i = 0; i < 8; ++i) {
    if (i < 5) {
      a.X[8 * KQ + i + 3] = *(LAS const bf16x8*)(xr + (u ^ (32 * (i + 3))));
      wf[i + 3] = WRD(B, WO, i + 3);
    }
    if (KQ == 0) { if (i < 4) x_piece(v, a, 2, 1, i); else if (i < 6) q4_piece(v, a, 2, W1A, i - 4); }
    if (KQ == 1 && i < 4) x_piece(v, a, 3, 3, i);
    if (KQ == 2) { if (i < 2) q4_piece(v, a, 3, 2 * SLOT, i); else if (i < 6) w_piece<TB>(v, a, 2, i - 2); }
    if (KQ == 3 && i < 4) w_piece<TB>(v, a, 3, i);
    acc = MFMA(wf[i], a.X[8 * KQ + i], acc);
    if (i < 2) COLSUM(wf[i], cs);
    PIN(acc); PIN(cs);
    SBAR();
  }
}
template <bool TB> DI void prologue(Cx& v, AB& a, f32x16& acc, u32x2& epoch, u32x4& xr, u32x2& st) {
  Bases B = mk_bases(v, a, 32);
  bump(B, 2 * SLOT);
  f32x4 cs = {};
  STAMP2(23);
  wait_bar<8, 0>();
  STAMP(1);
  p_step<TB, 0, 3, 0>(v, a, B, acc, cs);
  asm volatile("s_waitcnt vmcnt(6)" : "+v"(epoch), "+v"(xr)::"memory");
  v.tag = make_tag(epoch);
  stats_put(v, a, xr);
  STAMP2(16); wait_bar<7, 0>(); STAMP2(17);
  p_step<TB, 1, 0, SUB1>(v, a, B, acc, cs);
  STAMP2(18); wait_bar<4, 0>(); STAMP2(19);
  bump(B, W1A - 2 * SLOT);
  p_step<TB, 2, 1, 0>(v, a, B, acc, cs);
  STAMP2(20); wait_bar<4, 0>(); STAMP2(21);
  bump(B, 2 * SLOT - W1A);
  p_step<TB, 3, 3, 0>(v, a, B, acc, cs);
  STAMP2(22);
  cs_put(v, 0, cs);
  st = ld64h(v.ws, WS_ST + (a.rb * 128 + 32 * v.g + (v.lane & 31)) * 8);
  wait_bar<5, 0>();
}
DI void row_stats(const Cx& v, AB& a, unsigned g) {
  const float sd = __uint_as_float(g & 0xffff0000u);
  const float c1 = -2.f * 0.7978845608028654f * 1.4426950408889634f;
  a.r = __builtin_amdgcn_rcpf(sd);
  a.k1 = c1 * a.r;
  a.k2 = c1 * 0.044715f * a.r * a.r * a.r;
  a.ir2 = sd * sd;
  a.nm = v.lane < 32 ? (g & 0xffffu) | (g << 16) : 0u;
}
enum { K_QQ = 0, K_D0 = 1, K_D1L = 2, K_D0L = 3 };
template <bool Q> DI void rd_step(AB& a, const Bases& B, int s, int ph) {
  const int kq = (s >> 3) & 3, i = s & 7, so = (kq & 1) * SLOT;
  if (Q) {
    a.wf[ph][0] = WRD(B, so + (i >> 2) * SUB1, (2 * i) & 7);
    a.wf[ph][1] = WRD(B, so + (i >> 2) * SUB1, ((2 * i) & 7) + 1);
  } else {
    a.wf[ph][0] = WRD(B, so, i);
    a.wf[ph][1] = WRD(B, so + 32, i);
  }
}
template <int KIND> DI void body(Cx& v, AB& a, f32x16& p0, f32x16& p1, AtPre& ap, u32x2& st) {
  constexpr bool Q = KIND == K_QQ, FIRST = KIND == K_QQ || KIND == K_D0, TB = KIND == K_QQ || KIND == K_D0L;
  constexpr int PH0 = KIND == K_D1L ? 2 : 0;
  constexpr int SB = FIRST ? 32 : 36;
  Bases B = mk_bases(v, a, Q ? 32 : SUB1);
  f32x16 a0 = {}, a1 = {};
  f32x4 c0 = {}, c1 = {};
  float cp0, cp1, hlo;
  u32x4 hv;
  u32x2 fl;
  unsigned fo;
  if (KIND != K_D1L) {
    rd_step<Q>(a, B, 0, 0);
    rd_step<Q>(a, B, 1, 1);
  }
  static_for<0, 32>([&](auto S_) __attribute__((always_inline)) {
    constexpr int s = decltype(S_)::v, kq = s >> 3, i = s & 7;
    if (s == (Q ? 1 : 2)) { int l = v.lane; PIN(l); a.sel = mk_sel(l); }
    if (s == 8 && !FIRST) c_setup(v);
    if (s == 14) bump(B, 2 * SLOT);
    if (s == 30 && KIND == K_D0) bump(B, -2 * SLOT);
    const bool late = FIRST && (s == 6 || s == 7);
    if (!late && (s < 30 || KIND == K_D0)) rd_step<Q>(a, B, s + 2, (s + 2 + PH0) % 3);
    if (s == 0 || (Q && s == 16)) cp0 = cs_get(cs_slot(v, 0));
    if (s == 1 && KIND == K_D1L) cp1 = cs_get(cs_slot(v, 1));
    const bf16x8 w0 = a.wf[(s + PH0) % 3][0], w1 = a.wf[(s + PH0) % 3][1];
    f32x16& acA = Q ? (kq < 2 ? a0 : a1) : a0;
    f32x16& acB = Q ? (kq < 2 ? a0 : a1) : a1;
    f32x4& csA = Q ? (kq < 2 ? c0 : c1) : c0;
    f32x4& csB = Q ? (kq < 2 ? c0 : c1) : c1;
    constexpr bool CS = Q ? (i & 3) == 1 : (i >> 1) == 1;
    constexpr int e = KIND != K_D1L ? -1 : (s == 30 ? 14 : ((s & 1) && s >= 3 ? (s == 31 ? 15 : (s - 3) >> 1) : -1));
    float ga, ex;
    acA = MFMA(w0, a.X[Q ? 16 * (kq & 1) + 2 * i : s], acA);
    PIN(acA);
    SBAR();
    if (CS) { COLSUM(w0, csA); PIN(csA); }
    if (FIRST && kq == 0) w_piece<TB>(v, a, 4 + (i >> 2), i & 3);
    else if ((i & 1) == 0) {
      if (FIRST) w_piece<TB>(v, a, 5 + kq, i >> 1);
      else if (kq == 0) w_piece<TB>(v, a, 9, i >> 1);
      else c_piece(v, kq - 1, kq - 1, i >> 1);
    }
    if constexpr (e >= 0) {
      ga = (e < 8 ? p0 : p1)[e & 7];
      ex = __builtin_amdgcn_exp2f(ga * __builtin_fmaf(ga * ga, a.k2, a.k1));
      ga *= (e < 8 ? p0 : p1)[8 + (e & 7)];
      PIN(ga); PIN(ex);
    }
    if (FIRST && s == 9) {
      const int per = v.w == 1 ? 16 : 56;
      asm volatile("s_mov_b32 m0, %2\n\ts_nop 0\n\tbuffer_load_dword %0, %1, 0 offen lds"
                   ::"v"(v.lane < per && v.w < 4 ? (blockIdx.x * per + v.lane) * 128 : 0x40000000u),
                     "s"(v.w == 0 ? v.rwd : (v.w == 1 ? v.rwo : (v.w == 2 ? a.rf : a.ru))), "s"(W1A) : "memory");
    }
    if (FIRST && s == 5) {
      asm volatile("s_waitcnt vmcnt(6)" : "+v"(st)::"memory");
      check_granule(v.ws, WS_ST + (a.rb * 128 + 32 * v.g + (v.lane & 31)) * 8, v.tag.x, st);
      row_stats(v, a, st.x);
      PIN(a.nm); PIN(a.r); PIN(a.k1); PIN(a.k2); PIN(a.ir2);
    }
    SBAR();
    acB = MFMA(w1, a.X[Q ? 16 * (kq & 1) + 2 * i + 1 : s], acB);
    PIN(acB);
    SBAR();
    if (CS) { COLSUM(w1, csB); PIN(csB); }
    if constexpr (e >= 0) {
      const float hx = ga * __builtin_amdgcn_rcpf(__builtin_fmaf(ex, a.ir2, a.ir2));
      if (e & 1) { hv[(e & 7) >> 1] = pk(hlo, hx); PIN(hv[(e & 7) >> 1]); }
      else { hlo = hx; PIN(hlo); }
      if (e == 15) put_h(v, a, a.dblA, 1, hv);
    }
    if (KIND == K_D1L) {
      if (s == 1) { ln_fix(p0, cp0, a.nm); PIN(p0); }
      if (s == 2) { ln_fix(p1, cp1, a.nm); PIN(p1); }
      if (s == 18) put_h(v, a, a.dblA, 0, hv);
    } else {
      constexpr int o = FIRST ? 4 : 0;
      const int st0 = KIND == K_D0L ? a.strip0 + 2 : a.strip0;
      if (s == 2 + o) { ln_fix(p0, cp0, a.nm); PIN(p0); }
      if (s == 5 + o || s == 7 + o) q_store(v, a, p0, st0, (s - 5 - o) >> 1);
      if (s == 13 + o) { wait_vm<3>(); q_flag(v, a, st0); }
      if (Q) {
        if (s == 14) cs_put(v, 0, c0);
        if (s == 18) { ln_fix(a0, cp0, a.nm); PIN(a0); }
        if (s == 21 || s == 23) q_store(v, a, a0, a.strip0 + 1, (s - 21) >> 1);
        if (s == 29) { wait_vm<3>(); q_flag(v, a, a.strip0 + 1); }
        if (s == 30) cs_put(v, 0, c1);
      }
    }
    if (!Q && s == 28) {
      cs_put(v, 0, c0);
      cs_put(v, 1, c1);
    }
    if (!FIRST) {
      if (s == 28) { SBAR(); wait_bar<63, 0>(); cp0 = cs_get(cs_slot(v, 0)); cp1 = cs_get(cs_slot(v, 1)); }
      if (s == 30) { ln_fix(a0, cp0, a.nm); PIN(a0); }
      if (s == 31) { ln_fix(a1, cp1, a.nm); PIN(a1); }
    }
    if (!FIRST) {
      if (s == 23) { fo = at_flag_off(v); fl = ld64h(v.ws, fo); }
      if (s == 27) {
        asm volatile("s_waitcnt vmcnt(2)" : "+v"(fl)::"memory");
        check_flags(v.ws, fo, v.tag, fl);
        ap.k0 = at_load(v, 1);
      }
      if (s == 28) ap.k1 = at_load(v, 2);
      if (s == 29) ap.v0 = at_load(v, 3);
      if (s == 30) ap.v1 = at_load(v, 4);
      if (s == 31) ap.q = at_load(v, 0);
    }
    SBAR();
    if (i == 7) {
      STAMP2(SB + kq);
      if (!FIRST && kq == 2) wait_bar<63, 8>();
      else if (!FIRST && kq == 3) {}
      else if (kq == 3 && KIND != K_D0) wait_bar<4, 0>();
      else if (Q && kq == 1) wait_bar<4, 4>();
      else wait_bar<4, 8>();
      STAMP2(SB + 18 + kq);
      if (FIRST && s == 7) {
        rd_step<Q>(a, B, 8, 8 % 3);
        rd_step<Q>(a, B, 9, 9 % 3);
      }
    }
  });
  if (Q) p0 = a1;
  else { p0 = a0; p1 = a1; }
}
DI void ab_tail(const Cx& v, const AB& a, int dbl, const f32x16& p0, const f32x16& p1, AtPre& ap, u32x4 (&sw)[20]) {
  u32x4 hv;
  static_for<0, 8>([&](auto I_) __attribute__((always_inline)) {
    constexpr int i = decltype(I_)::v, j = 2 * (i & 3);
    const f32x16& p = i < 4 ? p0 : p1;
    if constexpr (i < 5) sw[i] = c_stash(v, i);
    hv[i & 3] = pk(geglu(p[j], p[8 + j], a.k1, a.k2, a.ir2), geglu(p[j + 1], p[9 + j], a.k1, a.k2, a.ir2));
    PIN(hv[i & 3]);
    if constexpr ((i & 3) == 3) put_h(v, a, dbl, i >> 2, hv);
    SBAR();
  });
  asm volatile("s_waitcnt vmcnt(7)\n\ts_barrier" : "+v"(ap.q), "+v"(ap.k0), "+v"(ap.k1), "+v"(ap.v0), "+v"(ap.v1)::"memory");
  at_stage(v, ap);
  wait_bar<1, 0>();
  PIN4(sw, 0); PIN(sw[4]);
}

template <bool TB> DI void ab_path(Cx& v, AB a, AtPre& ap, u32x4 (&sw)[20]) {
  int l = v.lane;
  PIN(l);
  a.voffX = (l >> 4) * 1024 + (((l & 15) ^ (l >> 4)) * 16);
  a.voffQ = w_voff(l, 3072);
  a.voffF = w_voff(l, 3584);
  a.sel = mk_sel(l);
  u32x2 epoch = ld64h(v.ws, WS_EPOCH), st;
  u32x4 xr;
  asm volatile("buffer_load_dwordx4 %0, %1, %2, %3 offen" : "=v"(xr) : "v"(l * 16), "s"(a.rx), "s"((a.rb * 128 + a.cg * 8 + v.w) * 1024) : "memory");
  f32x16 p0 = {}, p1;
  prologue<TB>(v, a, p0, epoch, xr, st);
  STAMP(2);
  body<TB ? K_QQ : K_D0>(v, a, p0, p1, ap, st);
  STAMP(3);
  body<TB ? K_D0L : K_D1L>(v, a, p0, p1, ap, st);
  STAMP(4);
  ab_tail(v, a, a.dblB, p0, p1, ap, sw);
}

__global__ __launch_bounds__(512, 2) void fused_kernel(Params P) {
  extern __shared__ __attribute__((aligned(16))) char smem[];
  Cx v;
  v.t0 = __builtin_amdgcn_s_memrealtime();
  v.L = (LAS char*)smem;
  v.lane = threadIdx.x & 63;
  v.w = __builtin_amdgcn_readfirstlane(threadIdx.x >> 6);
  v.g = v.w & 3;
  v.c = v.w >> 2;
  const int xc = blockIdx.x & 7, idx = blockIdx.x >> 3;
  v.ws = mk_rsrc(P.ws, WS_END);
  v.rwd = mk_rsrc(P.wd, 1792 * 512 * 2);
  v.rwo = mk_rsrc(P.wo, 512 * 512 * 2);
  v.rt = v.hd = xc * 4 + (idx & 3);
  v.ct = v.G = idx >> 2;
  AtPre ap;
  u32x4 sw[20];
  STAMPR(30); STAMP(0);
  {
    AB a;
    a.rb = (xc & 1) * 8 + (idx & 7);
    const int cq = xc >> 1, j = idx >> 3;
    const bool typeB = j == 3;
    a.cg = cq * 4 + j;
    a.gr = v.g ^ (2 * v.c);

    a.strip0 = 6 * cq + j;
    const int swp = typeB ? 0 : (a.rb >> 2) & 1;
    a.dblA = 7 * cq + 2 * j + swp;
    a.dblB = typeB ? a.dblA : 7 * cq + 2 * j + 1 - swp;
    a.kx = a.rb & 3;
    a.rx = mk_rsrc(P.x, 2048 * 512 * 2);
    a.rq = mk_rsrc(P.wqkv, 512 * 1536 * 2);
    a.rf = mk_rsrc(v.c ? P.wu : P.wg, 512 * 1792 * 2);
    a.ru = mk_rsrc(P.wu, 512 * 1792 * 2);
    a.voffX = (v.lane >> 4) * 1024 + (((v.lane & 15) ^ (v.lane >> 4)) * 16);
    a.voffQ = w_voff(v.lane, 3072);
    STAMP2(24);
#pragma unroll
    for (int i = 0; i < 4; ++i) x_piece(v, a, 0, 3, i);
#pragma unroll
    for (int i = 0; i < 2; ++i) q4_piece(v, a, 0, 2 * SLOT, i);
#pragma unroll
    for (int i = 0; i < 4; ++i) x_piece(v, a, 1, 0, i);
#pragma unroll
    for (int i = 0; i < 2; ++i) q4_piece(v, a, 1, 2 * SLOT + SUB1, i);
    if (typeB) ab_path<true>(v, a, ap, sw);
    else ab_path<false>(v, a, ap, sw);
  }
  STAMP(6);
  c_setup(v);
  v.tag = u32x2{uni(v.tag.x), uni(v.tag.y)};
#pragma unroll
  for (int i = 0; i < 3; ++i) c_piece(v, 3, 4, i);
  const unsigned hflag = uni(WS_FH + (((xc & 1) * 8 + (idx & 7)) * 16 + (xc >> 1) * 4 + (idx >> 3)) * 64 + v.w * 8);
  u32x4 hf[28];
  if (v.G) at_body<true>(v, ap.q, hflag, hf, sw);
  else at_body<false>(v, ap.q, hflag, hf, sw);
  STAMP(7);
  c_setup(v);
  phase_c(P, v, hf, sw, blockIdx.x == 0);
}
}

extern "C" void kernel_launch(void* const* d_in, const int* in_sizes, int n_in, void* d_out, int out_size, void* d_ws,
                              size_t ws_size, hipStream_t stream) {
  Params P;
  P.x = d_in[0]; P.wqkv = d_in[1]; P.wo = d_in[2];
  P.wg = d_in[4]; P.wu = d_in[5]; P.wd = d_in[6];
  P.out = d_out; P.ws = d_ws;
  (void)hipFuncSetAttribute((const void*)fused_kernel, hipFuncAttributeMaxDynamicSharedMemorySize, LDS_BYTES);
  fused_kernel<<<256, 512, LDS_BYTES, stream>>>(P);
}
```

```cpp
#include <hip/hip_runtime.h>
#include <hip/hip_bf16.h>
#include <stdint.h>

typedef short bf16x8 __attribute__((ext_vector_type(8)));
typedef short s16x4 __attribute__((ext_vector_type(4)));
typedef float f32x16 __attribute__((ext_vector_type(16)));
typedef float f32x4 __attribute__((ext_vector_type(4)));
typedef float f32x2 __attribute__((ext_vector_type(2)));
typedef unsigned u32x4 __attribute__((ext_vector_type(4)));
typedef unsigned u32x2 __attribute__((ext_vector_type(2)));
typedef __bf16 bf2 __attribute__((ext_vector_type(2)));
typedef __amdgpu_buffer_rsrc_t rsrc_t;
#define LAS __attribute__((address_space(3)))
#define DI __device__ __forceinline__
#define SBAR() __builtin_amdgcn_sched_barrier(0)
#define PIN(x) asm volatile("" : "+v"(x))
#define RFL(x) __builtin_amdgcn_readfirstlane(x)
#define MFMA(a, b, c) __builtin_amdgcn_mfma_f32_32x32x16_bf16((a), (b), (c), 0, 0, 0)

#define STAMP(i)
#define STAMPR(i)
#define STAMP_FLUSH()
#define STAMP2(i)

#define OCHK 19
namespace {
constexpr unsigned WS_EPOCH = 0;
constexpr unsigned WS_FQ = 1024;
constexpr unsigned WS_FH = WS_FQ + 16 * 24 * 64;
constexpr unsigned WS_FO = WS_FH + 16 * 16 * 64;
constexpr unsigned WS_ST = 65536;
constexpr unsigned WS_QKV = 131072;
constexpr unsigned SEC_BYTES = 32 * 64 * 1024;
constexpr unsigned WS_H = WS_QKV + 3 * SEC_BYTES;
constexpr unsigned WS_O = WS_H + 64 * 112 * 1024;
constexpr unsigned WS_END = WS_O + 64 * 32 * 1024;
static_assert(WS_FO + 256 * 64 <= WS_ST, "flags overlap");
constexpr int SLOT = 32800, SUB1 = 16416;
constexpr int AUX = 4 * SLOT;
constexpr int W1A = AUX + 8192;
constexpr int REG = 32768;
constexpr int LDS_BYTES = 163840;
constexpr int KV = 3 * REG;
constexpr int ONES = LDS_BYTES - 1024;
constexpr float QSCALE = 0.25f * 1.4426950408889634f;
constexpr unsigned SPIN_CAP = 1u << 18;

struct Params {
  const void *x, *wqkv, *wo, *wg, *wu, *wd;
  void *out, *ws;
};

template <int I> struct IC { static constexpr int v = I; };
template <int I, int N, class F> DI void static_for(F&& f) {
  if constexpr (I < N) {
    f(IC<I>{});
    static_for<I + 1, N>(f);
  }
}
template <int VM> DI void wait_vm() { asm volatile("s_waitcnt vmcnt(%0)" ::"n"(VM) : "memory"); }
template <int VM, int LG> DI void wait_bar() { asm volatile("s_waitcnt vmcnt(%0) lgkmcnt(%1)\n\ts_barrier" ::"n"(VM), "n"(LG) : "memory"); }
DI unsigned uni(unsigned x) {
  x = __builtin_amdgcn_readfirstlane(x);
  asm volatile("" : "+s"(x));
  return x;
}
DI rsrc_t mk_rsrc(const void* p, unsigned bytes) { return __builtin_amdgcn_make_buffer_rsrc((void*)p, 0, bytes, 0x00020000); }
DI void glds(rsrc_t rs, unsigned voff, unsigned soff, unsigned lds) {
  asm volatile("s_mov_b32 m0, %2\n\ts_nop 0\n\tbuffer_load_dwordx4 %0, %1, %3 offen lds" ::"v"(voff), "s"(rs),
               "s"(RFL(lds)), "s"(RFL(soff)) : "memory");
}
template <int IMM = 0> DI u32x4 ld128h(rsrc_t rs, unsigned voff, unsigned soff) {
  u32x4 r;
  asm volatile("buffer_load_dwordx4 %0, %1, %2, %3 offen offset:%4 sc1" : "=v"(r) : "v"(voff), "s"(rs), "s"(RFL(soff)), "n"(IMM) : "memory");
  return r;
}
DI u32x2 ld64h(rsrc_t rs, unsigned voff) {
  u32x2 r;
  asm volatile("buffer_load_dwordx2 %0, %1, %2, 0 offen sc1" : "=v"(r) : "v"(voff), "s"(rs) : "memory");
  return r;
}
DI void st128h(rsrc_t rs, unsigned voff, unsigned soff, u32x4 d) {
  asm volatile("buffer_store_dwordx4 %0, %1, %2, %3 offen sc1\n\ts_nop 1" ::"v"(d), "v"(voff), "s"(rs), "s"(RFL(soff)) : "memory");
}
DI void put_flag(rsrc_t ws, int lane, unsigned soff, u32x2 tag) {
  asm volatile("buffer_store_dwordx2 %0, %1, %2, %3 offen sc1" ::"v"(tag), "v"(lane ? 0x80000000u : 0u), "s"(ws), "s"(RFL(soff)) : "memory");
}
DI void check_flags(rsrc_t ws, unsigned voff, u32x2 tag, u32x2 v) {
  unsigned cnt;
  asm volatile(
      "s_mov_b32 %1, %5\n"
      "1:\n\t"
      "v_cmp_ne_u64 vcc, %2, %0\n\t"
      "s_cbranch_vccz 2f\n\t"
      "s_sleep 1\n\t"
      "buffer_load_dwordx2 %0, %3, %4, 0 offen sc1\n\t"
      "s_waitcnt vmcnt(0)\n\t"
      "s_sub_u32 %1, %1, 1\n\t"
      "s_cbranch_scc0 1b\n"
      "2:"
      : "+v"(v), "=&s"(cnt)
      : "s"(tag), "v"(voff), "s"(ws), "n"(SPIN_CAP)
      : "vcc", "scc", "memory");
}
DI void check_granule(rsrc_t ws, unsigned voff, unsigned tag, u32x2& v) {
  unsigned cnt, lo = v.x, hi = v.y;
  asm volatile(
      "s_mov_b32 %2, %6\n"
      "1:\n\t"
      "v_cmp_ne_u32 vcc, %3, %1\n\t"
      "s_cbranch_vccz 2f\n\t"
      "s_sleep 1\n\t"
      "buffer_load_dword %1, %4, %5, 0 offen offset:4 sc1\n\t"
      "s_waitcnt vmcnt(0)\n\t"
      "buffer_load_dword %0, %4, %5, 0 offen sc1\n\t"
      "s_waitcnt vmcnt(0)\n\t"
      "s_sub_u32 %2, %2, 1\n\t"
      "s_cbranch_scc0 1b\n"
      "2:"
      : "+v"(lo), "+v"(hi), "=&s"(cnt)
      : "s"(tag), "v"(voff), "s"(ws), "n"(SPIN_CAP)
      : "vcc", "scc", "memory");
  v.x = lo;
}
template <int CTRL> DI float dpp_add(float v) {
  return v + __builtin_bit_cast(float, __builtin_amdgcn_update_dpp(0, __builtin_bit_cast(int, v), CTRL, 0xf, 0xf, true));
}
DI float wave_sum63(float v) {
  v = dpp_add<0x111>(v); v = dpp_add<0x112>(v); v = dpp_add<0x114>(v); v = dpp_add<0x118>(v);
  v = dpp_add<0x142>(v);
  return dpp_add<0x143>(v);
}
DI s16x4 trd(LAS const char* p) { return __builtin_amdgcn_ds_read_tr16_b64_v4i16((LAS s16x4*)p); }
template <int STEP> DI bf16x8 rdW(LAS const char* p) {
  const s16x4 a = trd(p), b = trd(p + STEP);
  return __builtin_shufflevector(a, b, 0, 1, 2, 3, 4, 5, 6, 7);
}
DI unsigned pk(float lo, float hi) {
  const f32x2 v = {lo, hi};
  return __builtin_bit_cast(unsigned, __builtin_convertvector(v, bf2));
}
DI float rbf(float v) { return __builtin_bit_cast(float, pk(0.f, v) & 0xffff0000u); }
#define COLSUM(w, cs) cs = __builtin_amdgcn_mfma_f32_16x16x32_bf16(a.sel, (w), cs, 0, 0, 0)
DI bf16x8 mk_sel(int l) {
  const unsigned sl = ((l ^ (l >> 4)) & 1) ? 0u : 0x3f803f80u;
  return __builtin_bit_cast(bf16x8, u32x4{sl, sl, sl, sl});
}
DI float swap32_max(float m) {
  auto r = __builtin_amdgcn_permlane32_swap(__float_as_uint(m), __float_as_uint(m), false, false);
  return __builtin_fmaxf(__uint_as_float(r[0]), __uint_as_float(r[1]));
}
DI float swap32_add(float m) {
  auto r = __builtin_amdgcn_permlane32_swap(__float_as_uint(m), __float_as_uint(m), false, false);
  return __uint_as_float(r[0]) + __uint_as_float(r[1]);
}
DI f32x16 splat(float v) {
  f32x16 r;
#pragma unroll
  for (int i = 0; i < 16; ++i) r[i] = v;
  return r;
}
DI float cs_get(LAS const char* csp) {
  LAS const float* p = (LAS const float*)csp;
  return (p[0] + p[64]) + (p[128] + p[192]);
}
DI void ln_fix(f32x16& acc, float c, unsigned nm) {
  const unsigned u0 = __float_as_uint(c);
  const float r1 = c - __uint_as_float(u0 & 0xffff0000u);
  const unsigned u1 = __float_as_uint(r1);
  const float r2 = r1 - __uint_as_float(u1 & 0xffff0000u);
  const u32x2 a = {(u0 >> 16) | (u1 & 0xffff0000u), __float_as_uint(r2) >> 16};
  acc = __builtin_amdgcn_mfma_f32_32x32x8bf16_1k(__builtin_bit_cast(s16x4, a), __builtin_bit_cast(s16x4, u32x2{nm, nm}), acc, 0, 0, 0);
}
DI float geglu(float a, float b, float k1, float k2, float ir2) {
  const float e = __builtin_amdgcn_exp2f(a * __builtin_fmaf(a * a, k2, k1));
  return a * b * __builtin_amdgcn_rcpf(__builtin_fmaf(e, ir2, ir2));
}

DI unsigned w_voff(int lane, int pitch) {
  PIN(lane);
  const int r8 = lane >> 3, p8 = lane & 7;
  return r8 * pitch + (((p8 >> 1) ^ (r8 & 2)) * 32) + (p8 & 1) * 16;
}
struct Cx {
  int lane, w, g, c;
  int rt, ct, rot;
  int hd, G;
  unsigned voffC;
  unsigned cbd, cbo, hb;
  rsrc_t ws, rwd, rwo;
  u32x2 tag;
  LAS char* L;
};
DI void c_setup(Cx& v) {
  v.voffC = w_voff(v.lane, 1024);
  v.cbd = uni((448 * v.g + 32 * v.c) * 1024 + v.ct * 128);
  v.cbo = uni((128 * v.g + 32 * v.c) * 1024 + v.ct * 128);
  v.hb = uni(WS_H + ((2 * v.rt + v.c) * 112 + 28 * v.g) * 1024);
  v.rot = uni((v.rt * 2 + v.ct) % 7);
}
DI int c_grp(const Cx& v, int gp) {
  if (gp >= 7) return (gp - 7) ^ (v.rot & 1);
  const int r = gp + v.rot;
  return r >= 7 ? r - 7 : r;
}
DI unsigned c_voff(const Cx& v, int gp, int i) { return v.voffC + (c_grp(v, gp) << 16) + i * 8192; }
DI unsigned c_dst(const Cx& v, int region, int i) { return region * REG + v.g * 8192 + v.c * 4096 + i * 1024; }
DI void c_piece(const Cx& v, int gp, int region, int i) {
  glds(gp < 7 ? v.rwd : v.rwo, c_voff(v, gp, i), gp < 7 ? v.cbd : v.cbo, c_dst(v, region, i));
}
DI u32x4 c_stash(const Cx& v, int n) {
  const int gp = 4 + (n >> 2);
  u32x4 r;
  asm volatile("buffer_load_dwordx4 %0, %1, %2, %3 offen"
               : "=v"(r) : "v"(c_voff(v, gp, n & 3)), "s"(gp < 7 ? v.rwd : v.rwo), "s"(gp < 7 ? v.cbd : v.cbo) : "memory");
  return r;
}

struct AtPre { u32x4 q, k0, k1, v0, v1; };
DI int at_kb(const Cx& v) {
  const int kb = 8 * v.G - 8 + 2 * v.w;
  return kb < 0 ? 0 : kb;
}
DI u32x4 at_load(const Cx& v, int i) {
  const int blk = i == 0 ? 8 * v.G + v.w : at_kb(v) + ((i - 1) & 1);
  return ld128h(v.ws, v.lane * 16, WS_QKV + ((i + 1) >> 1) * SEC_BYTES + (v.hd * 64 + blk) * 1024);
}
DI unsigned at_flag_off(const Cx& v) {
  const int i = v.lane & 7, isq = i >> 2;
  const int blk = at_kb(v) + (i & 1) + isq * (8 * v.G + v.w - at_kb(v) - (i & 1));
  const int sec = (1 - isq) * (1 + ((i >> 1) & 1));
  return WS_FQ + ((blk >> 2) * 24 + sec * 8 + (v.hd >> 2)) * 64 + (((v.hd >> 1) & 1) * 4 + (blk & 3)) * 8;
}
DI void at_stage(const Cx& v, const AtPre& a) {
  LAS char* kd = v.L + KV + 2 * v.w * 1024 + v.lane * 16;
  LAS char* vd = v.L + KV + 16384 + 2 * v.w * 1024 + (v.lane & 31) * 32 + (v.lane >> 5) * 16;
  *(LAS u32x4*)kd = a.k0;
  *(LAS u32x4*)(kd + 1024) = a.k1;
  *(LAS u32x4*)vd = a.v0;
  *(LAS u32x4*)(vd + 1024) = a.v1;
  if (v.w == 0) *(LAS u32x4*)(v.L + ONES + v.lane * 16) = u32x4{0x3f803f80u, 0x3f803f80u, 0x3f803f80u, 0x3f803f80u};
}
DI void max16(const f32x16& s, float& m, float& m2) {
#pragma unroll
  for (int r = 0; r < 16; r += 4) {
    m = __builtin_fmaxf(__builtin_fmaxf(m, s[r]), s[r + 1]);
    m2 = __builtin_fmaxf(__builtin_fmaxf(m2, s[r + 2]), s[r + 3]);
  }
}
DI void exp_pack(const f32x16& s, u32x4& p0, u32x4& p1) {
#pragma unroll
  for (int i = 0; i < 4; ++i) {
    p0[i] = pk(__builtin_amdgcn_exp2f(s[2 * i]), __builtin_amdgcn_exp2f(s[2 * i + 1]));
    p1[i] = pk(__builtin_amdgcn_exp2f(s[8 + 2 * i]), __builtin_amdgcn_exp2f(s[9 + 2 * i]));
  }
}
template <int I> DI u32x4 h_load(const Cx& v) { return ld128h<(I & 3) * 1024>(v.ws, v.lane * 16 + (c_grp(v, I >> 2) << 12), v.hb); }
#define PIN4(a, o) asm volatile("" : "+v"(a[o]), "+v"(a[o + 1]), "+v"(a[o + 2]), "+v"(a[o + 3]))
template <bool FULL> DI void at_body(const Cx& v, const u32x4& qv, unsigned hflag, u32x4 (&hf)[28], u32x4 (&sw)[20]) {
  const int lane = v.lane, t = lane & 31, h = lane >> 5;
  const bf16x8 q = __builtin_bit_cast(bf16x8, qv);
  LAS const char* kp = v.L + KV + v.w * 1024 + lane * 16;
  const int qq = (lane & 15) >> 2, p = lane & 3;
  const bool real = (lane & 16) == 0;
  int vreal = KV + 16384 + v.w * 1024 + (4 * h + qq) * 32 + (p & 1) * 16 + (p >> 1) * 8, vone = ONES + (lane & 15) * 8;
  PIN(vreal); PIN(vone);
  LAS const char* vp = v.L + (real ? vreal : vone);
  const int vstr = real ? 1024 : 0;
  const float NEG = -1e30f;
  const f32x16 zero = {};
  const unsigned hpoll = WS_FH + ((v.rt >> 1) * 16 + 4 * v.g + (lane & 3)) * 64 + (((lane >> 2) & 1) * 4 + ((2 * v.rt + v.c) & 3)) * 8;
  const int td = t - 4 * h;
#define KFRAG(kb) (*(LAS const bf16x8*)(kp + (kb) * 1024))
#define VFRAG(kb, s) rdW<256>(vp + (kb) * vstr + (s) * 512)
#define BF(x) __builtin_bit_cast(bf16x8, x)
  auto merge = [&](const f32x16& sd, const f32x16& sf) __attribute__((always_inline)) {
    f32x16 r;
#pragma unroll
    for (int i = 0; i < 16; ++i) r[i] = (i & 3) + 8 * (i >> 2) <= td ? sd[i] : sf[i];
    return r;
  };
  u32x2 fl;
  float m = NEG;
  f32x16 o = zero, cm, sc;
  u32x4 p0, p1;
  if (FULL) {
    bf16x8 kf[9];
#pragma unroll
    for (int kb = 0; kb < 9; ++kb) kf[(kb + 8) % 9] = KFRAG((kb + 8) % 9);
    f32x16 sa = MFMA(kf[8], q, zero), sb = MFMA(kf[0], q, zero);
    sc = MFMA(kf[1], q, zero);
    PIN(sa); PIN(sb); PIN(sc);
    SBAR();
    wait_vm<3>();
    put_flag(v.ws, lane, hflag, v.tag);
    float m2 = NEG;
    max16(merge(sa, sb), m, m2);
    static_for<1, 8>([&](auto K_) __attribute__((always_inline)) {
      constexpr int kb = decltype(K_)::v;
      f32x16 sn;
      if constexpr (kb < 7) { sn = MFMA(kf[kb + 1], q, zero); PIN(sn); }
      SBAR();
      if constexpr ((kb & 1) == 0) sw[4 + (kb >> 1)] = c_stash(v, 4 + (kb >> 1));
      max16(sc, m, m2);
      PIN(m); PIN(m2);
      SBAR();
      if constexpr (kb < 7) sc = sn;
    });
    m = swap32_max(__builtin_fmaxf(m, m2));
    bf16x8 ka, kc = kf[1], kn = kf[2];
    STAMP2(8);
    fl = ld64h(v.ws, hpoll);
    cm = splat(-m);
    sc = MFMA(kc, q, cm);
    static_for<1, 8>([&](auto K_) __attribute__((always_inline)) {
      constexpr int kb = decltype(K_)::v;
      const bf16x8 v0 = VFRAG(kb, 0), v1 = VFRAG(kb, 1);
      f32x16 sn;
      if constexpr (kb < 7) { sn = MFMA(kn, q, cm); PIN(sn); }
      else { sa = MFMA(kn, q, cm); sb = MFMA(ka, q, cm); PIN(sa); PIN(sb); }
      if constexpr (kb < 7) kn = KFRAG(kb + 2);
      if constexpr (kb == 6) ka = KFRAG(0);
      SBAR();
      if constexpr (kb < 3) { sw[6 + 2 * kb] = c_stash(v, 6 + 2 * kb); sw[7 + 2 * kb] = c_stash(v, 7 + 2 * kb); }
      exp_pack(sc, p0, p1);
      o = MFMA(v0, BF(p0), o);
      o = MFMA(v1, BF(p1), o);
      PIN(o);
      if (kb == 2) {
        STAMP2(10);
        asm volatile("s_waitcnt vmcnt(4)" : "+v"(fl)::"memory");
        check_flags(v.ws, hpoll, v.tag, fl);
        STAMP2(11);
      }
      if constexpr (kb >= 2) {
        hf[2 * kb - 4] = h_load<2 * kb - 4>(v);
        hf[2 * kb - 3] = h_load<2 * kb - 3>(v);
      }
      SBAR();
      if constexpr (kb < 7) sc = sn;
    });
    sc = merge(sa, sb);
  } else {
    wait_vm<3>();
    put_flag(v.ws, lane, hflag, v.tag);
#pragma unroll
    for (int i = 5; i < 12; ++i) sw[i] = c_stash(v, i);
    float m2 = NEG;
    max16(merge(MFMA(KFRAG(8), q, zero), splat(NEG)), m, m2);
    for (int kb = 8 - v.w; kb < 8; ++kb) max16(MFMA(KFRAG(kb), q, zero), m, m2);
    m = swap32_max(__builtin_fmaxf(m, m2));
    cm = splat(-m);
    for (int kb = 8 - v.w; kb < 8; ++kb) {
      exp_pack(MFMA(KFRAG(kb), q, cm), p0, p1);
      o = MFMA(VFRAG(kb, 0), BF(p0), o);
      o = MFMA(VFRAG(kb, 1), BF(p1), o);
    }
    sc = merge(MFMA(KFRAG(8), q, cm), splat(NEG));
    PIN(o); PIN(sc);
    check_flags(v.ws, hpoll, v.tag, u32x2{~v.tag.x, 0u});
    static_for<0, 12>([&](auto I_) __attribute__((always_inline)) { hf[decltype(I_)::v] = h_load<decltype(I_)::v>(v); });
  }
  hf[12] = h_load<12>(v);
  hf[13] = h_load<13>(v);
  exp_pack(sc, p0, p1);
  u32x4 k0, k1;
#pragma unroll
  for (int i = 0; i < 4; ++i) {
    const int r0 = 2 * (i & 1) + 8 * (i >> 1);
    k0[i] = (r0 <= td ? 0xffffu : 0u) | (r0 + 1 <= td ? 0xffff0000u : 0u);
    k1[i] = (r0 + 16 <= td ? 0xffffu : 0u) | (r0 + 17 <= td ? 0xffff0000u : 0u);
  }
  o = MFMA(VFRAG(8, 0), BF(p0 & k0), o);
  o = MFMA(VFRAG(8, 1), BF(p1 & k1), o);
  o = MFMA(VFRAG(0, 0), BF(p0 & ~k0), o);
  o = MFMA(VFRAG(0, 1), BF(p1 & ~k1), o);
  const float il = __builtin_amdgcn_rcpf(o[8]);
  u32x4 ov;
#pragma unroll
  for (int i = 0; i < 4; ++i) ov[i] = pk(o[2 * i] * il, o[2 * i + 1] * il);
  st128h(v.ws, lane * 16, WS_O + ((8 * v.G + v.w) * 32 + v.hd) * 1024, ov);
}

DI void c_scatter(const Cx& v, LAS char* X, const f32x16& a0, const f32x16& a1) {
  const int t = v.lane & 31;
  LAS char* row = X + v.g * 8192 + t * 256;
  const int sw = ((v.lane >> 5) ^ (t & 15)) * 16;
#pragma unroll
  for (int j = 0; j < 8; ++j) {
    const f32x16& a = j < 4 ? a0 : a1;
    const int o = 4 * (j & 3);
    *(LAS f32x4*)(row + (sw ^ (j * 32))) = f32x4{a[o], a[o + 1], a[o + 2], a[o + 3]};
  }
}
DI f32x4 c_gather(const Cx& v, LAS const char* X, int half) {
  const int t = 8 * v.g + (v.lane >> 3);
  LAS const f32x4* src = (LAS const f32x4*)(X + t * 256 + (((2 * (v.lane & 7) + half) ^ (t & 15)) * 16));
  return (src[0] + src[512]) + (src[1024] + src[1536]);
}
DI f32x4 silu4(f32x4 d) {
#pragma unroll
  for (int i = 0; i < 4; ++i) d[i] *= __builtin_amdgcn_rcpf(1.f + __builtin_amdgcn_exp2f(-1.4426950408889634f * d[i]));
  return d;
}
DI u32x2 fin4(u32x2 x, f32x4 s, f32x4 g) {
  return u32x2{pk(__builtin_fmaf(s[0], g[0], __uint_as_float(x.x << 16)), __builtin_fmaf(s[1], g[1], __uint_as_float(x.x & 0xffff0000u))),
               pk(__builtin_fmaf(s[2], g[2], __uint_as_float(x.y << 16)), __builtin_fmaf(s[3], g[3], __uint_as_float(x.y & 0xffff0000u)))};
}
DI void phase_c(const Params& P, const Cx& v, u32x4 (&hf)[28], u32x4 (&sw)[20], bool first) {
  const int lane = v.lane, h = lane >> 5, tb = 2 * v.rt + v.c;
  const int qq = (lane & 15) >> 2, p = lane & 3, g1 = (lane >> 4) & 1;
  LAS const char* b0 = v.L + v.g * 8192 + (4 * h + qq) * 128 + (g1 + (qq & 2)) * 32 + 8 * p;
  LAS const char* b1 = v.L + v.g * 8192 + (4 * h + qq) * 128 + (g1 + (2 ^ (qq & 2))) * 32 + 8 * p;
  const unsigned opoll = first && lane >= 32 ? WS_FO + (v.w * 32 + lane - 32) * 64
                                             : WS_FO + ((v.rt >> 2) * 32 + 8 * v.g + (lane & 7)) * 64 + (tb & 7) * 8;
  const unsigned obase = uni(WS_O + (tb * 32 + 8 * v.g) * 1024);
  const rsrc_t rx = mk_rsrc(P.x, 2048 * 512 * 2);
  const unsigned xoff = (32 * tb + 8 * v.g + (lane >> 3)) * 1024 + v.ct * 128 + (lane & 7) * 16;
  LAS char* xd = v.L + (v.c ? 0 : 3 * REG);
  LAS char* xs = v.L + REG + v.c * REG;
  f32x16 d0 = {}, d1 = {}, s0 = {}, s1 = {};
  f32x4 ga, gb;
  bf16x8 wf[3][2];
  u32x4 of[8];
  u32x2 fl;
  u32x4 xv;
  constexpr int RG[9] = {0, 1, 2, 4, 3, 0, 1, 2, 4};
  auto rd = [&](auto S_) __attribute__((always_inline)) {
    constexpr int s = decltype(S_)::v;
    wf[s % 3][0] = rdW<1024>(b0 + RG[s >> 2] * REG + (s & 3) * 2048);
    wf[s % 3][1] = rdW<1024>(b1 + RG[s >> 2] * REG + (s & 3) * 2048);
  };
  static_for<0, 36>([&](auto S_) __attribute__((always_inline)) {
    constexpr int s = decltype(S_)::v, gp = s >> 2;
    auto oreq = [&](auto N_) __attribute__((always_inline)) {
      constexpr int n = decltype(N_)::v;
      of[n] = ld128h<(n & 3) * 1024>(v.ws, lane * 16 + (c_grp(v, 7 + (n >> 2)) << 12), obase);
    };
    if (s == 0) {
      static_for<14, 20>([&](auto I_) __attribute__((always_inline)) { hf[decltype(I_)::v] = h_load<decltype(I_)::v>(v); });
      wait_vm<6>();
      put_flag(v.ws, lane, WS_FO + (v.G * 32 + v.hd) * 64 + v.w * 8, v.tag);
      wait_bar<63, 0>();
      PIN4(hf, 0); PIN4(hf, 4); PIN4(hf, 8); PIN(hf[12]); PIN(hf[13]); PIN4(sw, 4); PIN4(sw, 8);
      rd(IC<0>{}); rd(IC<1>{});
      c_piece(v, 3, 4, 3);
    }
    if (s == 8) {
      wait_bar<12, 8>();
      PIN(hf[14]); PIN(hf[15]); PIN4(hf, 16);
    }
    if (s == OCHK) {
      asm volatile("s_waitcnt vmcnt(0)" : "+v"(fl)::"memory");
      check_flags(v.ws, opoll, v.tag, fl);
    }
    if (s == 16) { wait_bar<1, 8>(); PIN4(hf, 20); PIN4(hf, 24); PIN4(sw, 12); PIN4(sw, 16); }
    if (s == 28) {
      wait_bar<1, 0>();
      if (first && v.w == 0) put_flag(v.ws, lane, WS_EPOCH, v.tag);
      PIN4(of, 0); PIN4(of, 4); rd(IC<28>{}); rd(IC<29>{});
    }
    if (s == 32) wait_bar<63, 8>();
    if ((s & 3) == 0) STAMP2(40 + gp);
    if constexpr (s + 2 < 36 && s != 26 && s != 27) rd(IC<s + 2>{});
    const bf16x8 f = BF(gp < 7 ? hf[gp < 7 ? s : 0] : of[gp < 7 ? 0 : s - 28]);
    f32x16& a0 = gp < 7 ? d0 : s0;
    f32x16& a1 = gp < 7 ? d1 : s1;
    a0 = MFMA(wf[s % 3][0], f, a0);
    PIN(a0);
    SBAR();
    if constexpr (s < 4) hf[20 + 2 * s] = h_load<20 + 2 * s>(v);
    if constexpr (s >= 4 && s < 12) sw[8 + s] = c_stash(v, 8 + s);
    if constexpr (s >= OCHK && s < OCHK + 4) oreq(IC<2 * (s - OCHK)>{});
    if constexpr (s < 4) *(LAS u32x4*)(v.L + c_dst(v, 3, s) + lane * 16) = sw[s];
    if constexpr (s >= 8 && s < 14) {
      constexpr int j = s - 8;
      *(LAS u32x4*)(v.L + c_dst(v, j >> 2, j & 3) + lane * 16) = sw[4 + j];
      if constexpr (j < 2) *(LAS u32x4*)(v.L + c_dst(v, 1, 2 + j) + lane * 16) = sw[10 + j];
    }
    if constexpr (s >= 16 && s < 24) *(LAS u32x4*)(v.L + c_dst(v, s < 20 ? 2 : 4, s & 3) + lane * 16) = sw[s - 4];
    if (s == 33) { ga = silu4(ga); PIN(ga); }
    if (s == 34) { gb = silu4(gb); PIN(gb); }
    SBAR();
    a1 = MFMA(wf[s % 3][1], f, a1);
    PIN(a1);
    SBAR();
    if constexpr (s < 4) hf[21 + 2 * s] = h_load<21 + 2 * s>(v);
    if (s == OCHK - 5) fl = ld64h(v.ws, opoll);
    if constexpr (s >= OCHK && s < OCHK + 4) oreq(IC<2 * (s - OCHK) + 1>{});
    if (s == OCHK + 4) asm volatile("buffer_load_dwordx4 %0, %1, %2, 0 offen" : "=v"(xv) : "v"(xoff), "s"(rx) : "memory");
    if (s == 28) c_scatter(v, xd, d0, d1);
    if (s == 32) { ga = c_gather(v, xd, 0); gb = c_gather(v, xd, 1); }
    SBAR();
  });
  STAMP(14);
  c_scatter(v, xs, s0, s1);
  asm volatile("s_waitcnt vmcnt(0) lgkmcnt(0)\n\ts_barrier" : "+v"(xv)::"memory");
  const u32x2 y0 = fin4(u32x2{xv.x, xv.y}, c_gather(v, xs, 0), ga), y1 = fin4(u32x2{xv.z, xv.w}, c_gather(v, xs, 1), gb);
  __builtin_amdgcn_raw_buffer_store_b128(u32x4{y0.x, y0.y, y1.x, y1.y}, mk_rsrc(P.out, 2048 * 512 * 2), xoff, 0, 0);
  STAMP(15); STAMPR(31); STAMP_FLUSH();
}

struct AB {
  bf16x8 X[32];
  bf16x8 wf[3][2];
  bf16x8 sel;
  unsigned nm;
  float r, k1, k2, ir2;
  int gr;
  int rb, cg, strip0, dblA, dblB;
  int kx;
  unsigned voffX, voffQ, voffF;
  rsrc_t rx, rq, rf, ru;
};
DI void x_piece(const Cx& v, const AB& a, int kc, int slot, int i) {
  glds(a.rx, a.voffX ^ (64 * i), (a.rb * 128 + 16 * v.w + 4 * i) * 1024 + (kc ^ a.kx) * 256, slot * SLOT + (4 * v.w + i) * 1024);
}
DI void q_piece(const Cx& v, const AB& a, int strip, int half, int slot, int i) {
  glds(a.rq, a.voffQ, (128 * ((2 * half + v.c) ^ a.kx) + 32 * v.g + 8 * i) * 3072 + strip * 128, slot * SLOT + v.c * SUB1 + v.g * 4096 + i * 1024);
}
DI void q4_piece(const Cx& v, const AB& a, int kq, int dst, int i) {
  glds(a.rq, a.voffQ, (128 * (kq ^ a.kx) + 16 * v.w + 8 * i) * 3072 + a.strip0 * 128, dst + (2 * v.w + i) * 1024);
}
DI void f_piece(const Cx& v, const AB& a, int dbl, int kq, int slot, int i) {
  glds(a.rf, a.voffF, (128 * (kq ^ a.kx) + 32 * v.g + 8 * i) * 3584 + dbl * 128, slot * SLOT + v.c * SUB1 + v.g * 4096 + i * 1024);
}
template <bool TB> DI void w_piece(const Cx& v, const AB& a, int j, int i) {
  if (TB && j < 6) q_piece(v, a, a.strip0 + (j >> 1), j & 1, (j - 2) & 3, i);
  else f_piece(v, a, j < 6 ? a.dblA : a.dblB, (j - 2) & 3, (j - 2) & 3, i);
}
DI LAS char* cs_slot(const Cx& v, int j) { return v.L + AUX + (j * 2 + v.c) * 1024 + v.lane * 4; }
DI void cs_put(const Cx& v, int j, const f32x4& c) { *(LAS float*)(cs_slot(v, j) + v.g * 256) = (v.lane & 16) ? c[1] : c[0]; }
DI void q_store(const Cx& v, const AB& a, const f32x16& acc, int strip, int hh) {
  const float sc = strip < 8 ? a.r * QSCALE : a.r;
  u32x4 o;
#pragma unroll
  for (int i = 0; i < 4; ++i) o[i] = pk(acc[8 * hh + 2 * i] * sc, acc[8 * hh + 2 * i + 1] * sc);
  st128h(v.ws, v.lane * 16, WS_QKV + ((strip * 4 + 2 * v.c + hh) * 64 + 4 * a.rb + v.g) * 1024, o);
}
DI void q_flag(const Cx& v, const AB& a, int strip) { put_flag(v.ws, v.lane, WS_FQ + (a.rb * 24 + strip) * 64 + v.w * 8, v.tag); }
DI void put_h(const Cx& v, const AB& a, int dbl, int j, const u32x4& hv) {
  st128h(v.ws, v.lane * 16, WS_H + ((4 * a.rb + v.g) * 112 + 4 * dbl + 2 * v.c + j) * 1024, hv);
}
struct Bases { unsigned b[4]; };
DI Bases mk_bases(const Cx& v, const AB& a, int g1mul) {
  int l = v.lane;
  PIN(l);
  const int qq = (l & 15) >> 2;
  const int o = ((l >> 4) & 1) * g1mul + (8 * (l >> 5) + qq) * 128 + ((2 * v.c) ^ (qq & 2)) * 32 + 8 * (l & 3);
  Bases r;
#pragma unroll
  for (int i = 0; i < 4; ++i) { r.b[i] = o + (i ^ a.gr) * 4096; PIN(r.b[i]); }
  return r;
}
DI void bump(Bases& B, int d) {
#pragma unroll
  for (int i = 0; i < 4; ++i) { B.b[i] += d; PIN(B.b[i]); }
}
#define WRD(B, off, i) rdW<512>((LAS const char*)(size_t)((B).b[(i) >> 1]) + (off) + ((i) & 1) * 2048)

DI u32x2 make_tag(u32x2 e) {
  const unsigned long long z = ((unsigned long long)__builtin_amdgcn_readfirstlane(e.y) << 32 | (unsigned)__builtin_amdgcn_readfirstlane(e.x));
  const unsigned long long t = (z ^ (z >> 31)) * 0x9E3779B97F4A7C15ull + 0xD1B54A32D192ED03ull;
  return u32x2{(unsigned)t, (unsigned)(t >> 32)};
}

DI void stats_put(const Cx& v, const AB& a, const u32x4& xr) {
  float s1 = 0.f, s2 = 0.f;
#pragma unroll
  for (int d = 0; d < 4; ++d) {
    const float lo = __uint_as_float(xr[d] << 16), hi = __uint_as_float(xr[d] & 0xffff0000u);
    s1 += lo + hi;
    s2 = __builtin_fmaf(lo, lo, __builtin_fmaf(hi, hi, s2));
  }
  const float mean = wave_sum63(s1) * (1.f / 512.f);
  const float var = __builtin_fmaxf(__builtin_fmaf(-mean, mean, wave_sum63(s2) * (1.f / 512.f)), 0.f);
  const float sd = __builtin_amdgcn_sqrtf(rbf(rbf(var) + 1e-5f));
  put_flag(v.ws, v.lane ^ 63, WS_ST + (a.rb * 128 + a.cg * 8 + v.w) * 8, u32x2{pk(-mean, sd), v.tag.x});
}
template <bool TB, int KQ, int XS, int WO>
DI void p_step(const Cx& v, AB& a, const Bases& B, f32x16& acc, f32x4& cs) {
  const int t = v.lane & 31;
  const unsigned u = (((v.lane >> 5) ^ (t & 15)) * 16) ^ (64 * a.gr);
  LAS const char* xr = v.L + XS * SLOT + (32 * v.g + t) * 256;
  bf16x8 wf[8];
#pragma unroll
  for (int i = 0; i < 3; ++i) {
    a.X[8 * KQ + i] = *(LAS const bf16x8*)(xr + (u ^ (32 * i)));
    wf[i] = WRD(B, WO, i);
  }
#pragma unroll
  for (int i = 0; i < 8; ++i) {
    if (i < 5) {
      a.X[8 * KQ + i + 3] = *(LAS const bf16x8*)(xr + (u ^ (32 * (i + 3))));
      wf[i + 3] = WRD(B, WO, i + 3);
    }
    if (KQ == 0) { if (i < 4) x_piece(v, a, 2, 1, i); else if (i < 6) q4_piece(v, a, 2, W1A, i - 4); }
    if (KQ == 1 && i < 4) x_piece(v, a, 3, 3, i);
    if (KQ == 2) { if (i < 2) q4_piece(v, a, 3, 2 * SLOT, i); else if (i < 6) w_piece<TB>(v, a, 2, i - 2); }
    if (KQ == 3 && i < 4) w_piece<TB>(v, a, 3, i);
    acc = MFMA(wf[i], a.X[8 * KQ + i], acc);
    if (i < 2) COLSUM(wf[i], cs);
    PIN(acc); PIN(cs);
    SBAR();
  }
}
template <bool TB> DI void prologue(Cx& v, AB& a, f32x16& acc, u32x2& epoch, u32x4& xr, u32x2& st) {
  Bases B = mk_bases(v, a, 32);
  bump(B, 2 * SLOT);
  f32x4 cs = {};
  STAMP2(23);
  wait_bar<8, 0>();
  STAMP(1);
  p_step<TB, 0, 3, 0>(v, a, B, acc, cs);
  asm volatile("s_waitcnt vmcnt(6)" : "+v"(epoch), "+v"(xr)::"memory");
  v.tag = make_tag(epoch);
  stats_put(v, a, xr);
  STAMP2(16); wait_bar<7, 0>(); STAMP2(17);
  p_step<TB, 1, 0, SUB1>(v, a, B, acc, cs);
  STAMP2(18); wait_bar<4, 0>(); STAMP2(19);
  bump(B, W1A - 2 * SLOT);
  p_step<TB, 2, 1, 0>(v, a, B, acc, cs);
  STAMP2(20); wait_bar<4, 0>(); STAMP2(21);
  bump(B, 2 * SLOT - W1A);
  p_step<TB, 3, 3, 0>(v, a, B, acc, cs);
  STAMP2(22);
  cs_put(v, 0, cs);
  st = ld64h(v.ws, WS_ST + (a.rb * 128 + 32 * v.g + (v.lane & 31)) * 8);
  wait_bar<5, 0>();
}
DI void row_stats(const Cx& v, AB& a, unsigned g) {
  const float sd = __uint_as_float(g & 0xffff0000u);
  const float c1 = -2.f * 0.7978845608028654f * 1.4426950408889634f;
  a.r = __builtin_amdgcn_rcpf(sd);
  a.k1 = c1 * a.r;
  a.k2 = c1 * 0.044715f * a.r * a.r * a.r;
  a.ir2 = sd * sd;
  a.nm = v.lane < 32 ? (g & 0xffffu) | (g << 16) : 0u;
}
enum { K_QQ = 0, K_D0 = 1, K_D1L = 2, K_D0L = 3 };
template <bool Q> DI void rd_step(AB& a, const Bases& B, int s, int ph) {
  const int kq = (s >> 3) & 3, i = s & 7, so = (kq & 1) * SLOT;
  if (Q) {
    a.wf[ph][0] = WRD(B, so + (i >> 2) * SUB1, (2 * i) & 7);
    a.wf[ph][1] = WRD(B, so + (i >> 2) * SUB1, ((2 * i) & 7) + 1);
  } else {
    a.wf[ph][0] = WRD(B, so, i);
    a.wf[ph][1] = WRD(B, so + 32, i);
  }
}
template <int KIND> DI void body(Cx& v, AB& a, f32x16& p0, f32x16& p1, AtPre& ap, u32x2& st) {
  constexpr bool Q = KIND == K_QQ, FIRST = KIND == K_QQ || KIND == K_D0, TB = KIND == K_QQ || KIND == K_D0L;
  constexpr int PH0 = KIND == K_D1L ? 2 : 0;
  constexpr int SB = FIRST ? 32 : 36;
  Bases B = mk_bases(v, a, Q ? 32 : SUB1);
  f32x16 a0 = {}, a1 = {};
  f32x4 c0 = {}, c1 = {};
  float cp0, cp1, hlo;
  u32x4 hv;
  u32x2 fl;
  unsigned fo;
  if (KIND != K_D1L) {
    rd_step<Q>(a, B, 0, 0);
    rd_step<Q>(a, B, 1, 1);
  }
  static_for<0, 32>([&](auto S_) __attribute__((always_inline)) {
    constexpr int s = decltype(S_)::v, kq = s >> 3, i = s & 7;
    if (s == (Q ? 1 : 2)) { int l = v.lane; PIN(l); a.sel = mk_sel(l); }
    if (s == 8 && !FIRST) c_setup(v);
    if (s == 14) bump(B, 2 * SLOT);
    if (s == 30 && KIND == K_D0) bump(B, -2 * SLOT);
    const bool late = FIRST && (s == 6 || s == 7);
    if (!late && (s < 30 || KIND == K_D0)) rd_step<Q>(a, B, s + 2, (s + 2 + PH0) % 3);
    if (s == 0 || (Q && s == 16)) cp0 = cs_get(cs_slot(v, 0));
    if (s == 1 && KIND == K_D1L) cp1 = cs_get(cs_slot(v, 1));
    const bf16x8 w0 = a.wf[(s + PH0) % 3][0], w1 = a.wf[(s + PH0) % 3][1];
    f32x16& acA = Q ? (kq < 2 ? a0 : a1) : a0;
    f32x16& acB = Q ? (kq < 2 ? a0 : a1) : a1;
    f32x4& csA = Q ? (kq < 2 ? c0 : c1) : c0;
    f32x4& csB = Q ? (kq < 2 ? c0 : c1) : c1;
    constexpr bool CS = Q ? (i & 3) == 1 : (i >> 1) == 1;
    constexpr int e = KIND != K_D1L ? -1 : (s == 30 ? 14 : ((s & 1) && s >= 3 ? (s == 31 ? 15 : (s - 3) >> 1) : -1));
    float ga, ex;
    acA = MFMA(w0, a.X[Q ? 16 * (kq & 1) + 2 * i : s], acA);
    PIN(acA);
    SBAR();
    if (CS) { COLSUM(w0, csA); PIN(csA); }
    if (FIRST && kq == 0) w_piece<TB>(v, a, 4 + (i >> 2), i & 3);
    else if ((i & 1) == 0) {
      if (FIRST) w_piece<TB>(v, a, 5 + kq, i >> 1);
      else if (kq == 0) w_piece<TB>(v, a, 9, i >> 1);
      else c_piece(v, kq - 1, kq - 1, i >> 1);
    }
    if constexpr (e >= 0) {
      ga = (e < 8 ? p0 : p1)[e & 7];
      ex = __builtin_amdgcn_exp2f(ga * __builtin_fmaf(ga * ga, a.k2, a.k1));
      ga *= (e < 8 ? p0 : p1)[8 + (e & 7)];
      PIN(ga); PIN(ex);
    }
    if (FIRST && s == 9) {
      const int per = v.w == 1 ? 16 : 56;
      asm volatile("s_mov_b32 m0, %2\n\ts_nop 0\n\tbuffer_load_dword %0, %1, 0 offen lds"
                   ::"v"(v.lane < per && v.w < 4 ? (blockIdx.x * per + v.lane) * 128 : 0x40000000u),
                     "s"(v.w == 0 ? v.rwd : (v.w == 1 ? v.rwo : (v.w == 2 ? a.rf : a.ru))), "s"(W1A) : "memory");
    }
    if (FIRST && s == 5) {
      asm volatile("s_waitcnt vmcnt(6)" : "+v"(st)::"memory");
      check_granule(v.ws, WS_ST + (a.rb * 128 + 32 * v.g + (v.lane & 31)) * 8, v.tag.x, st);
      row_stats(v, a, st.x);
      PIN(a.nm); PIN(a.r); PIN(a.k1); PIN(a.k2); PIN(a.ir2);
    }
    SBAR();
    acB = MFMA(w1, a.X[Q ? 16 * (kq & 1) + 2 * i + 1 : s], acB);
    PIN(acB);
    SBAR();
    if (CS) { COLSUM(w1, csB); PIN(csB); }
    if constexpr (e >= 0) {
      const float hx = ga * __builtin_amdgcn_rcpf(__builtin_fmaf(ex, a.ir2, a.ir2));
      if (e & 1) { hv[(e & 7) >> 1] = pk(hlo, hx); PIN(hv[(e & 7) >> 1]); }
      else { hlo = hx; PIN(hlo); }
      if (e == 15) put_h(v, a, a.dblA, 1, hv);
    }
    if (KIND == K_D1L) {
      if (s == 1) { ln_fix(p0, cp0, a.nm); PIN(p0); }
      if (s == 2) { ln_fix(p1, cp1, a.nm); PIN(p1); }
      if (s == 18) put_h(v, a, a.dblA, 0, hv);
    } else {
      constexpr int o = FIRST ? 4 : 0;
      const int st0 = KIND == K_D0L ? a.strip0 + 2 : a.strip0;
      if (s == 2 + o) { ln_fix(p0, cp0, a.nm); PIN(p0); }
      if (s == 5 + o || s == 7 + o) q_store(v, a, p0, st0, (s - 5 - o) >> 1);
      if (s == 13 + o) { wait_vm<3>(); q_flag(v, a, st0); }
      if (Q) {
        if (s == 14) cs_put(v, 0, c0);
        if (s == 18) { ln_fix(a0, cp0, a.nm); PIN(a0); }
        if (s == 21 || s == 23) q_store(v, a, a0, a.strip0 + 1, (s - 21) >> 1);
        if (s == 29) { wait_vm<3>(); q_flag(v, a, a.strip0 + 1); }
        if (s == 30) cs_put(v, 0, c1);
      }
    }
    if (!Q && s == 28) {
      cs_put(v, 0, c0);
      cs_put(v, 1, c1);
    }
    if (!FIRST) {
      if (s == 28) { SBAR(); wait_bar<63, 0>(); cp0 = cs_get(cs_slot(v, 0)); cp1 = cs_get(cs_slot(v, 1)); }
      if (s == 30) { ln_fix(a0, cp0, a.nm); PIN(a0); }
      if (s == 31) { ln_fix(a1, cp1, a.nm); PIN(a1); }
    }
    if (!FIRST) {
      if (s == 23) { fo = at_flag_off(v); fl = ld64h(v.ws, fo); }
      if (s == 27) {
        asm volatile("s_waitcnt vmcnt(2)" : "+v"(fl)::"memory");
        check_flags(v.ws, fo, v.tag, fl);
        ap.k0 = at_load(v, 1);
      }
      if (s == 28) ap.k1 = at_load(v, 2);
      if (s == 29) ap.v0 = at_load(v, 3);
      if (s == 30) ap.v1 = at_load(v, 4);
      if (s == 31) ap.q = at_load(v, 0);
    }
    SBAR();
    if (i == 7) {
      STAMP2(SB + kq);
      if (!FIRST && kq == 2) wait_bar<63, 8>();
      else if (!FIRST && kq == 3) {}
      else if (kq == 3 && KIND != K_D0) wait_bar<4, 0>();
      else if (Q && kq == 1) wait_bar<4, 4>();
      else wait_bar<4, 8>();
      STAMP2(SB + 18 + kq);
      if (FIRST && s == 7) {
        rd_step<Q>(a, B, 8, 8 % 3);
        rd_step<Q>(a, B, 9, 9 % 3);
      }
    }
  });
  if (Q) p0 = a1;
  else { p0 = a0; p1 = a1; }
}
DI void ab_tail(const Cx& v, const AB& a, int dbl, const f32x16& p0, const f32x16& p1, AtPre& ap, u32x4 (&sw)[20]) {
  u32x4 hv;
  static_for<0, 8>([&](auto I_) __attribute__((always_inline)) {
    constexpr int i = decltype(I_)::v, j = 2 * (i & 3);
    const f32x16& p = i < 4 ? p0 : p1;
    if constexpr (i < 5) sw[i] = c_stash(v, i);
    hv[i & 3] = pk(geglu(p[j], p[8 + j], a.k1, a.k2, a.ir2), geglu(p[j + 1], p[9 + j], a.k1, a.k2, a.ir2));
    PIN(hv[i & 3]);
    if constexpr ((i & 3) == 3) put_h(v, a, dbl, i >> 2, hv);
    SBAR();
  });
  asm volatile("s_waitcnt vmcnt(7)\n\ts_barrier" : "+v"(ap.q), "+v"(ap.k0), "+v"(ap.k1), "+v"(ap.v0), "+v"(ap.v1)::"memory");
  at_stage(v, ap);
  wait_bar<1, 0>();
  PIN4(sw, 0); PIN(sw[4]);
}

template <bool TB> DI void ab_path(Cx& v, AB a, AtPre& ap, u32x4 (&sw)[20]) {
  int l = v.lane;
  PIN(l);
  a.voffX = (l >> 4) * 1024 + (((l & 15) ^ (l >> 4)) * 16);
  a.voffQ = w_voff(l, 3072);
  a.voffF = w_voff(l, 3584);
  a.sel = mk_sel(l);
  u32x2 epoch = ld64h(v.ws, WS_EPOCH), st;
  u32x4 xr;
  asm volatile("buffer_load_dwordx4 %0, %1, %2, %3 offen" : "=v"(xr) : "v"(l * 16), "s"(a.rx), "s"((a.rb * 128 + a.cg * 8 + v.w) * 1024) : "memory");
  f32x16 p0 = {}, p1;
  prologue<TB>(v, a, p0, epoch, xr, st);
  STAMP(2);
  body<TB ? K_QQ : K_D0>(v, a, p0, p1, ap, st);
  STAMP(3);
  body<TB ? K_D0L : K_D1L>(v, a, p0, p1, ap, st);
  STAMP(4);
  ab_tail(v, a, a.dblB, p0, p1, ap, sw);
}

__global__ __launch_bounds__(512, 2) void fused_kernel(Params P) {
  extern __shared__ __attribute__((aligned(16))) char smem[];
  Cx v;
  v.L = (LAS char*)smem;
  v.lane = threadIdx.x & 63;
  v.w = __builtin_amdgcn_readfirstlane(threadIdx.x >> 6);
  v.g = v.w & 3;
  v.c = v.w >> 2;
  const int xc = blockIdx.x & 7, idx = blockIdx.x >> 3;
  v.ws = mk_rsrc(P.ws, WS_END);
  v.rwd = mk_rsrc(P.wd, 1792 * 512 * 2);
  v.rwo = mk_rsrc(P.wo, 512 * 512 * 2);
  v.rt = v.hd = xc * 4 + (idx & 3);
  v.ct = v.G = idx >> 2;
  AtPre ap;
  u32x4 sw[20];
  STAMPR(30); STAMP(0);
  {
    AB a;
    a.rb = (xc & 1) * 8 + (idx & 7);
    const int cq = xc >> 1, j = idx >> 3;
    const bool typeB = j == 3;
    a.cg = cq * 4 + j;
    a.gr = v.g ^ (2 * v.c);

    a.strip0 = 6 * cq + j;
    const int swp = typeB ? 0 : (a.rb >> 2) & 1;
    a.dblA = 7 * cq + 2 * j + swp;
    a.dblB = typeB ? a.dblA : 7 * cq + 2 * j + 1 - swp;
    a.kx = a.rb & 3;
    a.rx = mk_rsrc(P.x, 2048 * 512 * 2);
    a.rq = mk_rsrc(P.wqkv, 512 * 1536 * 2);
    a.rf = mk_rsrc(v.c ? P.wu : P.wg, 512 * 1792 * 2);
    a.ru = mk_rsrc(P.wu, 512 * 1792 * 2);
    a.voffX = (v.lane >> 4) * 1024 + (((v.lane & 15) ^ (v.lane >> 4)) * 16);
    a.voffQ = w_voff(v.lane, 3072);
    STAMP2(24);
#pragma unroll
    for (int i = 0; i < 4; ++i) x_piece(v, a, 0, 3, i);
#pragma unroll
    for (int i = 0; i < 2; ++i) q4_piece(v, a, 0, 2 * SLOT, i);
#pragma unroll
    for (int i = 0; i < 4; ++i) x_piece(v, a, 1, 0, i);
#pragma unroll
    for (int i = 0; i < 2; ++i) q4_piece(v, a, 1, 2 * SLOT + SUB1, i);
    if (typeB) ab_path<true>(v, a, ap, sw);
    else ab_path<false>(v, a, ap, sw);
  }
  STAMP(6);
  c_setup(v);
  v.tag = u32x2{uni(v.tag.x), uni(v.tag.y)};
#pragma unroll
  for (int i = 0; i < 3; ++i) c_piece(v, 3, 4, i);
  const unsigned hflag = uni(WS_FH + (((xc & 1) * 8 + (idx & 7)) * 16 + (xc >> 1) * 4 + (idx >> 3)) * 64 + v.w * 8);
  u32x4 hf[28];
  if (v.G) at_body<true>(v, ap.q, hflag, hf, sw);
  else at_body<false>(v, ap.q, hflag, hf, sw);
  STAMP(7);
  c_setup(v);
  phase_c(P, v, hf, sw, blockIdx.x == 0);
}
}

extern "C" void kernel_launch(void* const* d_in, const int* in_sizes, int n_in, void* d_out, int out_size, void* d_ws,
                              size_t ws_size, hipStream_t stream) {
  Params P;
  P.x = d_in[0]; P.wqkv = d_in[1]; P.wo = d_in[2];
  P.wg = d_in[4]; P.wu = d_in[5]; P.wd = d_in[6];
  P.out = d_out; P.ws = d_ws;
  (void)hipFuncSetAttribute((const void*)fused_kernel, hipFuncAttributeMaxDynamicSharedMemorySize, LDS_BYTES);
  fused_kernel<<<256, 512, LDS_BYTES, stream>>>(P);
}
```

```cpp
#include <hip/hip_runtime.h>
#include <hip/hip_bf16.h>
#include <stdint.h>

typedef short bf16x8 __attribute__((ext_vector_type(8)));
typedef short s16x4 __attribute__((ext_vector_type(4)));
typedef float f32x16 __attribute__((ext_vector_type(16)));
typedef float f32x4 __attribute__((ext_vector_type(4)));
typedef float f32x2 __attribute__((ext_vector_type(2)));
typedef unsigned u32x4 __attribute__((ext_vector_type(4)));
typedef unsigned u32x2 __attribute__((ext_vector_type(2)));
typedef __bf16 bf2 __attribute__((ext_vector_type(2)));
typedef __amdgpu_buffer_rsrc_t rsrc_t;
#define LAS __attribute__((address_space(3)))
#define DI __device__ __forceinline__
#define SBAR() __builtin_amdgcn_sched_barrier(0)
#define PIN(x) asm volatile("" : "+v"(x))
#define RFL(x) __builtin_amdgcn_readfirstlane(x)
#define MFMA(a, b, c) __builtin_amdgcn_mfma_f32_32x32x16_bf16((a), (b), (c), 0, 0, 0)

#define STAMP(i)
#define STAMPR(i)
#define STAMP_FLUSH()
#define STAMP2(i)

#define KXM 3
#define SWP 0
#define HPOLL 6
namespace {
constexpr unsigned WS_EPOCH = 0;
constexpr unsigned WS_FQ = 1024;
constexpr unsigned WS_FH = WS_FQ + 16 * 24 * 64;
constexpr unsigned WS_FO = WS_FH + 16 * 16 * 64;
constexpr unsigned WS_ST = 65536;
constexpr unsigned WS_QKV = 131072;
constexpr unsigned SEC_BYTES = 32 * 64 * 1024;
constexpr unsigned WS_H = WS_QKV + 3 * SEC_BYTES;
constexpr unsigned WS_O = WS_H + 64 * 112 * 1024;
constexpr unsigned WS_END = WS_O + 64 * 32 * 1024;
static_assert(WS_FO + 256 * 64 <= WS_ST, "flags overlap");
constexpr int SLOT = 32800, SUB1 = 16416;
constexpr int AUX = 4 * SLOT;
constexpr int W1A = AUX + 8192;
constexpr int JUNK = AUX + 6144;
constexpr int REG = 32768;
constexpr int LDS_BYTES = 163840;
constexpr int KV = 3 * REG;
constexpr float QSCALE = 0.25f * 1.4426950408889634f;
constexpr unsigned SPIN_CAP = 1u << 18;

struct Params {
  const void *x, *wqkv, *wo, *wg, *wu, *wd;
  void *out, *ws;
};

template <int I> struct IC { static constexpr int v = I; };
template <int I, int N, class F> DI void static_for(F&& f) {
  if constexpr (I < N) {
    f(IC<I>{});
    static_for<I + 1, N>(f);
  }
}
template <int VM> DI void wait_vm() { asm volatile("s_waitcnt vmcnt(%0)" ::"n"(VM) : "memory"); }
template <int VM, int LG> DI void wait_bar() { asm volatile("s_waitcnt vmcnt(%0) lgkmcnt(%1)\n\ts_barrier" ::"n"(VM), "n"(LG) : "memory"); }
DI unsigned uni(unsigned x) {
  x = __builtin_amdgcn_readfirstlane(x);
  asm volatile("" : "+s"(x));
  return x;
}
DI rsrc_t mk_rsrc(const void* p, unsigned bytes) { return __builtin_amdgcn_make_buffer_rsrc((void*)p, 0, bytes, 0x00020000); }
DI void glds(rsrc_t rs, unsigned voff, unsigned soff, unsigned lds) {
  asm volatile("s_mov_b32 m0, %2\n\ts_nop 0\n\tbuffer_load_dwordx4 %0, %1, %3 offen lds" ::"v"(voff), "s"(rs),
               "s"(RFL(lds)), "s"(RFL(soff)) : "memory");
}
template <int IMM = 0> DI u32x4 ld128h(rsrc_t rs, unsigned voff, unsigned soff) {
  u32x4 r;
  asm volatile("buffer_load_dwordx4 %0, %1, %2, %3 offen offset:%4 sc1" : "=v"(r) : "v"(voff), "s"(rs), "s"(RFL(soff)), "n"(IMM) : "memory");
  return r;
}
DI u32x2 ld64h(rsrc_t rs, unsigned voff) {
  u32x2 r;
  asm volatile("buffer_load_dwordx2 %0, %1, %2, 0 offen sc1" : "=v"(r) : "v"(voff), "s"(rs) : "memory");
  return r;
}
DI void st128h(rsrc_t rs, unsigned voff, unsigned soff, u32x4 d) {
  asm volatile("buffer_store_dwordx4 %0, %1, %2, %3 offen sc1\n\ts_nop 1" ::"v"(d), "v"(voff), "s"(rs), "s"(RFL(soff)) : "memory");
}
DI void put_flag(rsrc_t ws, int lane, unsigned soff, u32x2 tag) {
  asm volatile("buffer_store_dwordx2 %0, %1, %2, %3 offen sc1" ::"v"(tag), "v"(lane ? 0x80000000u : 0u), "s"(ws), "s"(RFL(soff)) : "memory");
}
DI void check_flags(rsrc_t ws, unsigned voff, u32x2 tag, u32x2 v) {
  unsigned cnt;
  asm volatile(
      "s_mov_b32 %1, %5\n"
      "1:\n\t"
      "v_cmp_ne_u64 vcc, %2, %0\n\t"
      "s_cbranch_vccz 2f\n\t"
      "s_sleep 1\n\t"
      "buffer_load_dwordx2 %0, %3, %4, 0 offen sc1\n\t"
      "s_waitcnt vmcnt(0)\n\t"
      "s_sub_u32 %1, %1, 1\n\t"
      "s_cbranch_scc0 1b\n"
      "2:"
      : "+v"(v), "=&s"(cnt)
      : "s"(tag), "v"(voff), "s"(ws), "n"(SPIN_CAP)
      : "vcc", "scc", "memory");
}
DI void check_granule(rsrc_t ws, unsigned voff, unsigned tag, u32x2& v) {
  unsigned cnt, lo = v.x, hi = v.y;
  asm volatile(
      "s_mov_b32 %2, %6\n"
      "1:\n\t"
      "v_cmp_ne_u32 vcc, %3, %1\n\t"
      "s_cbranch_vccz 2f\n\t"
      "s_sleep 1\n\t"
      "buffer_load_dword %1, %4, %5, 0 offen offset:4 sc1\n\t"
      "s_waitcnt vmcnt(0)\n\t"
      "buffer_load_dword %0, %4, %5, 0 offen sc1\n\t"
      "s_waitcnt vmcnt(0)\n\t"
      "s_sub_u32 %2, %2, 1\n\t"
      "s_cbranch_scc0 1b\n"
      "2:"
      : "+v"(lo), "+v"(hi), "=&s"(cnt)
      : "s"(tag), "v"(voff), "s"(ws), "n"(SPIN_CAP)
      : "vcc", "scc", "memory");
  v.x = lo;
}
template <int CTRL> DI float dpp_add(float v) {
  return v + __builtin_bit_cast(float, __builtin_amdgcn_update_dpp(0, __builtin_bit_cast(int, v), CTRL, 0xf, 0xf, true));
}
DI float wave_sum63(float v) {
  v = dpp_add<0x111>(v); v = dpp_add<0x112>(v); v = dpp_add<0x114>(v); v = dpp_add<0x118>(v);
  v = dpp_add<0x142>(v);
  return dpp_add<0x143>(v);
}
DI s16x4 trd(LAS const char* p) { return __builtin_amdgcn_ds_read_tr16_b64_v4i16((LAS s16x4*)p); }
template <int STEP> DI bf16x8 rdW(LAS const char* p) {
  const s16x4 a = trd(p), b = trd(p + STEP);
  return __builtin_shufflevector(a, b, 0, 1, 2, 3, 4, 5, 6, 7);
}
DI unsigned pk(float lo, float hi) {
  const f32x2 v = {lo, hi};
  return __builtin_bit_cast(unsigned, __builtin_convertvector(v, bf2));
}
DI float rbf(float v) { return __builtin_bit_cast(float, pk(0.f, v) & 0xffff0000u); }
#define COLSUM(w, cs) cs = __builtin_amdgcn_mfma_f32_16x16x32_bf16(a.sel, (w), cs, 0, 0, 0)
DI bf16x8 mk_sel(int l) {
  const unsigned sl = ((l ^ (l >> 4)) & 1) ? 0u : 0x3f803f80u;
  return __builtin_bit_cast(bf16x8, u32x4{sl, sl, sl, sl});
}
DI float swap32_max(float m) {
  auto r = __builtin_amdgcn_permlane32_swap(__float_as_uint(m), __float_as_uint(m), false, false);
  return __builtin_fmaxf(__uint_as_float(r[0]), __uint_as_float(r[1]));
}
DI float swap32_add(float m) {
  auto r = __builtin_amdgcn_permlane32_swap(__float_as_uint(m), __float_as_uint(m), false, false);
  return __uint_as_float(r[0]) + __uint_as_float(r[1]);
}
DI f32x16 splat(float v) {
  f32x16 r;
#pragma unroll
  for (int i = 0; i < 16; ++i) r[i] = v;
  return r;
}
DI float cs_get(LAS const char* csp) {
  LAS const float* p = (LAS const float*)csp;
  return (p[0] + p[64]) + (p[128] + p[192]);
}
DI void ln_fix(f32x16& acc, float c, unsigned nm) {
  const unsigned u0 = __float_as_uint(c);
  const float r1 = c - __uint_as_float(u0 & 0xffff0000u);
  const unsigned u1 = __float_as_uint(r1);
  const float r2 = r1 - __uint_as_float(u1 & 0xffff0000u);
  const u32x2 a = {(u0 >> 16) | (u1 & 0xffff0000u), __float_as_uint(r2) >> 16};
  acc = __builtin_amdgcn_mfma_f32_32x32x8bf16_1k(__builtin_bit_cast(s16x4, a), __builtin_bit_cast(s16x4, u32x2{nm, nm}), acc, 0, 0, 0);
}
DI float geglu(float a, float b, float k1, float k2, float ir2) {
  const float e = __builtin_amdgcn_exp2f(a * __builtin_fmaf(a * a, k2, k1));
  return a * b * __builtin_amdgcn_rcpf(__builtin_fmaf(e, ir2, ir2));
}

DI unsigned w_voff(int lane, int pitch) {
  PIN(lane);
  const int r8 = lane >> 3, p8 = lane & 7;
  return r8 * pitch + (((p8 >> 1) ^ (r8 & 2)) * 32) + (p8 & 1) * 16;
}
struct Cx {
  int lane, w, g, c;
  int rt, ct, rot;
  int hd, G;
  unsigned voffC;
  unsigned aoff;
  unsigned cbd, cbo, hb, ob;
  rsrc_t ws, rwd, rwo;
  u32x2 tag;
  LAS char* L;
};
DI void c_setup(Cx& v) {
  v.voffC = w_voff(v.lane, 1024);
  v.cbd = uni((448 * v.g + 32 * v.c) * 1024 + v.ct * 128);
  v.cbo = uni((128 * v.g + 32 * v.c) * 1024 + v.ct * 128);
  v.hb = uni(WS_H + (2 * v.rt * 112 + 28 * v.g) * 1024);
  v.ob = uni(WS_O + (2 * v.rt * 32 + 8 * v.g) * 1024);
  v.rot = uni((v.rt * 2 + v.ct) % 7);
  int l = v.lane;
  PIN(l);
  v.aoff = v.c ? 0x40000000u : l * 16;
}
DI int c_grp(const Cx& v, int gp) {
  if (gp >= 7) return (gp - 7) ^ (v.rot & 1);
  const int r = gp + v.rot;
  return r >= 7 ? r - 7 : r;
}
DI unsigned c_voff(const Cx& v, int gp, int i) { return v.voffC + (c_grp(v, gp) << 16) + i * 8192; }
DI unsigned c_dst(const Cx& v, int region, int i) { return region * REG + v.g * 8192 + v.c * 4096 + i * 1024; }
DI void c_piece(const Cx& v, int gp, int region, int i) {
  glds(gp < 7 ? v.rwd : v.rwo, c_voff(v, gp, i), gp < 7 ? v.cbd : v.cbo, c_dst(v, region, i));
}

DI u32x4 w_park(const Cx& v, int n) {
  u32x4 r;
  asm volatile("buffer_load_dwordx4 %0, %1, %2, %3 offen"
               : "=v"(r) : "v"(c_voff(v, 4 + (n >> 3), n & 7) | (v.aoff & 0x40000000u)), "s"(v.rwd), "s"(v.cbd) : "memory");
  return r;
}

struct AtPre { u32x4 q, k0, k1, v0, v1; };
DI int at_kb(const Cx& v) {
  const int kb = 8 * v.G - 8 + 2 * v.w;
  return kb < 0 ? 0 : kb;
}
DI u32x4 at_load(const Cx& v, int i) {
  const int blk = i == 0 ? 8 * v.G + v.w : at_kb(v) + ((i - 1) & 1);
  return ld128h(v.ws, v.lane * 16, WS_QKV + ((i + 1) >> 1) * SEC_BYTES + (v.hd * 64 + blk) * 1024);
}
DI unsigned at_flag_off(const Cx& v) {
  const int i = v.lane & 7, isq = i >> 2;
  const int blk = at_kb(v) + (i & 1) + isq * (8 * v.G + v.w - at_kb(v) - (i & 1));
  const int sec = (1 - isq) * (1 + ((i >> 1) & 1));
  return WS_FQ + ((blk >> 2) * 24 + sec * 8 + (v.hd >> 2)) * 64 + (((v.hd >> 1) & 1) * 4 + (blk & 3)) * 8;
}
DI void at_stage(const Cx& v, const AtPre& a) {
  LAS char* kd = v.L + KV + 2 * v.w * 1024 + v.lane * 16;
  LAS char* vd = v.L + KV + 16384 + 2 * v.w * 1024 + (v.lane & 31) * 32 + (v.lane >> 5) * 16;
  *(LAS u32x4*)kd = v.w == 0 ? u32x4{0x3f803f80u, 0x3f803f80u, 0x3f803f80u, 0x3f803f80u} : a.k0;
  *(LAS u32x4*)(kd + 1024) = a.k1;
  *(LAS u32x4*)vd = a.v0;
  *(LAS u32x4*)(vd + 1024) = a.v1;
}
DI void max16(const f32x16& s, float& m, float& m2) {
#pragma unroll
  for (int r = 0; r < 16; r += 4) {
    m = __builtin_fmaxf(__builtin_fmaxf(m, s[r]), s[r + 1]);
    m2 = __builtin_fmaxf(__builtin_fmaxf(m2, s[r + 2]), s[r + 3]);
  }
}
DI void exp_pack(const f32x16& s, u32x4& p0, u32x4& p1) {
#pragma unroll
  for (int i = 0; i < 4; ++i) {
    p0[i] = pk(__builtin_amdgcn_exp2f(s[2 * i]), __builtin_amdgcn_exp2f(s[2 * i + 1]));
    p1[i] = pk(__builtin_amdgcn_exp2f(s[8 + 2 * i]), __builtin_amdgcn_exp2f(s[9 + 2 * i]));
  }
}
#define PIN4(a, o) asm volatile("" : "+v"(a[o]), "+v"(a[o + 1]), "+v"(a[o + 2]), "+v"(a[o + 3]))
#define BF(x) __builtin_bit_cast(bf16x8, x)
DI void pin8(u32x4 (&a)[8]) { PIN4(a, 0); PIN4(a, 4); }
DI void o_flag(const Cx& v, int qb) { put_flag(v.ws, v.lane, WS_FO + (v.G * 32 + v.hd) * 64 + qb * 8, v.tag); }
struct AtLane {
  LAS const char *kp, *vp;
  int vstr;
  int td;
};
#define KFRAG(kb) (*(LAS const bf16x8*)(a.kp + (kb) * 1024))
#define VFRAG(kb, s) rdW<256>(a.vp + (kb) * a.vstr + (s) * 512)
DI AtLane at_lane(const Cx& v, int qb) {
  const int lane = v.lane, h = lane >> 5, qq = (lane & 15) >> 2, p = lane & 3;
  AtLane a;
  a.kp = v.L + KV + qb * 1024 + lane * 16;
  int vreal = KV + 16384 + qb * 1024 + (4 * h + qq) * 32 + (p & 1) * 16 + (p >> 1) * 8, vone = KV + (lane & 15) * 8;
  PIN(vreal); PIN(vone);
  a.vp = v.L + ((lane & 16) ? vone : vreal);
  a.vstr = (lane & 16) ? 0 : 1024;
  a.td = (lane & 31) - 4 * h;
  return a;
}
DI f32x16 at_merge(const AtLane& a, const f32x16& sd, const f32x16& sf) {
  f32x16 r;
#pragma unroll
  for (int i = 0; i < 16; ++i) r[i] = (i & 3) + 8 * (i >> 2) <= a.td ? sd[i] : sf[i];
  return r;
}
DI u32x4 at_mask(const AtLane& a, int half) {
  u32x4 k;
#pragma unroll
  for (int i = 0; i < 4; ++i) {
    const int r0 = 2 * (i & 1) + 8 * (i >> 1) + 16 * half;
    k[i] = (r0 <= a.td ? 0xffffu : 0u) | (r0 + 1 <= a.td ? 0xffff0000u : 0u);
  }
  return k;
}
DI void o_store(const Cx& v, int qb, const f32x16& o) {
  const float il = __builtin_amdgcn_rcpf(o[8]);
  u32x4 ov;
#pragma unroll
  for (int i = 0; i < 4; ++i) ov[i] = pk(o[2 * i] * il, o[2 * i + 1] * il);
  st128h(v.ws, v.lane * 16, WS_O + ((8 * v.G + qb) * 32 + v.hd) * 1024, ov);
}
template <int GP, int N> DI u32x4 a_load(const Cx& v) {
  return ld128h<(N >> 1) * 1024>(v.ws, v.aoff + (c_grp(v, GP) << 12) + (N & 1) * (GP < 7 ? 112 : 32) * 1024, GP < 7 ? v.hb : v.ob);
}
DI void w3_half(const Cx& v, int hf) {
  if (!v.c) {
#pragma unroll
    for (int i = 4 * hf; i < 4 * hf + 4; ++i) c_piece(v, 3, 4, i);
  }
}
template <bool FULL> DI void at_body(const Cx& v, const AtPre& ap, u32x4 (&af)[4][8]) {
  const AtLane a = at_lane(v, v.w);
  const bf16x8 q = BF(ap.q);
  const float NEG = -1e30f;
  const f32x16 zero = {};
  const int th = (v.lane >> 3) & 1;
  const unsigned hpoll = WS_FH + ((v.rt >> 1) * 16 + 4 * v.g + (v.lane & 3)) * 64 + (((v.lane >> 2) & 1) * 4 + ((2 * v.rt + th) & 3)) * 8;
  float m = NEG, m2 = NEG;
  f32x16 o = zero, cm, sc;
  u32x4 p0, p1;
  u32x2 fl;
  if (FULL) {
    bf16x8 kf[9];
#pragma unroll
    for (int kb = 0; kb < 9; ++kb) kf[(kb + 8) % 9] = KFRAG((kb + 8) % 9);
    if (v.w == 0) kf[0] = BF(ap.k0);
    f32x16 sa = MFMA(kf[8], q, zero), sb = MFMA(kf[0], q, zero);
    sc = MFMA(kf[1], q, zero);
    PIN(sa); PIN(sb); PIN(sc);
    SBAR();
    w3_half(v, 0);
    max16(at_merge(a, sa, sb), m, m2);
    static_for<1, 8>([&](auto K_) __attribute__((always_inline)) {
      constexpr int kb = decltype(K_)::v;
      f32x16 sn;
      if constexpr (kb < 7) { sn = MFMA(kf[kb + 1], q, zero); PIN(sn); }
      SBAR();
      if (kb == 2) w3_half(v, 1);
      if (kb == HPOLL) fl = ld64h(v.ws, hpoll);
      max16(sc, m, m2);
      PIN(m); PIN(m2);
      SBAR();
      if constexpr (kb < 7) sc = sn;
    });
    m = swap32_max(__builtin_fmaxf(m, m2));
    STAMP2(8);
    cm = splat(-m);
    sc = MFMA(kf[1], q, cm);
    bf16x8 kz = kf[0], kn = kf[2], v0 = VFRAG(1, 0), v1 = VFRAG(1, 1);
    static_for<1, 8>([&](auto K_) __attribute__((always_inline)) {
      constexpr int kb = decltype(K_)::v;
      const bf16x8 n0 = VFRAG(kb + 1, 0), n1 = VFRAG(kb + 1, 1);
      f32x16 sn;
      if constexpr (kb < 7) { sn = MFMA(kn, q, cm); PIN(sn); kn = KFRAG(kb + 2); }
      else { sa = MFMA(kn, q, cm); sb = MFMA(kz, q, cm); PIN(sa); PIN(sb); }
      SBAR();
      exp_pack(sc, p0, p1);
      o = MFMA(v0, BF(p0), o);
      o = MFMA(v1, BF(p1), o);
      PIN(o);
      if (kb == 1) {
        STAMP2(10);
        asm volatile("s_waitcnt vmcnt(0)" : "+v"(fl)::"memory");
        check_flags(v.ws, hpoll, v.tag, v.c ? v.tag : fl);
        STAMP2(11);
      }
      static_for<(kb - 1) * 3, (kb < 7 ? kb * 3 : 20)>([&](auto N_) __attribute__((always_inline)) {
          af[decltype(N_)::v >> 3][decltype(N_)::v & 7] = a_load<(decltype(N_)::v >> 3), (decltype(N_)::v & 7)>(v);
        });
      SBAR();
      if constexpr (kb < 7) sc = sn;
      v0 = n0;
      v1 = n1;
    });
    sc = at_merge(a, sa, sb);
    exp_pack(sc, p0, p1);
    const u32x4 k0 = at_mask(a, 0), k1 = at_mask(a, 1);
    o = MFMA(v0, BF(p0 & k0), o);
    o = MFMA(v1, BF(p1 & k1), o);
    o = MFMA(VFRAG(0, 0), BF(p0 & ~k0), o);
    o = MFMA(VFRAG(0, 1), BF(p1 & ~k1), o);
  } else {
    w3_half(v, 0);
    w3_half(v, 1);
    max16(at_merge(a, MFMA(KFRAG(8), q, zero), splat(NEG)), m, m2);
    for (int kb = 8 - v.w; kb < 8; ++kb) max16(MFMA(KFRAG(kb), q, zero), m, m2);
    cm = splat(-swap32_max(__builtin_fmaxf(m, m2)));
    for (int kb = 8 - v.w; kb < 9; ++kb) {
      sc = MFMA(KFRAG(kb), q, cm);
      if (kb == 8) sc = at_merge(a, sc, splat(NEG));
      exp_pack(sc, p0, p1);
      o = MFMA(VFRAG(kb, 0), BF(p0), o);
      o = MFMA(VFRAG(kb, 1), BF(p1), o);
    }
    PIN(o);
    check_flags(v.ws, hpoll, v.tag, v.c ? v.tag : u32x2{~v.tag.x, 0u});
    static_for<0, 20>([&](auto N_) __attribute__((always_inline)) {
      af[decltype(N_)::v >> 3][decltype(N_)::v & 7] = a_load<(decltype(N_)::v >> 3), (decltype(N_)::v & 7)>(v);
    });
  }
  o_store(v, v.w, o);
}

DI void c_scatter2(const Cx& v, LAS char* X, const f32x16& a, int nb, int j0) {
  const int t = v.lane & 31;
  LAS char* row = X + v.g * 8192 + t * 256;
  const int sw = ((v.lane >> 5) ^ (t & 15)) * 16;
#pragma unroll
  for (int j = j0; j < j0 + 2; ++j) *(LAS f32x4*)(row + (sw ^ ((4 * nb + j) * 32))) = f32x4{a[4 * j], a[4 * j + 1], a[4 * j + 2], a[4 * j + 3]};
}
DI LAS f32x4* c_cell(const Cx& v, LAS char* X, int half) {
  const int t = 8 * v.g + (v.lane >> 3);
  return (LAS f32x4*)(X + t * 256 + (((2 * (v.lane & 7) + half) ^ (t & 15)) * 16));
}
DI f32x4 c_gather(const Cx& v, LAS char* X, int half) {
  LAS const f32x4* src = c_cell(v, X, half);
  return (src[0] + src[512]) + (src[1024] + src[1536]);
}
DI f32x4 silu4(f32x4 d) {
#pragma unroll
  for (int i = 0; i < 4; ++i) d[i] *= __builtin_amdgcn_rcpf(1.f + __builtin_amdgcn_exp2f(-1.4426950408889634f * d[i]));
  return d;
}
DI u32x2 fin4(u32x2 x, f32x4 s, f32x4 g) {
  return u32x2{pk(__builtin_fmaf(s[0], g[0], __uint_as_float(x.x << 16)), __builtin_fmaf(s[1], g[1], __uint_as_float(x.x & 0xffff0000u))),
               pk(__builtin_fmaf(s[2], g[2], __uint_as_float(x.y << 16)), __builtin_fmaf(s[3], g[3], __uint_as_float(x.y & 0xffff0000u)))};
}
DI LAS char* c_xd(const Cx& v, int half) { return v.L + (1 + half) * REG; }
DI LAS char* c_xs(const Cx& v, int half) { return v.L + (half ? 0 : 4 * REG); }
DI unsigned c_xoff(const Cx& v) { return (64 * v.rt + 8 * v.w + (v.lane >> 3)) * 1024 + v.ct * 128 + (v.lane & 7) * 16; }
DI u32x4 x_load(const Params& P, const Cx& v) {
  u32x4 r;
  asm volatile("buffer_load_dwordx4 %0, %1, %2, 0 offen" : "=v"(r) : "v"(c_xoff(v)), "s"(mk_rsrc(P.x, 2048 * 512 * 2)) : "memory");
  return r;
}
DI void gemm_role(const Params& P, const Cx& v, u32x4 (&af)[4][8], u32x4 (&wp)[8], u32x4& xv) {
  const int lane = v.lane, h = lane >> 5;
  const int qq = (lane & 15) >> 2, p = lane & 3, g1 = (lane >> 4) & 1;
  LAS const char* b0 = v.L + v.g * 8192 + (4 * h + qq) * 128 + (g1 + (qq & 2)) * 32 + 8 * p;
  LAS const char* b1 = v.L + v.g * 8192 + (4 * h + qq) * 128 + (g1 + (2 ^ (qq & 2))) * 32 + 8 * p;
  const unsigned opoll = WS_FO + ((v.rt >> 2) * 32 + 8 * v.g + (lane & 7)) * 64 + ((2 * v.rt + ((lane >> 3) & 1)) & 7) * 8;
  constexpr int RG[4] = {0, 1, 2, 4};
  f32x16 d[2][2] = {}, sm[2][2] = {};
  bf16x8 wf[4][2];
  u32x2 fl;
  auto rd = [&](auto S_) __attribute__((always_inline)) {
    constexpr int s = decltype(S_)::v;
    wf[s & 3][0] = rdW<1024>(b0 + RG[(s >> 2) & 3] * REG + (s & 3) * 2048);
    wf[s & 3][1] = rdW<1024>(b1 + RG[(s >> 2) & 3] * REG + (s & 3) * 2048);
  };
  auto areq = [&](auto G_, auto N_) __attribute__((always_inline)) {
    af[decltype(G_)::v & 3][decltype(N_)::v] = a_load<decltype(G_)::v, decltype(N_)::v>(v);
  };
  rd(IC<0>{});
  rd(IC<1>{});
  wait_vm<13>();
  pin8(af[0]);
  static_for<0, 32>([&](auto S_) __attribute__((always_inline)) {
    constexpr int s = decltype(S_)::v, gp = s >> 2, k = s & 3;
    if (k == 0) STAMP2(40 + gp);
    if constexpr (k == 2) {
      constexpr int N[8] = {8, 9, 16, 24, 8, 63, 16, 1};
      if constexpr (gp == 4) {
        asm volatile("s_waitcnt vmcnt(8)" : "+v"(fl)::"memory");
        check_flags(v.ws, opoll, v.tag, fl);
      } else wait_vm<N[gp]>();
      pin8(af[(gp + 1) & 3]);
      if (gp == 0) o_flag(v, v.w);
    }
    rd(IC<s + 2>{});
    static_for<0, 4>([&](auto J_) __attribute__((always_inline)) {
      constexpr int j = decltype(J_)::v, n = 4 * k + j;
      f32x16& acc = (gp < 7 ? d : sm)[j >> 1][j & 1];
      acc = MFMA(wf[s & 3][j >> 1], BF(af[gp & 3][2 * k + (j & 1)]), acc);
      PIN(acc);
      SBAR();
      if constexpr (gp == 0 && n < 12) areq(IC<2 + ((n + 4) >> 3)>{}, IC<(n + 4) & 7>{});
      if constexpr (gp == 1 && n < 8) *(LAS u32x4*)(v.L + c_dst(v, 0, n) + lane * 16) = wp[n];
      if constexpr (gp >= 2 && gp < 6 && n < 8) c_piece(v, gp + 3, RG[(gp + 3) & 3], n);
      if constexpr (gp >= 1 && gp < 6 && n >= 8) areq(IC<gp + 3>{}, IC<n - 8>{});
      if (s == 15 && j == 3) fl = ld64h(v.ws, opoll);
      if (s == 27 && j == 0) xv = x_load(P, v);
      if constexpr (gp == 7 && n < 8) c_scatter2(v, c_xd(v, n >> 2), d[(n >> 1) & 1][n >> 2], (n >> 1) & 1, (n & 1) * 2);
      SBAR();
    });
  });
  STAMP2(48);
  wait_bar<63, 8>();
  rd(IC<34>{});
  rd(IC<35>{});
  static_for<0, 5>([&](auto J_) __attribute__((always_inline)) {
    constexpr int j = decltype(J_)::v;
    static_for<0, 4>([&](auto K_) __attribute__((always_inline)) {
      constexpr int k = decltype(K_)::v;
      if constexpr (j < 4) {
        sm[j & 1][j >> 1] = MFMA(wf[k][j & 1], BF(af[0][2 * k + (j >> 1)]), sm[j & 1][j >> 1]);
        PIN(sm[j & 1][j >> 1]);
        SBAR();
      }
      if constexpr (j > 0 && (k & 1)) c_scatter2(v, c_xs(v, (j - 1) >> 1), sm[(j - 1) & 1][(j - 1) >> 1], (j - 1) & 1, k - 1);
      SBAR();
    });
  });
  STAMP(14);
}
DI void side_role(const Params& P, const Cx& v, f32x4& ga, f32x4& gb, u32x4& xv) {
  wait_vm<0>();
  o_flag(v, v.w);
  xv = x_load(P, v);
  if (blockIdx.x == 0) check_flags(v.ws, WS_FO + (v.g * 64 + v.lane) * 64, v.tag, u32x2{~v.tag.x, 0u});
  wait_bar<63, 0>();
#pragma unroll
  for (int hf = 0; hf < 2; ++hf) *c_cell(v, c_xd(v, 0), hf) = silu4(c_gather(v, c_xd(v, 0), hf));
  ga = silu4(c_gather(v, c_xd(v, 1), 0));
  gb = silu4(c_gather(v, c_xd(v, 1), 1));
}

struct AB {
  bf16x8 X[32];
  bf16x8 wf[3][2];
  bf16x8 sel;
  unsigned nm;
  float r, k1, k2, ir2;
  int gr;
  int rb, cg, strip0, dblA, dblB;
  int kx;
  unsigned voffX, voffQ, voffF;
  rsrc_t rx, rq, rf;
};
DI void x_piece(const Cx& v, const AB& a, int kc, int slot, int i) {
  glds(a.rx, a.voffX ^ (64 * i), (a.rb * 128 + 16 * v.w + 4 * i) * 1024 + (kc ^ a.kx) * 256, slot * SLOT + (4 * v.w + i) * 1024);
}
DI void s_piece(const Cx& v, const AB& a, int kq, int i) {
  glds(a.rq, a.voffQ, (128 * (kq ^ a.kx) + 16 * v.w + 8 * i) * 3072 + a.strip0 * 128, W1A + (2 * v.w + i) * 1024);
}
DI void f_piece(const Cx& v, const AB& a, int dbl, int kq, int slot, int i) {
  glds(a.rf, a.voffF, (128 * (kq ^ a.kx) + 32 * v.g + 8 * i) * 3584 + dbl * 128, slot * SLOT + v.c * SUB1 + v.g * 4096 + i * 1024);
}
template <bool TB> DI void u_piece(const Cx& v, const AB& a, int kq, int i) {
  const int slot = 1 + 2 * (kq & 1);
  if (TB) glds(a.rq, a.voffQ, (128 * (kq ^ a.kx) + 32 * v.g + 8 * i) * 3072 + (a.strip0 + 1 + v.c) * 128,
               slot * SLOT + v.c * SUB1 + v.g * 4096 + i * 1024);
  else f_piece(v, a, a.dblA, kq, slot, i);
}
DI LAS char* cs_slot(const Cx& v, int j) { return v.L + AUX + (j * 2 + v.c) * 1024 + v.lane * 4; }
DI void cs_put(const Cx& v, int j, const f32x4& c) { *(LAS float*)(cs_slot(v, j) + v.g * 256) = (v.lane & 16) ? c[1] : c[0]; }
DI void q_store(const Cx& v, const AB& a, const f32x16& acc, int strip, int hh) {
  const float sc = strip < 8 ? a.r * QSCALE : a.r;
  u32x4 o;
#pragma unroll
  for (int i = 0; i < 4; ++i) o[i] = pk(acc[8 * hh + 2 * i] * sc, acc[8 * hh + 2 * i + 1] * sc);
  st128h(v.ws, v.lane * 16, WS_QKV + ((strip * 4 + 2 * v.c + hh) * 64 + 4 * a.rb + v.g) * 1024, o);
}
DI void q_flag(const Cx& v, const AB& a, int strip) { put_flag(v.ws, v.lane, WS_FQ + (a.rb * 24 + strip) * 64 + v.w * 8, v.tag); }
DI void put_h(const Cx& v, const AB& a, int dbl, int j, const u32x4& hv) {
  st128h(v.ws, v.lane * 16, WS_H + ((4 * a.rb + v.g) * 112 + 4 * dbl + 2 * v.c + j) * 1024, hv);
}
struct Bases { unsigned b[4]; };
DI Bases mk_bases(const Cx& v, const AB& a, int g1mul) {
  int l = v.lane;
  PIN(l);
  const int qq = (l & 15) >> 2;
  const int o = ((l >> 4) & 1) * g1mul + (8 * (l >> 5) + qq) * 128 + ((2 * v.c) ^ (qq & 2)) * 32 + 8 * (l & 3);
  Bases r;
#pragma unroll
  for (int i = 0; i < 4; ++i) { r.b[i] = o + (i ^ a.gr) * 4096; PIN(r.b[i]); }
  return r;
}
DI void bump(Bases& B, int d) {
#pragma unroll
  for (int i = 0; i < 4; ++i) { B.b[i] += d; PIN(B.b[i]); }
}
#define WRD(B, off, i) rdW<512>((LAS const char*)(size_t)((B).b[(i) >> 1]) + (off) + ((i) & 1) * 2048)

DI u32x2 make_tag(u32x2 e) {
  const unsigned long long z = ((unsigned long long)__builtin_amdgcn_readfirstlane(e.y) << 32 | (unsigned)__builtin_amdgcn_readfirstlane(e.x));
  const unsigned long long t = (z ^ (z >> 31)) * 0x9E3779B97F4A7C15ull + 0xD1B54A32D192ED03ull;
  return u32x2{(unsigned)t, (unsigned)(t >> 32)};
}

DI void stats_sum(const u32x4& xr, float& s1, float& s2) {
  s1 = s2 = 0.f;
#pragma unroll
  for (int d = 0; d < 4; ++d) {
    const float lo = __uint_as_float(xr[d] << 16), hi = __uint_as_float(xr[d] & 0xffff0000u);
    s1 += lo + hi;
    s2 = __builtin_fmaf(lo, lo, __builtin_fmaf(hi, hi, s2));
  }
}
DI void stats_put(const Cx& v, const AB& a, float s1, float s2) {
  const float mean = s1 * (1.f / 512.f);
  const float var = __builtin_fmaxf(__builtin_fmaf(-mean, mean, s2 * (1.f / 512.f)), 0.f);
  const float sd = __builtin_amdgcn_sqrtf(rbf(rbf(var) + 1e-5f));
  put_flag(v.ws, v.lane ^ 63, WS_ST + (a.rb * 128 + a.cg * 8 + v.w) * 8, u32x2{pk(-mean, sd), v.tag.x});
}
DI void row_stats(const Cx& v, AB& a, unsigned g) {
  const float sd = __uint_as_float(g & 0xffff0000u);
  const float c1 = -2.f * 0.7978845608028654f * 1.4426950408889634f;
  a.r = __builtin_amdgcn_rcpf(sd);
  a.k1 = c1 * a.r;
  a.k2 = c1 * 0.044715f * a.r * a.r * a.r;
  a.ir2 = sd * sd;
  a.nm = v.lane < 32 ? (g & 0xffffu) | (g << 16) : 0u;
}
DI void rd_step(AB& a, const Bases& B, int s) {
  a.wf[(s + 2) % 3][0] = WRD(B, ((s >> 3) & 1) * SLOT, s & 7);
  a.wf[(s + 2) % 3][1] = WRD(B, ((s >> 3) & 1) * SLOT + 32, s & 7);
}
template <bool TB> DI void rd_front(AB& a, const Bases& B, int i) {
  a.wf[i % 3][0] = WRD(B, 0, i);
  a.wf[i % 3][1] = WRD(B, TB ? SUB1 : 32, i);
}
template <bool TB, int Q>
DI void p_step(const Cx& v, AB& a, Bases& B, int dst, f32x16& acc, f32x4& cs) {
  const int t = v.lane & 31;
  const unsigned u = (((v.lane >> 5) ^ (t & 15)) * 16) ^ (64 * a.gr);
  LAS const char* xr = v.L + 2 * (Q & 1) * SLOT + (32 * v.g + t) * 256;
  bf16x8 wf[8];
#pragma unroll
  for (int i = 0; i < 3; ++i) {
    a.X[8 * Q + i] = *(LAS const bf16x8*)(xr + (u ^ (32 * i)));
    wf[i] = WRD(B, 0, i);
  }
#pragma unroll
  for (int i = 0; i < 8; ++i) {
    if (i < 5) {
      a.X[8 * Q + i + 3] = *(LAS const bf16x8*)(xr + (u ^ (32 * (i + 3))));
      wf[i + 3] = WRD(B, 0, i + 3);
    }
    if (i == 5) bump(B, dst + (1 + 2 * (Q & 1)) * SLOT - W1A);
    if (i >= 6 && Q > 1) rd_front<TB>(a, B, i - 6);
    if (Q == 0) { if (i < 4) u_piece<TB>(v, a, 0, i); else x_piece(v, a, 1, 2, i - 4); }
    else if ((i & 1) == 0) {
      if (Q < 3) u_piece<TB>(v, a, Q + 1, i >> 1);
      else f_piece(v, a, a.dblB, 1, 1, i >> 1);
    }
    acc = MFMA(wf[i], a.X[8 * Q + i], acc);
    if (i < 2) COLSUM(wf[i], cs);
    PIN(acc); PIN(cs);
    SBAR();
  }
}
template <bool TB, int Q>
DI void t_step(Cx& v, AB& a, Bases& B, int dst, f32x16& a0, f32x16& a1, f32x4& c0, f32x4& c1, f32x16& ps, u32x2& st, u32x4& xr) {
  float cp, s1, s2;
  static_for<0, 8>([&](auto I_) __attribute__((always_inline)) {
    constexpr int i = decltype(I_)::v;
    const bf16x8 w0 = a.wf[i % 3][0], w1 = a.wf[i % 3][1];
    a0 = MFMA(w0, a.X[8 * Q + i], a0);
    PIN(a0);
    SBAR();
    if (i < 6) rd_front<TB>(a, B, i + 2);
    if (i == 6) {
      if (Q < 3) bump(B, W1A - dst - (1 + 2 * (Q & 1)) * SLOT);
      else B = mk_bases(v, a, SUB1);
    }
    if (Q == 3 && i >= 6) rd_step(a, B, i - 6);
    if (Q == 3 && i == 0) cp = cs_get(cs_slot(v, 2));
    if ((i >> 1) == 1) { COLSUM(w0, c0); PIN(c0); }
    if (Q == 0) {
      if (i < 2) s_piece(v, a, 1, i);
      else if (i < 6) u_piece<TB>(v, a, 1, i - 2);
      else x_piece(v, a, 2, 0, i - 6);
    } else if (Q < 3) {
      if (i < 2) s_piece(v, a, Q + 1, i);
      else if (i < 6) { if (Q < 2) x_piece(v, a, Q + 2, 2 * (Q & 1), i - 2); else f_piece(v, a, a.dblB, 0, 0, i - 2); }
    } else if ((i & 1) == 0) f_piece(v, a, a.dblB, 2, 2, i >> 1);
    SBAR();
    a1 = MFMA(w1, a.X[8 * Q + i], a1);
    PIN(a1);
    SBAR();
    if ((i >> 1) == 1) { COLSUM(w1, c1); PIN(c1); }
    if (Q == 0) {
      if (i >= 6) x_piece(v, a, 2, 0, i - 4);
      if (i == 4) {
        asm volatile("s_waitcnt vmcnt(5)" : "+v"(st), "+v"(xr)::"memory");
        v.tag = make_tag(st);
        stats_sum(xr, s1, s2);
        PIN(s1); PIN(s2);
      }
      if (i == 5) { s1 = wave_sum63(s1); PIN(s1); }
      if (i == 6) { s2 = wave_sum63(s2); PIN(s2); }
      if (i == 7) stats_put(v, a, s1, s2);
    }
    if (Q == 2 && i == 6) {
      asm volatile("s_waitcnt vmcnt(6)" : "+v"(st)::"memory");
      check_granule(v.ws, WS_ST + (a.rb * 128 + 32 * v.g + (v.lane & 31)) * 8, v.tag.x, st);
      row_stats(v, a, st.x);
      PIN(a.nm); PIN(a.r); PIN(a.k1); PIN(a.k2); PIN(a.ir2);
    }
    if (Q == 1 && i == 7) {
      const int per = v.w == 1 ? 16 : 56;
      asm volatile("s_mov_b32 m0, %2\n\ts_nop 0\n\tbuffer_load_dword %0, %1, 0 offen lds"
                   ::"v"(v.lane < per && v.w < 2 ? (blockIdx.x * per + v.lane) * 128 : 0x40000000u), "s"(v.w == 0 ? v.rwd : v.rwo), "s"(JUNK)
                   : "memory");
    }
    if (Q == 3) {
      if (i == 2) { ln_fix(ps, cp, a.nm); PIN(ps); }
      if (i == 5 || i == 7) q_store(v, a, ps, a.strip0, (i - 5) >> 1);
      if (i == 5) { cs_put(v, 0, c0); cs_put(v, 1, c1); }
    }
    SBAR();
  });
}
template <bool TB> DI void front(Cx& v, AB& a, Bases& B, f32x16& a0, f32x16& a1) {
  B = mk_bases(v, a, 32);
  bump(B, W1A);
  int dst = TB ? 0 : ((v.lane >> 4) & 1) * (SUB1 - 32);
  PIN(dst);
  f32x16 ps = {};
  f32x4 cs = {}, c0 = {}, c1 = {};
  u32x2 st;
  u32x4 xr;
  STAMP2(23);
  wait_bar<0, 0>();
  STAMP(1);
  p_step<TB, 0>(v, a, B, dst, ps, cs);
  st = ld64h(v.ws, WS_EPOCH);
  asm volatile("buffer_load_dwordx4 %0, %1, %2, %3 offen"
               : "=v"(xr) : "v"(v.lane * 16), "s"(a.rx), "s"(RFL((a.rb * 128 + a.cg * 8 + v.w) * 1024)) : "memory");
  wait_bar<6, 0>(); STAMP2(16);
  rd_front<TB>(a, B, 0);
  rd_front<TB>(a, B, 1);
  t_step<TB, 0>(v, a, B, dst, a0, a1, c0, c1, ps, st, xr);
  wait_bar<8, 0>(); STAMP2(17);
  p_step<TB, 1>(v, a, B, dst, ps, cs);
  wait_bar<8, 0>(); STAMP2(18);
  rd_front<TB>(a, B, 0);
  rd_front<TB>(a, B, 1);
  t_step<TB, 1>(v, a, B, dst, a0, a1, c0, c1, ps, st, xr);
  wait_bar<5, 0>(); STAMP2(19);
  p_step<TB, 2>(v, a, B, dst, ps, cs);
  st = ld64h(v.ws, WS_ST + (a.rb * 128 + 32 * v.g + (v.lane & 31)) * 8);
  wait_bar<63, 8>(); STAMP2(20);
  t_step<TB, 2>(v, a, B, dst, a0, a1, c0, c1, ps, st, xr);
  wait_bar<4, 0>(); STAMP2(21);
  p_step<TB, 3>(v, a, B, dst, ps, cs);
  cs_put(v, 2, cs);
  wait_bar<4, 0>(); STAMP2(22);
  t_step<TB, 3>(v, a, B, dst, a0, a1, c0, c1, ps, st, xr);
  wait_bar<4, 8>();
}
template <bool TB> DI void body(Cx& v, AB& a, Bases& B, f32x16& p0, f32x16& p1, AtPre& ap) {
  f32x16 a0 = {}, a1 = {};
  f32x4 c0 = {}, c1 = {};
  float cp0, cp1, hlo;
  u32x4 hv;
  u32x2 fl;
  unsigned fo;
  static_for<0, 32>([&](auto S_) __attribute__((always_inline)) {
    constexpr int s = decltype(S_)::v, kq = s >> 3, i = s & 7;
    if (s == 2) { int l = v.lane; PIN(l); a.sel = mk_sel(l); }
    const bf16x8 w0 = a.wf[(s + 2) % 3][0], w1 = a.wf[(s + 2) % 3][1];
    constexpr bool CS = (i >> 1) == 1;
    constexpr int e = TB ? -1 : (s == 30 ? 14 : ((s & 1) && s >= 3 ? (s == 31 ? 15 : (s - 3) >> 1) : -1));
    float ga, ex;
    a0 = MFMA(w0, a.X[s], a0);
    PIN(a0);
    SBAR();
    if (s == 14) bump(B, 2 * SLOT);
    if (s < 30) rd_step(a, B, s + 2);
    if (s == 0) cp0 = cs_get(cs_slot(v, 0));
    if (s == 1) cp1 = cs_get(cs_slot(v, 1));
    if (s == 7) c_setup(v);
    if (CS) { COLSUM(w0, c0); PIN(c0); }
    if ((i & 1) == 0) {
      if (kq == 0) f_piece(v, a, a.dblB, 3, 3, i >> 1);
      else c_piece(v, kq - 1, kq - 1, i >> 1);
    }
    if constexpr (e >= 0) {
      ga = (e < 8 ? p0 : p1)[e & 7];
      ex = __builtin_amdgcn_exp2f(ga * __builtin_fmaf(ga * ga, a.k2, a.k1));
      ga *= (e < 8 ? p0 : p1)[8 + (e & 7)];
      PIN(ga); PIN(ex);
    }
    SBAR();
    a1 = MFMA(w1, a.X[s], a1);
    PIN(a1);
    SBAR();
    if (CS) { COLSUM(w1, c1); PIN(c1); }
    if constexpr (e >= 0) {
      const float hx = ga * __builtin_amdgcn_rcpf(__builtin_fmaf(ex, a.ir2, a.ir2));
      if (e & 1) { hv[(e & 7) >> 1] = pk(hlo, hx); PIN(hv[(e & 7) >> 1]); }
      else { hlo = hx; PIN(hlo); }
      if (e == 15) put_h(v, a, a.dblA, 1, hv);
    }
    if (s == 1) { ln_fix(p0, cp0, a.nm); PIN(p0); }
    if (s == 2) { ln_fix(p1, cp1, a.nm); PIN(p1); }
    if (s == 4) { wait_vm<3>(); q_flag(v, a, a.strip0); }
    if (!TB && s == 18) put_h(v, a, a.dblA, 0, hv);
    if (TB) {
      if (s == 5 || s == 7) q_store(v, a, p0, a.strip0 + 1, (s - 5) >> 1);
      if (s == 9 || s == 11) q_store(v, a, p1, a.strip0 + 2, (s - 9) >> 1);
      if (s == 16) { wait_vm<3>(); q_flag(v, a, a.strip0 + 1); q_flag(v, a, a.strip0 + 2); }
    }
    if (s == 28) {
      cs_put(v, 0, c0);
      cs_put(v, 1, c1);
      SBAR();
      wait_bar<63, 0>();
      cp0 = cs_get(cs_slot(v, 0));
      cp1 = cs_get(cs_slot(v, 1));
    }
    if (s == 30) { ln_fix(a0, cp0, a.nm); PIN(a0); }
    if (s == 31) { ln_fix(a1, cp1, a.nm); PIN(a1); }
    if (s == 23) { fo = at_flag_off(v); fl = ld64h(v.ws, fo); }
    if (s == 27) {
      asm volatile("s_waitcnt vmcnt(2)" : "+v"(fl)::"memory");
      check_flags(v.ws, fo, v.tag, fl);
      ap.k0 = at_load(v, 1);
    }
    if (s == 28) ap.k1 = at_load(v, 2);
    if (s == 29) ap.v0 = at_load(v, 3);
    if (s == 30) ap.v1 = at_load(v, 4);
    if (s == 31) ap.q = at_load(v, 0);
    SBAR();
    if (i == 7 && kq < 3) {
      STAMP2(36 + kq);
      if (kq < 2) wait_bar<4, 8>();
      else wait_bar<63, 8>();
      STAMP2(54 + kq);
    }
  });
  p0 = a0;
  p1 = a1;
}
DI void ab_tail(const Cx& v, const AB& a, int dbl, const f32x16& p0, const f32x16& p1, AtPre& ap, u32x4 (&wp)[8]) {
  u32x4 hv;
  static_for<0, 8>([&](auto I_) __attribute__((always_inline)) {
    constexpr int i = decltype(I_)::v, j = 2 * (i & 3);
    const f32x16& p = i < 4 ? p0 : p1;
    if constexpr (i < 4) { wp[2 * i] = w_park(v, 2 * i); wp[2 * i + 1] = w_park(v, 2 * i + 1); }
    hv[i & 3] = pk(geglu(p[j], p[8 + j], a.k1, a.k2, a.ir2), geglu(p[j + 1], p[9 + j], a.k1, a.k2, a.ir2));
    PIN(hv[i & 3]);
    if constexpr ((i & 3) == 3) put_h(v, a, dbl, i >> 2, hv);
    SBAR();
  });
  asm volatile("s_waitcnt vmcnt(10)\n\ts_barrier" : "+v"(ap.q), "+v"(ap.k0), "+v"(ap.k1), "+v"(ap.v0), "+v"(ap.v1)::"memory");
  at_stage(v, ap);
  wait_vm<0>();
  put_flag(v.ws, v.lane, WS_FH + (a.rb * 16 + a.cg) * 64 + v.w * 8, v.tag);
  wait_bar<63, 0>();
  PIN4(wp, 0); PIN4(wp, 4);
}

template <bool TB> DI void ab_path(Cx& v, AB a, AtPre& ap, u32x4 (&wp)[8]) {
  int l = v.lane;
  PIN(l);
  a.voffX = (l >> 4) * 1024 + (((l & 15) ^ (l >> 4)) * 16);
  a.voffQ = w_voff(l, 3072);
  a.voffF = w_voff(l, 3584);
  a.sel = mk_sel(l);
  f32x16 p0 = {}, p1 = {};
  Bases B;
  front<TB>(v, a, B, p0, p1);
  STAMP(3);
  body<TB>(v, a, B, p0, p1, ap);
  STAMP(4);
  ab_tail(v, a, a.dblB, p0, p1, ap, wp);
}

__global__ __launch_bounds__(512, 2) void fused_kernel(Params P) {
  extern __shared__ __attribute__((aligned(16))) char smem[];
  Cx v;
  v.L = (LAS char*)smem;
  v.lane = threadIdx.x & 63;
  v.w = __builtin_amdgcn_readfirstlane(threadIdx.x >> 6);
  v.g = v.w & 3;
  v.c = v.w >> 2;
  const int xc = blockIdx.x & 7, idx = blockIdx.x >> 3;
  v.ws = mk_rsrc(P.ws, WS_END);
  v.rwd = mk_rsrc(P.wd, 1792 * 512 * 2);
  v.rwo = mk_rsrc(P.wo, 512 * 512 * 2);
  v.rt = v.hd = xc * 4 + (idx & 3);
  v.ct = v.G = idx >> 2;
  AtPre ap;
  u32x4 wp[8];
  STAMPR(30); STAMP(0);
  {
    AB a;
    a.rb = (xc & 1) * 8 + (idx & 7);
    const int cq = xc >> 1, j = idx >> 3;
    const bool typeB = j == 3;
    a.cg = cq * 4 + j;
    a.gr = v.g ^ (2 * v.c);

    a.strip0 = 6 * cq + j;
    const int swp = typeB ? 0 : (a.rb >> 2) & SWP;
    a.dblA = 7 * cq + 2 * j + swp;
    a.dblB = typeB ? a.dblA : 7 * cq + 2 * j + 1 - swp;
    a.kx = a.rb & KXM;
    a.rx = mk_rsrc(P.x, 2048 * 512 * 2);
    a.rq = mk_rsrc(P.wqkv, 512 * 1536 * 2);
    a.rf = mk_rsrc(v.c ? P.wu : P.wg, 512 * 1792 * 2);
    a.voffX = (v.lane >> 4) * 1024 + (((v.lane & 15) ^ (v.lane >> 4)) * 16);
    a.voffQ = w_voff(v.lane, 3072);
    STAMP2(24);
#pragma unroll
    for (int i = 0; i < 4; ++i) x_piece(v, a, 0, 0, i);
#pragma unroll
    for (int i = 0; i < 2; ++i) s_piece(v, a, 0, i);
    if (typeB) ab_path<true>(v, a, ap, wp);
    else ab_path<false>(v, a, ap, wp);
  }
  STAMP(6);
  c_setup(v);
  v.tag = u32x2{uni(v.tag.x), uni(v.tag.y)};
  u32x4 af[4][8];
  __builtin_amdgcn_s_setprio(2);
  if (v.G) at_body<true>(v, ap, af);
  else at_body<false>(v, ap, af);
  __builtin_amdgcn_s_setprio(0);
  STAMP(7);
  c_setup(v);
  f32x4 ga, gb;
  u32x4 xv;
  if (v.c) side_role(P, v, ga, gb, xv);
  else gemm_role(P, v, af, wp, xv);
  asm volatile("s_waitcnt vmcnt(0) lgkmcnt(0)\n\ts_barrier" : "+v"(xv)::"memory");
  if (blockIdx.x == 0 && v.w == 0) put_flag(v.ws, v.lane, WS_EPOCH, v.tag);
  if (!v.c) {
    ga = *c_cell(v, c_xd(v, 0), 0);
    gb = *c_cell(v, c_xd(v, 0), 1);
  }
  const u32x2 y0 = fin4(u32x2{xv.x, xv.y}, c_gather(v, c_xs(v, v.c), 0), ga), y1 = fin4(u32x2{xv.z, xv.w}, c_gather(v, c_xs(v, v.c), 1), gb);
  __builtin_amdgcn_raw_buffer_store_b128(u32x4{y0.x, y0.y, y1.x, y1.y}, mk_rsrc(P.out, 2048 * 512 * 2), c_xoff(v), 0, 0);
  STAMP(15); STAMPR(31);
}
}

extern "C" void kernel_launch(void* const* d_in, const int* in_sizes, int n_in, void* d_out, int out_size, void* d_ws,
                              size_t ws_size, hipStream_t stream) {
  Params P;
  P.x = d_in[0]; P.wqkv = d_in[1]; P.wo = d_in[2];
  P.wg = d_in[4]; P.wu = d_in[5]; P.wd = d_in[6];
  P.out = d_out; P.ws = d_ws;
  (void)hipFuncSetAttribute((const void*)fused_kernel, hipFuncAttributeMaxDynamicSharedMemorySize, LDS_BYTES);
  fused_kernel<<<256, 512, LDS_BYTES, stream>>>(P);
}
```

```cpp
#include <hip/hip_runtime.h>
#include <hip/hip_bf16.h>
#include <stdint.h>

typedef short bf16x8 __attribute__((ext_vector_type(8)));
typedef short s16x4 __attribute__((ext_vector_type(4)));
typedef float f32x16 __attribute__((ext_vector_type(16)));
typedef float f32x4 __attribute__((ext_vector_type(4)));
typedef float f32x2 __attribute__((ext_vector_type(2)));
typedef unsigned u32x4 __attribute__((ext_vector_type(4)));
typedef unsigned u32x2 __attribute__((ext_vector_type(2)));
typedef __bf16 bf2 __attribute__((ext_vector_type(2)));
typedef __amdgpu_buffer_rsrc_t rsrc_t;
#define LAS __attribute__((address_space(3)))
#define DI __device__ __forceinline__
#define SBAR() __builtin_amdgcn_sched_barrier(0)
#define PIN(x) asm volatile("" : "+v"(x))
#define RFL(x) __builtin_amdgcn_readfirstlane(x)
#define MFMA(a, b, c) __builtin_amdgcn_mfma_f32_32x32x16_bf16((a), (b), (c), 0, 0, 0)

#define STAMP(i)
#define STAMPR(i)
#define STAMP_FLUSH()
#define STAMP2(i)

#define KXM 3
#define SWP 0
#define HPOLL 6
namespace {
constexpr unsigned WS_EPOCH = 0;
constexpr unsigned WS_FQ = 1024;
constexpr unsigned WS_FH = WS_FQ + 16 * 24 * 64;
constexpr unsigned WS_FO = WS_FH + 16 * 16 * 64;
constexpr unsigned WS_ST = 65536;
constexpr unsigned WS_QKV = 131072;
constexpr unsigned SEC_BYTES = 32 * 64 * 1024;
constexpr unsigned WS_H = WS_QKV + 3 * SEC_BYTES;
constexpr unsigned WS_O = WS_H + 64 * 112 * 1024;
constexpr unsigned WS_END = WS_O + 64 * 32 * 1024;
static_assert(WS_FO + 256 * 64 <= WS_ST, "flags overlap");
constexpr int SLOT = 32768, SUB1 = 16384;
constexpr int SBUF = 4 * SLOT;
constexpr int AUX = SBUF;
constexpr int REG = 32768;
constexpr int LDS_BYTES = 163840;
constexpr int KV = 3 * REG;
constexpr float QSCALE = 0.25f * 1.4426950408889634f;
constexpr unsigned SPIN_CAP = 1u << 18;

struct Params {
  const void *x, *wqkv, *wo, *wg, *wu, *wd;
  void *out, *ws;
};

template <int I> struct IC { static constexpr int v = I; };
template <int I, int N, class F> DI void static_for(F&& f) {
  if constexpr (I < N) {
    f(IC<I>{});
    static_for<I + 1, N>(f);
  }
}
template <int VM> DI void wait_vm() { asm volatile("s_waitcnt vmcnt(%0)" ::"n"(VM) : "memory"); }
template <int VM, int LG> DI void wait_bar() { asm volatile("s_waitcnt vmcnt(%0) lgkmcnt(%1)\n\ts_barrier" ::"n"(VM), "n"(LG) : "memory"); }
DI unsigned uni(unsigned x) {
  x = __builtin_amdgcn_readfirstlane(x);
  asm volatile("" : "+s"(x));
  return x;
}
DI rsrc_t mk_rsrc(const void* p, unsigned bytes) { return __builtin_amdgcn_make_buffer_rsrc((void*)p, 0, bytes, 0x00020000); }
DI void glds(rsrc_t rs, unsigned voff, unsigned soff, unsigned lds) {
  asm volatile("s_mov_b32 m0, %2\n\ts_nop 0\n\tbuffer_load_dwordx4 %0, %1, %3 offen lds" ::"v"(voff), "s"(rs),
               "s"(RFL(lds)), "s"(RFL(soff)) : "memory");
}
template <int IMM = 0> DI u32x4 ld128h(rsrc_t rs, unsigned voff, unsigned soff) {
  u32x4 r;
  asm volatile("buffer_load_dwordx4 %0, %1, %2, %3 offen offset:%4 sc1" : "=v"(r) : "v"(voff), "s"(rs), "s"(RFL(soff)), "n"(IMM) : "memory");
  return r;
}
DI u32x2 ld64h(rsrc_t rs, unsigned voff) {
  u32x2 r;
  asm volatile("buffer_load_dwordx2 %0, %1, %2, 0 offen sc1" : "=v"(r) : "v"(voff), "s"(rs) : "memory");
  return r;
}
DI void st128h(rsrc_t rs, unsigned voff, unsigned soff, u32x4 d) {
  asm volatile("buffer_store_dwordx4 %0, %1, %2, %3 offen sc1\n\ts_nop 1" ::"v"(d), "v"(voff), "s"(rs), "s"(RFL(soff)) : "memory");
}
DI void put_flag(rsrc_t ws, int lane, unsigned soff, u32x2 tag) {
  asm volatile("buffer_store_dwordx2 %0, %1, %2, %3 offen sc1" ::"v"(tag), "v"(lane ? 0x80000000u : 0u), "s"(ws), "s"(RFL(soff)) : "memory");
}
DI void check_flags(rsrc_t ws, unsigned voff, u32x2 tag, u32x2 v) {
  unsigned cnt;
  asm volatile(
      "s_mov_b32 %1, %5\n"
      "1:\n\t"
      "v_cmp_ne_u64 vcc, %2, %0\n\t"
      "s_cbranch_vccz 2f\n\t"
      "s_sleep 1\n\t"
      "buffer_load_dwordx2 %0, %3, %4, 0 offen sc1\n\t"
      "s_waitcnt vmcnt(0)\n\t"
      "s_sub_u32 %1, %1, 1\n\t"
      "s_cbranch_scc0 1b\n"
      "2:"
      : "+v"(v), "=&s"(cnt)
      : "s"(tag), "v"(voff), "s"(ws), "n"(SPIN_CAP)
      : "vcc", "scc", "memory");
}
DI void check_granule(rsrc_t ws, unsigned voff, unsigned tag, u32x2& v) {
  unsigned cnt, lo = v.x, hi = v.y;
  asm volatile(
      "s_mov_b32 %2, %6\n"
      "1:\n\t"
      "v_cmp_ne_u32 vcc, %3, %1\n\t"
      "s_cbranch_vccz 2f\n\t"
      "s_sleep 1\n\t"
      "buffer_load_dword %1, %4, %5, 0 offen offset:4 sc1\n\t"
      "s_waitcnt vmcnt(0)\n\t"
      "buffer_load_dword %0, %4, %5, 0 offen sc1\n\t"
      "s_waitcnt vmcnt(0)\n\t"
      "s_sub_u32 %2, %2, 1\n\t"
      "s_cbranch_scc0 1b\n"
      "2:"
      : "+v"(lo), "+v"(hi), "=&s"(cnt)
      : "s"(tag), "v"(voff), "s"(ws), "n"(SPIN_CAP)
      : "vcc", "scc", "memory");
  v.x = lo;
}
template <int CTRL> DI float dpp_add(float v) {
  return v + __builtin_bit_cast(float, __builtin_amdgcn_update_dpp(0, __builtin_bit_cast(int, v), CTRL, 0xf, 0xf, true));
}
DI float wave_sum63(float v) {
  v = dpp_add<0x111>(v); v = dpp_add<0x112>(v); v = dpp_add<0x114>(v); v = dpp_add<0x118>(v);
  v = dpp_add<0x142>(v);
  return dpp_add<0x143>(v);
}
DI s16x4 trd(LAS const char* p) { return __builtin_amdgcn_ds_read_tr16_b64_v4i16((LAS s16x4*)p); }
template <int STEP> DI bf16x8 rdW(LAS const char* p) {
  const s16x4 a = trd(p), b = trd(p + STEP);
  return __builtin_shufflevector(a, b, 0, 1, 2, 3, 4, 5, 6, 7);
}
DI unsigned pk(float lo, float hi) {
  const f32x2 v = {lo, hi};
  return __builtin_bit_cast(unsigned, __builtin_convertvector(v, bf2));
}
DI float rbf(float v) { return __builtin_bit_cast(float, pk(0.f, v) & 0xffff0000u); }
#define COLSUM(w, cs) cs = __builtin_amdgcn_mfma_f32_16x16x32_bf16(a.sel, (w), cs, 0, 0, 0)
DI bf16x8 mk_sel(int l) {
  const unsigned sl = ((l ^ (l >> 4)) & 1) ? 0u : 0x3f803f80u;
  return __builtin_bit_cast(bf16x8, u32x4{sl, sl, sl, sl});
}
DI float swap32_max(float m) {
  auto r = __builtin_amdgcn_permlane32_swap(__float_as_uint(m), __float_as_uint(m), false, false);
  return __builtin_fmaxf(__uint_as_float(r[0]), __uint_as_float(r[1]));
}
DI float swap32_add(float m) {
  auto r = __builtin_amdgcn_permlane32_swap(__float_as_uint(m), __float_as_uint(m), false, false);
  return __uint_as_float(r[0]) + __uint_as_float(r[1]);
}
DI f32x16 splat(float v) {
  f32x16 r;
#pragma unroll
  for (int i = 0; i < 16; ++i) r[i] = v;
  return r;
}
DI void ln_fix(f32x16& acc, float c, unsigned nm) {
  const unsigned u0 = __float_as_uint(c);
  const float r1 = c - __uint_as_float(u0 & 0xffff0000u);
  const unsigned u1 = __float_as_uint(r1);
  const float r2 = r1 - __uint_as_float(u1 & 0xffff0000u);
  const u32x2 a = {(u0 >> 16) | (u1 & 0xffff0000u), __float_as_uint(r2) >> 16};
  acc = __builtin_amdgcn_mfma_f32_32x32x8bf16_1k(__builtin_bit_cast(s16x4, a), __builtin_bit_cast(s16x4, u32x2{nm, nm}), acc, 0, 0, 0);
}
DI float geglu(float a, float b, float k1, float k2, float ir2) {
  const float e = __builtin_amdgcn_exp2f(a * __builtin_fmaf(a * a, k2, k1));
  return a * b * __builtin_amdgcn_rcpf(__builtin_fmaf(e, ir2, ir2));
}

DI unsigned w_voff(int lane, int pitch, int x = 0) {
  PIN(lane);
  const int r8 = lane >> 3, p8 = lane & 7;
  return r8 * pitch + (((p8 >> 1) ^ (r8 & 2) ^ x) * 32) + (p8 & 1) * 16;
}
struct Cx {
  int lane, w, g, c;
  int rt, ct, rot;
  int hd, G;
  unsigned voffC;
  unsigned cbd, cbq, cbo, hb, ob;
  rsrc_t ws, rwd, rwo;
  u32x2 tag;
  LAS char* L;
};
DI void c_setup(Cx& v) {
  v.voffC = w_voff(v.lane, 1024);
  v.cbd = uni((448 * v.g + 32 * v.c) * 1024 + v.ct * 128);
  v.cbq = uni(448 * v.g * 1024 + v.ct * 128);
  v.cbo = uni((128 * v.g + 32 * v.c) * 1024 + v.ct * 128);
  v.hb = uni(WS_H + (2 * v.rt * 112 + 28 * v.g) * 1024);
  v.ob = uni(WS_O + (2 * v.rt * 32 + 8 * v.g) * 1024);
  v.rot = uni((v.rt & 3) * 2);
}
DI int c_grp(const Cx& v, int gp) {
  if (gp >= 7) return (gp - 7) ^ (v.rot & 1);
  const int r = gp + v.rot;
  return r >= 7 ? r - 7 : r;
}
DI unsigned c_voff(const Cx& v, int gp, int i) { return v.voffC + (c_grp(v, gp) << 16) + i * 8192; }
DI unsigned c_dst(const Cx& v, int region, int i) { return region * REG + v.g * 8192 + v.c * 4096 + i * 1024; }
DI void c_piece(const Cx& v, int gp, int region, int i) {
  glds(gp < 7 ? v.rwd : v.rwo, c_voff(v, gp, i), gp < 7 ? v.cbd : v.cbo, c_dst(v, region, i));
}

DI u32x4 w_park(const Cx& v, int gp, int i) {
  u32x4 r;
  asm volatile("buffer_load_dwordx4 %0, %1, %2, %3 offen" : "=v"(r) : "v"(c_voff(v, gp, i)), "s"(v.rwd), "s"(v.cbq) : "memory");
  return r;
}
DI unsigned hs_word(const Cx& v, int i) { return KV + 128 + (i < 2 ? v.g * 8 + i * 4 : 32 + (i - 2) * 4); }
DI void hs_set(const Cx& v, int i, int val) { *(volatile LAS int*)(v.L + hs_word(v, i)) = val; }
DI void hs_wait(const Cx& v, int i, int need) {
  unsigned cnt;
  int x;
  asm volatile(
      "s_mov_b32 %1, %4\n"
      "1:\n\t"
      "ds_read_b32 %0, %2\n\t"
      "s_waitcnt lgkmcnt(0)\n\t"
      "v_cmp_gt_i32 vcc, %3, %0\n\t"
      "s_cbranch_vccz 2f\n\t"
      "s_sleep 2\n\t"
      "s_sub_u32 %1, %1, 1\n\t"
      "s_cbranch_scc0 1b\n"
      "2:"
      : "=&v"(x), "=&s"(cnt)
      : "v"(hs_word(v, i)), "n"(need), "n"(SPIN_CAP * 8)
      : "vcc", "scc", "memory");
}

struct AtPre { u32x4 q, k0, k1, v0, v1; };
DI int at_kb(const Cx& v) {
  const int kb = 8 * v.G - 8 + 2 * v.w;
  return kb < 0 ? 0 : kb;
}
DI u32x4 at_load(const Cx& v, int i) {
  const int blk = i == 0 ? 8 * v.G + v.w : at_kb(v) + ((i - 1) & 1);
  return ld128h(v.ws, v.lane * 16, WS_QKV + ((i + 1) >> 1) * SEC_BYTES + (v.hd * 64 + blk) * 1024);
}
DI unsigned at_flag_off(const Cx& v) {
  const int i = v.lane & 7, isq = i >> 2;
  const int blk = at_kb(v) + (i & 1) + isq * (8 * v.G + v.w - at_kb(v) - (i & 1));
  const int sec = (1 - isq) * (1 + ((i >> 1) & 1));
  return WS_FQ + ((blk >> 2) * 24 + sec * 8 + (v.hd >> 2)) * 64 + (((v.hd >> 1) & 1) * 4 + (blk & 3)) * 8;
}
DI void at_stage(const Cx& v, const AtPre& a) {
  LAS char* kd = v.L + KV + 2 * v.w * 1024 + v.lane * 16;
  LAS char* vd = v.L + KV + 16384 + 2 * v.w * 1024 + (v.lane & 31) * 32 + (v.lane >> 5) * 16;
  const unsigned one = (v.lane & 8) ? 0u : 0x3f803f80u;
  *(LAS u32x4*)kd = v.w == 0 ? u32x4{one, one, one, one} : a.k0;
  *(LAS u32x4*)(kd + 1024) = a.k1;
  *(LAS u32x4*)vd = a.v0;
  *(LAS u32x4*)(vd + 1024) = a.v1;
}
DI void max16(const f32x16& s, float& m, float& m2) {
#pragma unroll
  for (int r = 0; r < 16; r += 4) {
    m = __builtin_fmaxf(__builtin_fmaxf(m, s[r]), s[r + 1]);
    m2 = __builtin_fmaxf(__builtin_fmaxf(m2, s[r + 2]), s[r + 3]);
  }
}
DI void exp_pack(const f32x16& s, u32x4& p0, u32x4& p1) {
#pragma unroll
  for (int i = 0; i < 4; ++i) {
    p0[i] = pk(__builtin_amdgcn_exp2f(s[2 * i]), __builtin_amdgcn_exp2f(s[2 * i + 1]));
    p1[i] = pk(__builtin_amdgcn_exp2f(s[8 + 2 * i]), __builtin_amdgcn_exp2f(s[9 + 2 * i]));
  }
}
#define PIN4(a, o) asm volatile("" : "+v"(a[o]), "+v"(a[o + 1]), "+v"(a[o + 2]), "+v"(a[o + 3]))
#define BF(x) __builtin_bit_cast(bf16x8, x)
DI void pin8(u32x4 (&a)[8]) { PIN4(a, 0); PIN4(a, 4); }
DI void undef8(u32x4 (&a)[8]) {
  asm volatile("" : "=v"(a[0]), "=v"(a[1]), "=v"(a[2]), "=v"(a[3]), "=v"(a[4]), "=v"(a[5]), "=v"(a[6]), "=v"(a[7]));
}
DI void o_flag(const Cx& v, int qb) { put_flag(v.ws, v.lane, WS_FO + (v.G * 32 + v.hd) * 64 + qb * 8, v.tag); }
struct AtLane {
  LAS const char *kp, *vp;
  int vstr;
  int td;
};
#define KFRAG(kb) (*(LAS const bf16x8*)(a.kp + (kb) * 1024))
#define VFRAG(kb, s) rdW<256>(a.vp + (kb) * a.vstr + (s) * 512)
DI AtLane at_lane(const Cx& v, int qb) {
  const int lane = v.lane, h = lane >> 5, qq = (lane & 15) >> 2, p = lane & 3;
  AtLane a;
  a.kp = v.L + KV + qb * 1024 + lane * 16;
  int vreal = KV + 16384 + qb * 1024 + (4 * h + qq) * 32 + (p & 1) * 16 + (p >> 1) * 8, vone = KV + (lane & 15) * 8;
  PIN(vreal); PIN(vone);
  a.vp = v.L + ((lane & 16) ? vone : vreal);
  a.vstr = (lane & 16) ? 0 : 1024;
  a.td = (lane & 31) - 4 * h;
  return a;
}
DI f32x16 at_merge(const AtLane& a, const f32x16& sd, const f32x16& sf) {
  f32x16 r;
#pragma unroll
  for (int i = 0; i < 16; ++i) r[i] = (i & 3) + 8 * (i >> 2) <= a.td ? sd[i] : sf[i];
  return r;
}
DI u32x4 at_mask(const AtLane& a, int half) {
  u32x4 k;
#pragma unroll
  for (int i = 0; i < 4; ++i) {
    const int r0 = 2 * (i & 1) + 8 * (i >> 1) + 16 * half;
    k[i] = (r0 <= a.td ? 0xffffu : 0u) | (r0 + 1 <= a.td ? 0xffff0000u : 0u);
  }
  return k;
}
DI void o_store(const Cx& v, int qb, const f32x16& o) {
  const float il = __builtin_amdgcn_rcpf(o[8]);
  u32x4 ov;
#pragma unroll
  for (int i = 0; i < 4; ++i) ov[i] = pk(o[2 * i] * il, o[2 * i + 1] * il);
  st128h(v.ws, v.lane * 16, WS_O + ((8 * v.G + qb) * 32 + v.hd) * 1024, ov);
}
template <int GP, int N> DI u32x4 a_load(const Cx& v) {
  return ld128h<(N >> 1) * 1024>(v.ws, v.lane * 16 + (c_grp(v, GP) << 12) + (N & 1) * (GP < 7 ? 112 : 32) * 1024, GP < 7 ? v.hb : v.ob);
}
DI void w_early(const Cx& v, int q, u32x4 (&af)[4][8]) {
  if (v.c) {
    af[q >> 2][(2 * q) & 7] = w_park(v, 5 + (q >> 2), (2 * q) & 7);
    af[q >> 2][(2 * q + 1) & 7] = w_park(v, 5 + (q >> 2), (2 * q + 1) & 7);
  } else c_piece(v, 3, 4, q);
}
template <bool FULL> DI void at_body(const Cx& v, const AtPre& ap, u32x4 (&af)[4][8]) {
  const AtLane a = at_lane(v, v.w);
  const bf16x8 q = BF(ap.q);
  const float NEG = -1e30f;
  const f32x16 zero = {};
  const int th = (v.lane >> 3) & 1;
  const unsigned hpoll = WS_FH + ((v.rt >> 1) * 16 + 4 * v.g + (v.lane & 3)) * 64 + (((v.lane >> 2) & 1) * 4 + ((2 * v.rt + th) & 3)) * 8;
  float m = NEG, m2 = NEG;
  f32x16 o = zero, cm, sc;
  u32x4 p0, p1;
  u32x2 fl;
  if (FULL) {
    bf16x8 kf[9];
#pragma unroll
    for (int kb = 0; kb < 9; ++kb) kf[(kb + 8) % 9] = KFRAG((kb + 8) % 9);
    if (v.w == 0) kf[0] = BF(ap.k0);
    f32x16 sa = MFMA(kf[8], q, zero), sb = MFMA(kf[0], q, zero);
    sc = MFMA(kf[1], q, zero);
    PIN(sa); PIN(sb); PIN(sc);
    SBAR();
    w_early(v, 0, af);
    max16(at_merge(a, sa, sb), m, m2);
    static_for<1, 8>([&](auto K_) __attribute__((always_inline)) {
      constexpr int kb = decltype(K_)::v;
      f32x16 sn;
      if constexpr (kb < 7) { sn = MFMA(kf[kb + 1], q, zero); PIN(sn); }
      SBAR();
      if (kb == HPOLL) fl = ld64h(v.ws, hpoll);
      w_early(v, kb, af);
      max16(sc, m, m2);
      PIN(m); PIN(m2);
      SBAR();
      if constexpr (kb < 7) sc = sn;
    });
    m = swap32_max(__builtin_fmaxf(m, m2));
    STAMP2(8);
    cm = splat(-m);
    sc = MFMA(kf[1], q, cm);
    bf16x8 kz = kf[0], kn = kf[2], v0 = VFRAG(1, 0), v1 = VFRAG(1, 1);
    static_for<1, 8>([&](auto K_) __attribute__((always_inline)) {
      constexpr int kb = decltype(K_)::v;
      const bf16x8 n0 = VFRAG(kb + 1, 0), n1 = VFRAG(kb + 1, 1);
      f32x16 sn;
      if constexpr (kb < 7) { sn = MFMA(kn, q, cm); PIN(sn); kn = KFRAG(kb + 2); }
      else { sa = MFMA(kn, q, cm); sb = MFMA(kz, q, cm); PIN(sa); PIN(sb); }
      SBAR();
      exp_pack(sc, p0, p1);
      o = MFMA(v0, BF(p0), o);
      o = MFMA(v1, BF(p1), o);
      PIN(o);
      if (kb == 1) {
        STAMP2(10);
        asm volatile("s_waitcnt vmcnt(%1)" : "+v"(fl) : "n"(8 - HPOLL) : "memory");
        check_flags(v.ws, hpoll, v.tag, v.c ? v.tag : fl);
        STAMP2(11);
      }
      if (!v.c) static_for<(kb - 1) * 3, (kb < 7 ? kb * 3 : 20)>([&](auto N_) __attribute__((always_inline)) {
          af[decltype(N_)::v >> 3][decltype(N_)::v & 7] = a_load<(decltype(N_)::v >> 3), (decltype(N_)::v & 7)>(v);
        });
      SBAR();
      if constexpr (kb < 7) sc = sn;
      v0 = n0;
      v1 = n1;
    });
    sc = at_merge(a, sa, sb);
    exp_pack(sc, p0, p1);
    const u32x4 k0 = at_mask(a, 0), k1 = at_mask(a, 1);
    o = MFMA(v0, BF(p0 & k0), o);
    o = MFMA(v1, BF(p1 & k1), o);
    o = MFMA(VFRAG(0, 0), BF(p0 & ~k0), o);
    o = MFMA(VFRAG(0, 1), BF(p1 & ~k1), o);
  } else {
#pragma unroll
    for (int i = 0; i < 8; ++i) w_early(v, i, af);
    max16(at_merge(a, MFMA(KFRAG(8), q, zero), splat(NEG)), m, m2);
    for (int kb = 8 - v.w; kb < 8; ++kb) max16(MFMA(KFRAG(kb), q, zero), m, m2);
    cm = splat(-swap32_max(__builtin_fmaxf(m, m2)));
    for (int kb = 8 - v.w; kb < 9; ++kb) {
      sc = MFMA(KFRAG(kb), q, cm);
      if (kb == 8) sc = at_merge(a, sc, splat(NEG));
      exp_pack(sc, p0, p1);
      o = MFMA(VFRAG(kb, 0), BF(p0), o);
      o = MFMA(VFRAG(kb, 1), BF(p1), o);
    }
    PIN(o);
    check_flags(v.ws, hpoll, v.tag, v.c ? v.tag : u32x2{~v.tag.x, 0u});
    if (!v.c) static_for<0, 20>([&](auto N_) __attribute__((always_inline)) {
      af[decltype(N_)::v >> 3][decltype(N_)::v & 7] = a_load<(decltype(N_)::v >> 3), (decltype(N_)::v & 7)>(v);
    });
  }
  o_store(v, v.w, o);
}

DI void c_scatter2(const Cx& v, LAS char* X, const f32x16& a, int nb, int j0) {
  const int t = v.lane & 31;
  LAS char* row = X + v.g * 8192 + t * 256;
  const int sw = ((v.lane >> 5) ^ (t & 15)) * 16;
#pragma unroll
  for (int j = j0; j < j0 + 2; ++j) *(LAS f32x4*)(row + (sw ^ ((4 * nb + j) * 32))) = f32x4{a[4 * j], a[4 * j + 1], a[4 * j + 2], a[4 * j + 3]};
}
DI LAS f32x4* c_cell(const Cx& v, LAS char* X, int half) {
  const int t = 8 * v.g + (v.lane >> 3);
  return (LAS f32x4*)(X + t * 256 + (((2 * (v.lane & 7) + half) ^ (t & 15)) * 16));
}
DI f32x4 c_gather(const Cx& v, LAS char* X, int half) {
  LAS const f32x4* src = c_cell(v, X, half);
  return (src[0] + src[512]) + (src[1024] + src[1536]);
}
DI f32x4 silu4(f32x4 d) {
#pragma unroll
  for (int i = 0; i < 4; ++i) d[i] *= __builtin_amdgcn_rcpf(1.f + __builtin_amdgcn_exp2f(-1.4426950408889634f * d[i]));
  return d;
}
DI u32x2 fin4(u32x2 x, f32x4 s, f32x4 g) {
  return u32x2{pk(__builtin_fmaf(s[0], g[0], __uint_as_float(x.x << 16)), __builtin_fmaf(s[1], g[1], __uint_as_float(x.x & 0xffff0000u))),
               pk(__builtin_fmaf(s[2], g[2], __uint_as_float(x.y << 16)), __builtin_fmaf(s[3], g[3], __uint_as_float(x.y & 0xffff0000u)))};
}
DI LAS char* c_xd(const Cx& v, int half) { return v.L + (1 + half) * REG; }
DI LAS char* c_xs(const Cx& v, int half) { return v.L + (half ? 0 : 4 * REG); }
DI unsigned c_xoff(const Cx& v) { return (64 * v.rt + 8 * v.w + (v.lane >> 3)) * 1024 + v.ct * 128 + (v.lane & 7) * 16; }
DI u32x4 x_load(const Params& P, const Cx& v) {
  u32x4 r;
  asm volatile("buffer_load_dwordx4 %0, %1, %2, 0 offen" : "=v"(r) : "v"(c_xoff(v)), "s"(mk_rsrc(P.x, 2048 * 512 * 2)) : "memory");
  return r;
}
struct CFrag {
  LAS const char *b0, *b1;
  bf16x8 wf[4][2];
};
DI CFrag c_frag(const Cx& v) {
  const int lane = v.lane, h = lane >> 5, qq = (lane & 15) >> 2, p = lane & 3, g1 = (lane >> 4) & 1;
  CFrag c;
  c.b0 = v.L + v.g * 8192 + (4 * h + qq) * 128 + (g1 + (qq & 2)) * 32 + 8 * p;
  c.b1 = v.L + v.g * 8192 + (4 * h + qq) * 128 + (g1 + (2 ^ (qq & 2))) * 32 + 8 * p;
  return c;
}
template <int S> DI void c_rd(CFrag& c) {
  constexpr int RG[4] = {0, 1, 2, 4};
  c.wf[S & 3][0] = rdW<1024>(c.b0 + RG[(S >> 2) & 3] * REG + (S & 3) * 2048);
  c.wf[S & 3][1] = rdW<1024>(c.b1 + RG[(S >> 2) & 3] * REG + (S & 3) * 2048);
}
template <int GP> DI void c_close(const Cx& v, CFrag& c, const u32x4 (&af)[8], f32x16 (&ac)[2][2], LAS char* x0, LAS char* x1) {
  c_rd<4 * GP + 2>(c);
  c_rd<4 * GP + 3>(c);
  static_for<0, 5>([&](auto J_) __attribute__((always_inline)) {
    constexpr int j = decltype(J_)::v;
    static_for<0, 4>([&](auto K_) __attribute__((always_inline)) {
      constexpr int k = decltype(K_)::v;
      if constexpr (j < 4) {
        ac[j & 1][j >> 1] = MFMA(c.wf[k][j & 1], BF(af[2 * k + (j >> 1)]), ac[j & 1][j >> 1]);
        PIN(ac[j & 1][j >> 1]);
        SBAR();
      }
      if constexpr (j > 0 && (k & 1)) c_scatter2(v, j > 2 ? x1 : x0, ac[(j - 1) & 1][(j - 1) >> 1], (j - 1) & 1, k - 1);
      SBAR();
    });
  });
}
DI void gemm_role(const Params& P, const Cx& v, u32x4 (&af)[4][8], u32x4 (&wp)[8], u32x4& xv) {
  CFrag c = c_frag(v);
  f32x16 d[2][2] = {};
  c_rd<0>(c);
  c_rd<1>(c);
  static_for<4, 8>([&](auto N_) __attribute__((always_inline)) { af[2][decltype(N_)::v] = a_load<2, decltype(N_)::v>(v); });
  __builtin_amdgcn_s_setprio(0);
  wait_vm<17>();
  pin8(af[0]);
  static_for<0, 24>([&](auto S_) __attribute__((always_inline)) {
    constexpr int s = decltype(S_)::v, gp = s >> 2, k = s & 3;
    if (k == 0) STAMP2(40 + gp);
    if constexpr (k == 2) {
      constexpr int N[6] = {8, 13, 12, 12, 8, 1};
      wait_vm<N[gp]>();
      pin8(af[(gp + 1) & 3]);
      if (gp == 0) o_flag(v, v.w);
      if (gp >= 4) hs_wait(v, 1, gp + 1);
    }
    if (k == 0 && gp >= 2) hs_set(v, 0, gp);
    c_rd<s + 2>(c);
    static_for<0, 4>([&](auto J_) __attribute__((always_inline)) {
      constexpr int j = decltype(J_)::v, n = 4 * k + j;
      d[j >> 1][j & 1] = MFMA(c.wf[k][j >> 1], BF(af[gp & 3][2 * k + (j & 1)]), d[j >> 1][j & 1]);
      PIN(d[j >> 1][j & 1]);
      SBAR();
      if constexpr (gp < 4 && !(n & 1)) af[(gp + 3) & 3][n / 2] = a_load<gp + 3, n / 2>(v);
      if constexpr (gp == 1 && (n & 1)) *(LAS u32x4*)(v.L + c_dst(v, 0, n / 2) + v.lane * 16) = wp[n / 2];
      if (s == 20 && j == 0) xv = x_load(P, v);
      SBAR();
    });
  });
  STAMP2(46);
  c_close<6>(v, c, af[2], d, c_xd(v, 0), c_xd(v, 1));
  asm volatile("s_waitcnt lgkmcnt(0)" ::: "memory");
  hs_set(v, 2 + v.g, 1);
#pragma unroll
  for (int q = 0; q < 4; ++q) hs_wait(v, 2 + q, 1);
#pragma unroll
  for (int i = 0; i < 4; ++i) *c_cell(v, c_xd(v, i >> 1), i & 1) = silu4(c_gather(v, c_xd(v, i >> 1), i & 1));
  STAMP(14);
}
DI void side_role(const Params& P, const Cx& v, u32x4 (&af)[4][8], u32x4& xv) {
  wait_vm<0>();
  o_flag(v, v.w);
  pin8(af[0]);
  pin8(af[1]);
  const unsigned oown = WS_FO + ((v.rt >> 2) * 32 + 8 * v.g + (v.lane & 7)) * 64 + ((2 * v.rt + ((v.lane >> 3) & 1)) & 7) * 8;
  const unsigned opoll = blockIdx.x == 0 && v.lane >= 32 ? WS_FO + (v.g * 64 + v.lane - 32) * 64 : oown;
  auto hand = [&](int j) __attribute__((always_inline)) {
    hs_wait(v, 0, 2 + j);
#pragma unroll
    for (int i = 0; i < 8; ++i) *(LAS u32x4*)(v.L + (1 + j) * REG + v.g * 8192 + i * 1024 + v.lane * 16) = af[j][i];
    asm volatile("s_waitcnt lgkmcnt(0)" ::: "memory");
    hs_set(v, 1, 5 + j);
  };
  hand(0);
  hand(1);
  __builtin_amdgcn_s_setprio(0);
  u32x2 fl;
#pragma unroll
  for (int j = 0; j < 2; ++j) {
    if (j) fl = ld64h(v.ws, opoll);
    hs_wait(v, 0, 4 + j);
#pragma unroll
    for (int i = 0; i < 8; ++i) glds(v.rwo, c_voff(v, 7 + j, i), v.cbo - 32768, (j ? 0 : 4 * REG) + v.g * 8192 + i * 1024);
  }
  asm volatile("s_waitcnt vmcnt(8)" : "+v"(fl)::"memory");
  check_flags(v.ws, opoll, v.tag, fl);
  STAMP2(47);
  static_for<0, 16>([&](auto N_) __attribute__((always_inline)) {
    af[2 + decltype(N_)::v / 8][decltype(N_)::v & 7] = a_load<7 + decltype(N_)::v / 8, (decltype(N_)::v & 7)>(v);
  });
  xv = x_load(P, v);
  if (blockIdx.x == 0) fl = ld64h(v.ws, opoll + (v.lane >= 32 ? 32 * 64 : 0));
  CFrag c = c_frag(v);
  f32x16 sm[2][2] = {};
  c_rd<28>(c);
  c_rd<29>(c);
  wait_vm<9>();
  pin8(af[2]);
  STAMP2(48);
  static_for<28, 32>([&](auto S_) __attribute__((always_inline)) {
    constexpr int s = decltype(S_)::v, k = s & 3;
    c_rd<s + 2>(c);
#pragma unroll
    for (int j = 0; j < 4; ++j) {
      sm[j >> 1][j & 1] = MFMA(c.wf[k][j >> 1], BF(af[2][2 * k + (j & 1)]), sm[j >> 1][j & 1]);
      PIN(sm[j >> 1][j & 1]);
    }
    SBAR();
  });
  asm volatile("s_waitcnt vmcnt(0)" : "+v"(fl)::"memory");
  pin8(af[3]);
  c_close<8>(v, c, af[3], sm, c_xs(v, 0), c_xs(v, 1));
  if (blockIdx.x == 0) check_flags(v.ws, opoll + (v.lane >= 32 ? 32 * 64 : 0), v.tag, fl);
  STAMP(14);
}

struct AB {
  bf16x8 X[32];
  bf16x8 wf[3][2];
  bf16x8 sel;
  unsigned nm;
  float r, k1, k2, ir2;
  int gr;
  int rb, cg, strip0, dblA, dblB;
  int kx;
  unsigned voffX, voffQ, voffF;
  rsrc_t rx, rq, rf;
};
DI void x_piece(const Cx& v, const AB& a, int kc, int slot, int i) {
  glds(a.rx, a.voffX ^ (64 * i), (a.rb * 128 + 16 * v.w + 4 * i) * 1024 + (kc ^ a.kx) * 256, slot * SLOT + (4 * v.w + i) * 1024);
}
DI void s_piece(const Cx& v, const AB& a, int kq, int i) {
  glds(a.rq, a.voffQ, (128 * (kq ^ a.kx) + 16 * v.w + 8 * i) * 3072 + a.strip0 * 128, SBUF + (kq & 1) * SUB1 + (2 * v.w + i) * 1024);
}
DI void f_piece(const Cx& v, const AB& a, int dbl, int kq, int slot, int i) {
  glds(a.rf, a.voffF, (128 * (kq ^ a.kx) + 32 * v.g + 8 * i) * 3584 + dbl * 128, slot * SLOT + v.c * SUB1 + v.g * 4096 + i * 1024);
}
template <bool TB> DI void u_piece(const Cx& v, const AB& a, int kq, int i) {
  const int slot = 1 + 2 * (kq & 1);
  if (TB) glds(a.rq, a.voffQ, (128 * (kq ^ a.kx) + 32 * v.g + 8 * i) * 3072 + (a.strip0 + 1 + v.c) * 128,
               slot * SLOT + v.c * SUB1 + v.g * 4096 + i * 1024);
  else f_piece(v, a, a.dblA, kq, slot, i);
}
DI void cs_put(const Cx& v, const f32x4& c0, const f32x4& c1, const f32x4& c2) {
  const bool hi = v.lane & 16;
  *(LAS f32x4*)(v.L + AUX + v.w * 1024 + v.lane * 16) = f32x4{hi ? c0[1] : c0[0], hi ? c1[1] : c1[0], hi ? c2[1] : c2[0], 0.f};
}
DI f32x4 cs_get(const Cx& v) {
  LAS const f32x4* p = (LAS const f32x4*)(v.L + AUX + v.c * 4096 + v.lane * 16);
  return (p[0] + p[64]) + (p[128] + p[192]);
}
DI void q_store(const Cx& v, const AB& a, const f32x16& acc, int strip, int hh) {
  const float sc = strip < 8 ? a.r * QSCALE : a.r;
  u32x4 o;
#pragma unroll
  for (int i = 0; i < 4; ++i) o[i] = pk(acc[8 * hh + 2 * i] * sc, acc[8 * hh + 2 * i + 1] * sc);
  st128h(v.ws, v.lane * 16, WS_QKV + ((strip * 4 + 2 * v.c + hh) * 64 + 4 * a.rb + v.g) * 1024, o);
}
DI void q_flag(const Cx& v, const AB& a, int strip) { put_flag(v.ws, v.lane, WS_FQ + (a.rb * 24 + strip) * 64 + v.w * 8, v.tag); }
DI void put_h(const Cx& v, const AB& a, int dbl, int j, const u32x4& hv) {
  st128h(v.ws, v.lane * 16, WS_H + ((4 * a.rb + v.g) * 112 + 4 * dbl + 2 * v.c + j) * 1024, hv);
}
struct Bases { unsigned b[4]; };
template <bool F> DI Bases mk_bases(const Cx& v, const AB& a, int base) {
  int l = v.lane;
  PIN(l);
  const int qq = (l & 15) >> 2, g1 = (l >> 4) & 1;
  const int o = base + (F ? g1 * SUB1 : 0) + (8 * (l >> 5) + qq) * 128 + ((2 * v.c) ^ (qq & 2) ^ g1) * 32 + 8 * (l & 3);
  Bases r;
#pragma unroll
  for (int i = 0; i < 4; ++i) { r.b[i] = o + (i ^ a.gr) * 4096; PIN(r.b[i]); }
  return r;
}
DI void bump(Bases& B, int d) {
#pragma unroll
  for (int i = 0; i < 4; ++i) { B.b[i] += d; PIN(B.b[i]); }
}
#define WRD(B, off, i) rdW<512>((LAS const char*)(size_t)((B).b[(i) >> 1]) + (off) + ((i) & 1) * 2048)
#define WRX(B, off, i) rdW<512>((LAS const char*)(size_t)((B).b[(i) >> 1] ^ 32u) + (off) + ((i) & 1) * 2048)

DI u32x2 make_tag(u32x2 e) {
  const unsigned long long z = ((unsigned long long)__builtin_amdgcn_readfirstlane(e.y) << 32 | (unsigned)__builtin_amdgcn_readfirstlane(e.x));
  const unsigned long long t = (z ^ (z >> 31)) * 0x9E3779B97F4A7C15ull + 0xD1B54A32D192ED03ull;
  return u32x2{(unsigned)t, (unsigned)(t >> 32)};
}

DI void stats_sum(const u32x4& xr, float& s1, float& s2) {
  s1 = s2 = 0.f;
#pragma unroll
  for (int d = 0; d < 4; ++d) {
    const float lo = __uint_as_float(xr[d] << 16), hi = __uint_as_float(xr[d] & 0xffff0000u);
    s1 += lo + hi;
    s2 = __builtin_fmaf(lo, lo, __builtin_fmaf(hi, hi, s2));
  }
}
DI void stats_put(const Cx& v, const AB& a, float s1, float s2) {
  const float mean = s1 * (1.f / 512.f);
  const float var = __builtin_fmaxf(__builtin_fmaf(-mean, mean, s2 * (1.f / 512.f)), 0.f);
  const float sd = __builtin_amdgcn_sqrtf(rbf(rbf(var) + 1e-5f));
  put_flag(v.ws, v.lane ^ 63, WS_ST + (a.rb * 128 + a.cg * 8 + v.w) * 8, u32x2{pk(-mean, sd), v.tag.x});
}
DI void row_stats(const Cx& v, AB& a, unsigned g) {
  const float sd = __uint_as_float(g & 0xffff0000u);
  const float c1 = -2.f * 0.7978845608028654f * 1.4426950408889634f;
  a.r = __builtin_amdgcn_rcpf(sd);
  a.k1 = c1 * a.r;
  a.k2 = c1 * 0.044715f * a.r * a.r * a.r;
  a.ir2 = sd * sd;
  a.nm = v.lane < 32 ? (g & 0xffffu) | (g << 16) : 0u;
}
DI void rd_step(AB& a, const Bases& B, int s) {
  a.wf[(s + 2) % 3][0] = WRD(B, ((s >> 3) & 1) * SLOT, s & 7);
  a.wf[(s + 2) % 3][1] = WRX(B, ((s >> 3) & 1) * SLOT, s & 7);
}
template <bool TB, int Q>
DI void f_step(Cx& v, AB& a, const Bases& BS, Bases& BU, f32x16& ps, f32x16& a0, f32x16& a1, f32x4& cs, f32x4& c0, f32x4& c1,
               u32x2& st, u32x4& xr) {
  const int t = v.lane & 31;
  const unsigned u = (((v.lane >> 5) ^ (t & 15)) * 16) ^ (64 * a.gr);
  LAS const char* xb = v.L + (32 * v.g + t) * 256;
  bf16x8 ws[3];
  float s1, s2;
  f32x4 cp;
  auto rdx = [&](int q, int i) __attribute__((always_inline)) { a.X[8 * q + i] = *(LAS const bf16x8*)(xb + 2 * (q & 1) * SLOT + (u ^ (32 * i))); };
  auto rd = [&](int i) __attribute__((always_inline)) {
    a.wf[i % 3][0] = WRD(BU, 0, i);
    a.wf[i % 3][1] = TB ? WRD(BU, SUB1, i) : WRX(BU, 0, i);
    ws[i % 3] = WRD(BS, (Q & 1) * SUB1, i);
  };
  auto req = [&](int n) __attribute__((always_inline)) {
    if (Q < 3) { if (n < 2) s_piece(v, a, Q + 1, n); else u_piece<TB>(v, a, Q + 1, n - 2); }
    else if (n < 4) f_piece(v, a, a.dblB, 1, 1, n);
  };
  if (Q == 0) { rdx(0, 0); rdx(0, 1); }
  rd(0);
  rd(1);
  static_for<0, 8>([&](auto I_) __attribute__((always_inline)) {
    constexpr int i = decltype(I_)::v;
    if (i == 4) {
      if (Q == 0) wait_bar<8, 0>();
      else if (Q < 3) wait_bar<Q == 2 ? 8 : 6, 15>();
      else {
        wait_bar<4, 6>();
        cp = cs_get(v);
      }
    }
    const bf16x8 x = a.X[8 * Q + i];
    a0 = MFMA(a.wf[i % 3][0], x, a0);
    PIN(a0);
    SBAR();
    if (Q == 0 && i < 3) { rdx(0, 2 * i + 2); rdx(0, 2 * i + 3); }
    if (Q < 3 && i >= 4) { rdx(Q + 1, 2 * i - 8); rdx(Q + 1, 2 * i - 7); }
    if (i < 2) { COLSUM(a.wf[i % 3][0], c0); PIN(c0); }
    if (i < 3) req(2 * i);
    if (i >= 4) {
      if (Q < 2) x_piece(v, a, Q + 2, 2 * (Q & 1), i - 4);
      else f_piece(v, a, a.dblB, 2 * (Q & 1), 2 * (Q & 1), i - 4);
    }
    if (Q == 3 && i == 5) { ln_fix(a0, cp[0], a.nm); PIN(a0); }
    SBAR();
    a1 = MFMA(a.wf[i % 3][1], x, a1);
    PIN(a1);
    SBAR();
    if (i < 2) { COLSUM(a.wf[i % 3][1], c1); PIN(c1); }
    if (i < 3) req(2 * i + 1);
    if (Q == 3 && i == 6) { ln_fix(a1, cp[1], a.nm); PIN(a1); }
    if (Q == 0 && i == 3) {
      st = ld64h(v.ws, WS_EPOCH);
      asm volatile("buffer_load_dwordx4 %0, %1, %2, %3 offen"
                   : "=v"(xr) : "v"(v.lane * 16), "s"(a.rx), "s"(RFL((a.rb * 128 + a.cg * 8 + v.w) * 1024)) : "memory");
    }
    if (Q == 1) {
      if (i == 3) { PIN(st); PIN(xr); v.tag = make_tag(st); stats_sum(xr, s1, s2); PIN(s1); PIN(s2); }
      if (i == 4) { s1 = wave_sum63(s1); PIN(s1); }
      if (i == 5) { s2 = wave_sum63(s2); PIN(s2); }
      if (i == 6) stats_put(v, a, s1, s2);
    }
    if (Q == 2 && i == 3) st = ld64h(v.ws, WS_ST + (a.rb * 128 + 32 * v.g + (v.lane & 31)) * 8);
    if (Q == 3 && i == 2) {
      PIN(st);
      check_granule(v.ws, WS_ST + (a.rb * 128 + 32 * v.g + (v.lane & 31)) * 8, v.tag.x, st);
      row_stats(v, a, st.x);
      PIN(a.nm); PIN(a.r); PIN(a.k1); PIN(a.k2); PIN(a.ir2);
    }
    SBAR();
    const bf16x8 wsi = ws[i % 3];
    ps = MFMA(wsi, x, ps);
    PIN(ps);
    SBAR();
    if (i < 6) rd(i + 2);
    if (i < 2) { COLSUM(wsi, cs); PIN(cs); }
    if (Q == 3) {
      if (i == 2) cs_put(v, c0, c1, cs);
      if (i == 4) { ln_fix(ps, cp[2], a.nm); PIN(ps); }
      if (i == 6) BU = mk_bases<true>(v, a, 0);
      if (i >= 6) rd_step(a, BU, i - 6);
    } else if (i == 6) bump(BU, (Q & 1) ? -2 * SLOT : 2 * SLOT);
    if (Q == 1 && i == 7) {
      const int per = v.w == 1 ? 16 : 56;
      asm volatile("buffer_load_dword %0, %1, %2, 0 offen"
                   : "=v"(xr.x) : "v"(v.lane < per && v.w < 2 ? (blockIdx.x * per + v.lane) * 128 : 0x40000000u), "s"(v.w == 0 ? v.rwd : v.rwo)
                   : "memory");
    }
    SBAR();
  });
}
template <bool TB> DI void front(Cx& v, AB& a, Bases& BU, f32x16& ps, f32x16& a0, f32x16& a1) {
  const Bases BS = mk_bases<false>(v, a, SBUF);
  BU = mk_bases<!TB>(v, a, SLOT);
  f32x4 cs = {}, c0 = {}, c1 = {};
  u32x2 st;
  u32x4 xr;
  STAMP2(23);
  wait_bar<4, 0>();
  STAMP(1);
  f_step<TB, 0>(v, a, BS, BU, ps, a0, a1, cs, c0, c1, st, xr);
  STAMP2(16); wait_bar<4, 0>(); STAMP2(17);
  f_step<TB, 1>(v, a, BS, BU, ps, a0, a1, cs, c0, c1, st, xr);
  STAMP2(18); wait_bar<6, 0>(); STAMP2(19);
  f_step<TB, 2>(v, a, BS, BU, ps, a0, a1, cs, c0, c1, st, xr);
  STAMP2(20); wait_bar<4, 0>(); STAMP2(21);
  PIN(xr);
  f_step<TB, 3>(v, a, BS, BU, ps, a0, a1, cs, c0, c1, st, xr);
  STAMP2(22);
  wait_bar<4, 8>();
}
template <bool TB> DI void body(Cx& v, AB& a, Bases& B, const f32x16& ps, f32x16& p0, f32x16& p1, AtPre& ap) {
  f32x16 a0 = {}, a1 = {};
  f32x4 c0 = {}, c1 = {};
  float hlo;
  f32x4 cp;
  u32x4 hv;
  u32x2 fl;
  unsigned fo;
  static_for<0, 32>([&](auto S_) __attribute__((always_inline)) {
    constexpr int s = decltype(S_)::v, kq = s >> 3, i = s & 7;
    if (s == 2) { int l = v.lane; PIN(l); a.sel = mk_sel(l); }
    const bf16x8 w0 = a.wf[(s + 2) % 3][0], w1 = a.wf[(s + 2) % 3][1];
    constexpr bool CS = (i >> 1) == 1;
    constexpr int e = TB ? -1 : (s == 30 ? 14 : ((s & 1) && s >= 3 ? (s == 31 ? 15 : (s - 3) >> 1) : -1));
    float ga, ex;
    a0 = MFMA(w0, a.X[s], a0);
    PIN(a0);
    SBAR();
    if (s == 14) bump(B, 2 * SLOT);
    if (s < 30) rd_step(a, B, s + 2);
    if (s == 7) c_setup(v);
    if (CS) { COLSUM(w0, c0); PIN(c0); }
    if ((i & 1) == 0) {
      if (kq == 0) f_piece(v, a, a.dblB, 3, 3, i >> 1);
      else c_piece(v, kq - 1, kq - 1, i >> 1);
    }
    if constexpr (e >= 0) {
      ga = (e < 8 ? p0 : p1)[e & 7];
      ex = __builtin_amdgcn_exp2f(ga * __builtin_fmaf(ga * ga, a.k2, a.k1));
      ga *= (e < 8 ? p0 : p1)[8 + (e & 7)];
      PIN(ga); PIN(ex);
    }
    SBAR();
    a1 = MFMA(w1, a.X[s], a1);
    PIN(a1);
    SBAR();
    if (CS) { COLSUM(w1, c1); PIN(c1); }
    if constexpr (e >= 0) {
      const float hx = ga * __builtin_amdgcn_rcpf(__builtin_fmaf(ex, a.ir2, a.ir2));
      if (e & 1) { hv[(e & 7) >> 1] = pk(hlo, hx); PIN(hv[(e & 7) >> 1]); }
      else { hlo = hx; PIN(hlo); }
      if (e == 15) put_h(v, a, a.dblA, 1, hv);
    }
    if (s < 2) q_store(v, a, ps, a.strip0, s);
    if (s == 8) { wait_vm<4>(); q_flag(v, a, a.strip0); }
    if (!TB && s == 18) put_h(v, a, a.dblA, 0, hv);
    if (TB) {
      if (s == 3 || s == 5) q_store(v, a, p0, a.strip0 + 1, (s - 3) >> 1);
      if (s == 7 || s == 9) q_store(v, a, p1, a.strip0 + 2, (s - 7) >> 1);
      if (s == 14) { wait_vm<3>(); q_flag(v, a, a.strip0 + 1); q_flag(v, a, a.strip0 + 2); }
    }
    if (s == 28) {
      cs_put(v, c0, c1, c1);
      SBAR();
      wait_bar<63, 0>();
      cp = cs_get(v);
    }
    if (s == 30) { ln_fix(a0, cp[0], a.nm); PIN(a0); }
    if (s == 31) { ln_fix(a1, cp[1], a.nm); PIN(a1); }
    if (s == 23) { fo = at_flag_off(v); fl = ld64h(v.ws, fo); }
    if (s == 27) {
      asm volatile("s_waitcnt vmcnt(2)" : "+v"(fl)::"memory");
      check_flags(v.ws, fo, v.tag, fl);
      ap.k0 = at_load(v, 1);
    }
    if (s == 28) ap.k1 = at_load(v, 2);
    if (s == 29) ap.v0 = at_load(v, 3);
    if (s == 30) ap.v1 = at_load(v, 4);
    if (s == 31) ap.q = at_load(v, 0);
    SBAR();
    if (i == 7 && kq < 3) {
      STAMP2(36 + kq);
      if (kq < 2) wait_bar<4, 8>();
      else wait_bar<63, 8>();
      STAMP2(54 + kq);
    }
  });
  p0 = a0;
  p1 = a1;
}
DI void ab_tail(const Cx& v, const AB& a, int dbl, const f32x16& p0, const f32x16& p1, AtPre& ap, u32x4 (&wp)[8]) {
  u32x4 hv;
  static_for<0, 8>([&](auto I_) __attribute__((always_inline)) {
    constexpr int i = decltype(I_)::v, j = 2 * (i & 3);
    const f32x16& p = i < 4 ? p0 : p1;
    if constexpr (i < 4) { if (!v.c) { wp[2 * i] = w_park(v, 4, 2 * i); wp[2 * i + 1] = w_park(v, 4, 2 * i + 1); } }
    hv[i & 3] = pk(geglu(p[j], p[8 + j], a.k1, a.k2, a.ir2), geglu(p[j + 1], p[9 + j], a.k1, a.k2, a.ir2));
    PIN(hv[i & 3]);
    if constexpr ((i & 3) == 3) put_h(v, a, dbl, i >> 2, hv);
    SBAR();
  });
  if (v.c) wait_vm<2>();
  asm volatile("s_waitcnt vmcnt(10)\n\ts_barrier" : "+v"(ap.q), "+v"(ap.k0), "+v"(ap.k1), "+v"(ap.v0), "+v"(ap.v1)::"memory");
  at_stage(v, ap);
  wait_vm<0>();
  put_flag(v.ws, v.lane, WS_FH + (a.rb * 16 + a.cg) * 64 + v.w * 8, v.tag);
  wait_bar<63, 0>();
  PIN4(wp, 0); PIN4(wp, 4);
}

template <bool TB> DI void ab_path(Cx& v, AB a, AtPre& ap, u32x4 (&wp)[8]) {
  int l = v.lane;
  PIN(l);
  a.voffX = (l >> 4) * 1024 + (((l & 15) ^ (l >> 4)) * 16);
  a.voffQ = w_voff(l, 3072);
  a.voffF = w_voff(l, 3584, v.c);
  a.sel = mk_sel(l);
#pragma unroll
  for (int i = 0; i < 4; ++i) u_piece<TB>(v, a, 0, i);
#pragma unroll
  for (int i = 0; i < 4; ++i) x_piece(v, a, 1, 2, i);
  f32x16 ps = {}, p0 = {}, p1 = {};
  Bases B;
  front<TB>(v, a, B, ps, p0, p1);
  STAMP(3);
  body<TB>(v, a, B, ps, p0, p1, ap);
  STAMP(4);
  ab_tail(v, a, a.dblB, p0, p1, ap, wp);
}

__global__ __launch_bounds__(512, 2) void fused_kernel(Params P) {
  extern __shared__ __attribute__((aligned(16))) char smem[];
  Cx v;
  v.L = (LAS char*)smem;
  v.lane = threadIdx.x & 63;
  v.w = __builtin_amdgcn_readfirstlane(threadIdx.x >> 6);
  v.g = v.w & 3;
  v.c = v.w >> 2;
  const int xc = blockIdx.x & 7, idx = blockIdx.x >> 3;
  v.ws = mk_rsrc(P.ws, WS_END);
  v.rwd = mk_rsrc(P.wd, 1792 * 512 * 2);
  v.rwo = mk_rsrc(P.wo, 512 * 512 * 2);
  v.rt = v.hd = xc * 4 + (idx & 3);
  v.ct = v.G = idx >> 2;
  AtPre ap;
  u32x4 wp[8];
  STAMPR(30); STAMP(0);
  {
    AB a;
    a.rb = (xc & 1) * 8 + (idx & 7);
    const int cq = xc >> 1, j = idx >> 3;
    const bool typeB = j == 3;
    a.cg = cq * 4 + j;
    a.gr = v.g ^ (2 * v.c);

    a.strip0 = 6 * cq + j;
    const int swp = typeB ? 0 : (a.rb >> 2) & SWP;
    a.dblA = 7 * cq + 2 * j + swp;
    a.dblB = typeB ? a.dblA : 7 * cq + 2 * j + 1 - swp;
    a.kx = a.rb & KXM;
    a.rx = mk_rsrc(P.x, 2048 * 512 * 2);
    a.rq = mk_rsrc(P.wqkv, 512 * 1536 * 2);
    a.rf = mk_rsrc(v.c ? P.wu : P.wg, 512 * 1792 * 2);
    a.voffX = (v.lane >> 4) * 1024 + (((v.lane & 15) ^ (v.lane >> 4)) * 16);
    a.voffQ = w_voff(v.lane, 3072);
    STAMP2(24);
#pragma unroll
    for (int i = 0; i < 4; ++i) x_piece(v, a, 0, 0, i);
#pragma unroll
    for (int i = 0; i < 2; ++i) s_piece(v, a, 0, i);
    if (typeB) ab_path<true>(v, a, ap, wp);
    else ab_path<false>(v, a, ap, wp);
  }
  STAMP(6);
  c_setup(v);
  v.tag = u32x2{uni(v.tag.x), uni(v.tag.y)};
  u32x4 af[4][8];
  __builtin_amdgcn_s_setprio(2);
  if (v.G) at_body<true>(v, ap, af);
  else at_body<false>(v, ap, af);
  STAMP(7);
  c_setup(v);
  u32x4 xv;
  if (v.c) {
    side_role(P, v, af, xv);
    undef8(af[0]); undef8(af[1]); undef8(af[2]); undef8(wp);
  }
  if (!uni(v.c)) gemm_role(P, v, af, wp, xv);
  asm volatile("s_waitcnt vmcnt(0) lgkmcnt(0)\n\ts_barrier" : "+v"(xv)::"memory");
  if (blockIdx.x == 0 && v.w == 0) put_flag(v.ws, v.lane, WS_EPOCH, v.tag);
  const f32x4 ga = *c_cell(v, c_xd(v, v.c), 0), gb = *c_cell(v, c_xd(v, v.c), 1);
  const u32x2 y0 = fin4(u32x2{xv.x, xv.y}, c_gather(v, c_xs(v, v.c), 0), ga), y1 = fin4(u32x2{xv.z, xv.w}, c_gather(v, c_xs(v, v.c), 1), gb);
  __builtin_amdgcn_raw_buffer_store_b128(u32x4{y0.x, y0.y, y1.x, y1.y}, mk_rsrc(P.out, 2048 * 512 * 2), c_xoff(v), 0, 0);
  STAMP(15); STAMPR(31);
}
}

extern "C" void kernel_launch(void* const* d_in, const int* in_sizes, int n_in, void* d_out, int out_size, void* d_ws,
                              size_t ws_size, hipStream_t stream) {
  Params P;
  P.x = d_in[0]; P.wqkv = d_in[1]; P.wo = d_in[2];
  P.wg = d_in[4]; P.wu = d_in[5]; P.wd = d_in[6];
  P.out = d_out; P.ws = d_ws;
  (void)hipFuncSetAttribute((const void*)fused_kernel, hipFuncAttributeMaxDynamicSharedMemorySize, LDS_BYTES);
  fused_kernel<<<256, 512, LDS_BYTES, stream>>>(P);
}
```

```cpp
#include <hip/hip_runtime.h>
#include <hip/hip_bf16.h>
#include <stdint.h>

typedef short bf16x8 __attribute__((ext_vector_type(8)));
typedef short s16x4 __attribute__((ext_vector_type(4)));
typedef float f32x16 __attribute__((ext_vector_type(16)));
typedef float f32x4 __attribute__((ext_vector_type(4)));
typedef float f32x2 __attribute__((ext_vector_type(2)));
typedef unsigned u32x4 __attribute__((ext_vector_type(4)));
typedef unsigned u32x2 __attribute__((ext_vector_type(2)));
typedef __bf16 bf2 __attribute__((ext_vector_type(2)));
typedef __amdgpu_buffer_rsrc_t rsrc_t;
#define LAS __attribute__((address_space(3)))
#define DI __device__ __forceinline__
#define SBAR() __builtin_amdgcn_sched_barrier(0)
#define PIN(x) asm volatile("" : "+v"(x))
#define RFL(x) __builtin_amdgcn_readfirstlane(x)
#define MFMA(a, b, c) __builtin_amdgcn_mfma_f32_32x32x16_bf16((a), (b), (c), 0, 0, 0)

#define STAMP(i)
#define STAMPR(i)
#define STAMP_FLUSH()
#define STAMP2(i)

#define KXM 3
#define SWP 0
#define HPOLL 6
namespace {
constexpr unsigned WS_EPOCH = 0;
constexpr unsigned WS_FQ = 1024;
constexpr unsigned WS_FH = WS_FQ + 16 * 24 * 64;
constexpr unsigned WS_FO = WS_FH + 16 * 16 * 64;
constexpr unsigned WS_ST = 65536;
constexpr unsigned WS_QKV = 131072;
constexpr unsigned SEC_BYTES = 32 * 64 * 1024;
constexpr unsigned WS_H = WS_QKV + 3 * SEC_BYTES;
constexpr unsigned WS_O = WS_H + 64 * 112 * 1024;
constexpr unsigned WS_END = WS_O + 64 * 32 * 1024;
static_assert(WS_FO + 256 * 64 <= WS_ST, "flags overlap");
constexpr int SLOT = 32768, SUB1 = 16384;
constexpr int SBUF = 4 * SLOT;
constexpr int AUX = SBUF;
constexpr int REG = 32768;
constexpr int LDS_BYTES = 163840;
constexpr int KV = 3 * REG;
constexpr float QSCALE = 0.25f * 1.4426950408889634f;
constexpr unsigned SPIN_CAP = 1u << 18;

struct Params {
  const void *x, *wqkv, *wo, *wg, *wu, *wd;
  void *out, *ws;
};

template <int I> struct IC { static constexpr int v = I; };
template <int I, int N, class F> DI void static_for(F&& f) {
  if constexpr (I < N) {
    f(IC<I>{});
    static_for<I + 1, N>(f);
  }
}
template <int VM> DI void wait_vm() { asm volatile("s_waitcnt vmcnt(%0)" ::"n"(VM) : "memory"); }
template <int VM, int LG> DI void wait_bar() { asm volatile("s_waitcnt vmcnt(%0) lgkmcnt(%1)\n\ts_barrier" ::"n"(VM), "n"(LG) : "memory"); }
DI unsigned uni(unsigned x) {
  x = __builtin_amdgcn_readfirstlane(x);
  asm volatile("" : "+s"(x));
  return x;
}
DI rsrc_t mk_rsrc(const void* p, unsigned bytes) { return __builtin_amdgcn_make_buffer_rsrc((void*)p, 0, bytes, 0x00020000); }
DI void glds(rsrc_t rs, unsigned voff, unsigned soff, unsigned lds) {
  asm volatile("s_mov_b32 m0, %2\n\ts_nop 0\n\tbuffer_load_dwordx4 %0, %1, %3 offen lds" ::"v"(voff), "s"(rs),
               "s"(RFL(lds)), "s"(RFL(soff)) : "memory");
}
template <int IMM = 0> DI u32x4 ld128h(rsrc_t rs, unsigned voff, unsigned soff) {
  u32x4 r;
  asm volatile("buffer_load_dwordx4 %0, %1, %2, %3 offen offset:%4 sc1" : "=v"(r) : "v"(voff), "s"(rs), "s"(RFL(soff)), "n"(IMM) : "memory");
  return r;
}
DI u32x2 ld64h(rsrc_t rs, unsigned voff) {
  u32x2 r;
  asm volatile("buffer_load_dwordx2 %0, %1, %2, 0 offen sc1" : "=v"(r) : "v"(voff), "s"(rs) : "memory");
  return r;
}
DI void st128h(rsrc_t rs, unsigned voff, unsigned soff, u32x4 d) {
  asm volatile("buffer_store_dwordx4 %0, %1, %2, %3 offen sc1\n\ts_nop 1" ::"v"(d), "v"(voff), "s"(rs), "s"(RFL(soff)) : "memory");
}
DI void put_flag(rsrc_t ws, int lane, unsigned soff, u32x2 tag) {
  asm volatile("buffer_store_dwordx2 %0, %1, %2, %3 offen sc1" ::"v"(tag), "v"(lane ? 0x80000000u : 0u), "s"(ws), "s"(RFL(soff)) : "memory");
}
DI void check_flags(rsrc_t ws, unsigned voff, u32x2 tag, u32x2 v) {
  unsigned cnt;
  asm volatile(
      "s_mov_b32 %1, %5\n"
      "1:\n\t"
      "v_cmp_ne_u64 vcc, %2, %0\n\t"
      "s_cbranch_vccz 2f\n\t"
      "s_sleep 1\n\t"
      "buffer_load_dwordx2 %0, %3, %4, 0 offen sc1\n\t"
      "s_waitcnt vmcnt(0)\n\t"
      "s_sub_u32 %1, %1, 1\n\t"
      "s_cbranch_scc0 1b\n"
      "2:"
      : "+v"(v), "=&s"(cnt)
      : "s"(tag), "v"(voff), "s"(ws), "n"(SPIN_CAP)
      : "vcc", "scc", "memory");
}
DI void check_granule(rsrc_t ws, unsigned voff, unsigned tag, u32x2& v) {
  unsigned cnt, lo = v.x, hi = v.y;
  asm volatile(
      "s_mov_b32 %2, %6\n"
      "1:\n\t"
      "v_cmp_ne_u32 vcc, %3, %1\n\t"
      "s_cbranch_vccz 2f\n\t"
      "s_sleep 1\n\t"
      "buffer_load_dword %1, %4, %5, 0 offen offset:4 sc1\n\t"
      "s_waitcnt vmcnt(0)\n\t"
      "buffer_load_dword %0, %4, %5, 0 offen sc1\n\t"
      "s_waitcnt vmcnt(0)\n\t"
      "s_sub_u32 %2, %2, 1\n\t"
      "s_cbranch_scc0 1b\n"
      "2:"
      : "+v"(lo), "+v"(hi), "=&s"(cnt)
      : "s"(tag), "v"(voff), "s"(ws), "n"(SPIN_CAP)
      : "vcc", "scc", "memory");
  v.x = lo;
}
template <int CTRL> DI float dpp_add(float v) {
  return v + __builtin_bit_cast(float, __builtin_amdgcn_update_dpp(0, __builtin_bit_cast(int, v), CTRL, 0xf, 0xf, true));
}
DI float wave_sum63(float v) {
  v = dpp_add<0x111>(v); v = dpp_add<0x112>(v); v = dpp_add<0x114>(v); v = dpp_add<0x118>(v);
  v = dpp_add<0x142>(v);
  return dpp_add<0x143>(v);
}
DI s16x4 trd(LAS const char* p) { return __builtin_amdgcn_ds_read_tr16_b64_v4i16((LAS s16x4*)p); }
template <int STEP> DI bf16x8 rdW(LAS const char* p) {
  const s16x4 a = trd(p), b = trd(p + STEP);
  return __builtin_shufflevector(a, b, 0, 1, 2, 3, 4, 5, 6, 7);
}
DI unsigned pk(float lo, float hi) {
  const f32x2 v = {lo, hi};
  return __builtin_bit_cast(unsigned, __builtin_convertvector(v, bf2));
}
DI float rbf(float v) { return __builtin_bit_cast(float, pk(0.f, v) & 0xffff0000u); }
#define COLSUM(w, cs) cs = __builtin_amdgcn_mfma_f32_16x16x32_bf16(a.sel, (w), cs, 0, 0, 0)
DI bf16x8 mk_sel(int l) {
  const unsigned sl = ((l ^ (l >> 4)) & 1) ? 0u : 0x3f803f80u;
  return __builtin_bit_cast(bf16x8, u32x4{sl, sl, sl, sl});
}
DI float swap32_max(float m) {
  auto r = __builtin_amdgcn_permlane32_swap(__float_as_uint(m), __float_as_uint(m), false, false);
  return __builtin_fmaxf(__uint_as_float(r[0]), __uint_as_float(r[1]));
}
DI float swap32_add(float m) {
  auto r = __builtin_amdgcn_permlane32_swap(__float_as_uint(m), __float_as_uint(m), false, false);
  return __uint_as_float(r[0]) + __uint_as_float(r[1]);
}
DI f32x16 splat(float v) {
  f32x16 r;
#pragma unroll
  for (int i = 0; i < 16; ++i) r[i] = v;
  return r;
}
DI void ln_fix(f32x16& acc, float c, unsigned nm) {
  const unsigned u0 = __float_as_uint(c);
  const float r1 = c - __uint_as_float(u0 & 0xffff0000u);
  const unsigned u1 = __float_as_uint(r1);
  const float r2 = r1 - __uint_as_float(u1 & 0xffff0000u);
  const u32x2 a = {(u0 >> 16) | (u1 & 0xffff0000u), __float_as_uint(r2) >> 16};
  acc = __builtin_amdgcn_mfma_f32_32x32x8bf16_1k(__builtin_bit_cast(s16x4, a), __builtin_bit_cast(s16x4, u32x2{nm, nm}), acc, 0, 0, 0);
}
DI float geglu(float a, float b, float k1, float k2, float ir2) {
  const float e = __builtin_amdgcn_exp2f(a * __builtin_fmaf(a * a, k2, k1));
  return a * b * __builtin_amdgcn_rcpf(__builtin_fmaf(e, ir2, ir2));
}

DI unsigned w_voff(int lane, int pitch, int x = 0) {
  PIN(lane);
  const int r8 = lane >> 3, p8 = lane & 7;
  return r8 * pitch + (((p8 >> 1) ^ (r8 & 2) ^ x) * 32) + (p8 & 1) * 16;
}
struct Cx {
  int lane, w, g, c;
  int rt, ct, rot;
  int hd, G;
  unsigned voffC;
  unsigned cbd, cbo, hb, ob;
  rsrc_t ws, rwd, rwo;
  u32x2 tag;
  LAS char* L;
};
DI void c_setup(Cx& v) {
  v.voffC = w_voff(v.lane, 1024);
  v.cbd = uni((448 * v.g + 32 * v.c) * 1024 + v.ct * 128);
  v.cbo = uni((128 * v.g + 32 * v.c) * 1024 + v.ct * 128);
  v.hb = uni(WS_H + (2 * v.rt * 112 + 28 * v.g) * 1024);
  v.ob = uni(WS_O + (2 * v.rt * 32 + 8 * v.g) * 1024);
  v.rot = uni((v.rt & 3) * 2);
}
DI int c_grp(const Cx& v, int gp) {
  if (gp >= 7) return (gp - 7) ^ (v.rot & 1);
  const int r = gp + v.rot;
  return r >= 7 ? r - 7 : r;
}
DI unsigned c_voff(const Cx& v, int gp, int i) { return v.voffC + (c_grp(v, gp) << 16) + i * 8192; }
DI unsigned c_dst(const Cx& v, int region, int i) { return region * REG + v.g * 8192 + v.c * 4096 + i * 1024; }
DI void c_piece(const Cx& v, int gp, int region, int i) {
  glds(gp < 7 ? v.rwd : v.rwo, c_voff(v, gp, i), gp < 7 ? v.cbd : v.cbo, c_dst(v, region, i));
}

DI u32x4 w_park(const Cx& v, int n) {
  u32x4 r;
  asm volatile("buffer_load_dwordx4 %0, %1, %2, %3 offen"
               : "=v"(r) : "v"(c_voff(v, 4 + (n >> 3), n & 7)), "s"(v.rwd), "s"(v.cbd) : "memory");
  return r;
}

struct AtPre { u32x4 q, k0, k1, v0, v1; };
DI int at_kb(const Cx& v) {
  const int kb = 8 * v.G - 8 + 2 * v.w;
  return kb < 0 ? 0 : kb;
}
DI u32x4 at_load(const Cx& v, int i) {
  const int blk = i == 0 ? 8 * v.G + v.w : at_kb(v) + ((i - 1) & 1);
  return ld128h(v.ws, v.lane * 16, WS_QKV + ((i + 1) >> 1) * SEC_BYTES + (v.hd * 64 + blk) * 1024);
}
DI unsigned at_flag_off(const Cx& v) {
  const int i = v.lane & 7, isq = i >> 2;
  const int blk = at_kb(v) + (i & 1) + isq * (8 * v.G + v.w - at_kb(v) - (i & 1));
  const int sec = (1 - isq) * (1 + ((i >> 1) & 1));
  return WS_FQ + ((blk >> 2) * 24 + sec * 8 + (v.hd >> 2)) * 64 + (((v.hd >> 1) & 1) * 4 + (blk & 3)) * 8;
}
DI void at_stage(const Cx& v, const AtPre& a) {
  LAS char* kd = v.L + KV + 2 * v.w * 1024 + v.lane * 16;
  LAS char* vd = v.L + KV + 16384 + 2 * v.w * 1024 + (v.lane & 31) * 32 + (v.lane >> 5) * 16;
  *(LAS u32x4*)kd = v.w == 0 ? u32x4{0x3f803f80u, 0x3f803f80u, 0x3f803f80u, 0x3f803f80u} : a.k0;
  *(LAS u32x4*)(kd + 1024) = a.k1;
  *(LAS u32x4*)vd = a.v0;
  *(LAS u32x4*)(vd + 1024) = a.v1;
}
DI void max16(const f32x16& s, float& m, float& m2) {
#pragma unroll
  for (int r = 0; r < 16; r += 4) {
    m = __builtin_fmaxf(__builtin_fmaxf(m, s[r]), s[r + 1]);
    m2 = __builtin_fmaxf(__builtin_fmaxf(m2, s[r + 2]), s[r + 3]);
  }
}
DI void exp_pack(const f32x16& s, u32x4& p0, u32x4& p1) {
#pragma unroll
  for (int i = 0; i < 4; ++i) {
    p0[i] = pk(__builtin_amdgcn_exp2f(s[2 * i]), __builtin_amdgcn_exp2f(s[2 * i + 1]));
    p1[i] = pk(__builtin_amdgcn_exp2f(s[8 + 2 * i]), __builtin_amdgcn_exp2f(s[9 + 2 * i]));
  }
}
#define PIN4(a, o) asm volatile("" : "+v"(a[o]), "+v"(a[o + 1]), "+v"(a[o + 2]), "+v"(a[o + 3]))
#define BF(x) __builtin_bit_cast(bf16x8, x)
DI void pin8(u32x4 (&a)[8]) { PIN4(a, 0); PIN4(a, 4); }
DI void o_flag(const Cx& v, int qb) { put_flag(v.ws, v.lane, WS_FO + (v.G * 32 + v.hd) * 64 + qb * 8, v.tag); }
struct AtLane {
  LAS const char *kp, *vp;
  int vstr;
  int td;
};
#define KFRAG(kb) (*(LAS const bf16x8*)(a.kp + (kb) * 1024))
#define VFRAG(kb, s) rdW<256>(a.vp + (kb) * a.vstr + (s) * 512)
DI AtLane at_lane(const Cx& v, int qb) {
  const int lane = v.lane, h = lane >> 5, qq = (lane & 15) >> 2, p = lane & 3;
  AtLane a;
  a.kp = v.L + KV + qb * 1024 + lane * 16;
  int vreal = KV + 16384 + qb * 1024 + (4 * h + qq) * 32 + (p & 1) * 16 + (p >> 1) * 8, vone = KV + (lane & 15) * 8;
  PIN(vreal); PIN(vone);
  a.vp = v.L + ((lane & 16) ? vone : vreal);
  a.vstr = (lane & 16) ? 0 : 1024;
  a.td = (lane & 31) - 4 * h;
  return a;
}
DI f32x16 at_merge(const AtLane& a, const f32x16& sd, const f32x16& sf) {
  f32x16 r;
#pragma unroll
  for (int i = 0; i < 16; ++i) r[i] = (i & 3) + 8 * (i >> 2) <= a.td ? sd[i] : sf[i];
  return r;
}
DI u32x4 at_mask(const AtLane& a, int half) {
  u32x4 k;
#pragma unroll
  for (int i = 0; i < 4; ++i) {
    const int r0 = 2 * (i & 1) + 8 * (i >> 1) + 16 * half;
    k[i] = (r0 <= a.td ? 0xffffu : 0u) | (r0 + 1 <= a.td ? 0xffff0000u : 0u);
  }
  return k;
}
DI void o_store(const Cx& v, int qb, const f32x16& o) {
  const float il = __builtin_amdgcn_rcpf(o[8]);
  u32x4 ov;
#pragma unroll
  for (int i = 0; i < 4; ++i) ov[i] = pk(o[2 * i] * il, o[2 * i + 1] * il);
  st128h(v.ws, v.lane * 16, WS_O + ((8 * v.G + qb) * 32 + v.hd) * 1024, ov);
}
template <int GP, int N> DI u32x4 a_load(const Cx& v) {
  return ld128h<(N >> 1) * 1024>(v.ws, v.lane * 16 + (c_grp(v, GP) << 12) + (N & 1) * (GP < 7 ? 112 : 32) * 1024, GP < 7 ? v.hb : v.ob);
}
DI void w3_half(const Cx& v, int hf) {
  if (!v.c) {
#pragma unroll
    for (int i = 4 * hf; i < 4 * hf + 4; ++i) c_piece(v, 3, 4, i);
  }
}
template <bool FULL> DI void at_body(const Cx& v, const AtPre& ap, u32x4 (&af)[4][8]) {
  const AtLane a = at_lane(v, v.w);
  const bf16x8 q = BF(ap.q);
  const float NEG = -1e30f;
  const f32x16 zero = {};
  const int th = (v.lane >> 3) & 1;
  const unsigned hpoll = WS_FH + ((v.rt >> 1) * 16 + 4 * v.g + (v.lane & 3)) * 64 + (((v.lane >> 2) & 1) * 4 + ((2 * v.rt + th) & 3)) * 8;
  float m = NEG, m2 = NEG;
  f32x16 o = zero, cm, sc;
  u32x4 p0, p1;
  u32x2 fl;
  if (FULL) {
    bf16x8 kf[9];
#pragma unroll
    for (int kb = 0; kb < 9; ++kb) kf[(kb + 8) % 9] = KFRAG((kb + 8) % 9);
    if (v.w == 0) kf[0] = BF(ap.k0);
    f32x16 sa = MFMA(kf[8], q, zero), sb = MFMA(kf[0], q, zero);
    sc = MFMA(kf[1], q, zero);
    PIN(sa); PIN(sb); PIN(sc);
    SBAR();
    w3_half(v, 0);
    max16(at_merge(a, sa, sb), m, m2);
    static_for<1, 8>([&](auto K_) __attribute__((always_inline)) {
      constexpr int kb = decltype(K_)::v;
      f32x16 sn;
      if constexpr (kb < 7) { sn = MFMA(kf[kb + 1], q, zero); PIN(sn); }
      SBAR();
      if (kb == 2) w3_half(v, 1);
      if (kb == HPOLL) fl = ld64h(v.ws, hpoll);
      max16(sc, m, m2);
      PIN(m); PIN(m2);
      SBAR();
      if constexpr (kb < 7) sc = sn;
    });
    m = swap32_max(__builtin_fmaxf(m, m2));
    STAMP2(8);
    cm = splat(-m);
    sc = MFMA(kf[1], q, cm);
    bf16x8 kz = kf[0], kn = kf[2], v0 = VFRAG(1, 0), v1 = VFRAG(1, 1);
    static_for<1, 8>([&](auto K_) __attribute__((always_inline)) {
      constexpr int kb = decltype(K_)::v;
      const bf16x8 n0 = VFRAG(kb + 1, 0), n1 = VFRAG(kb + 1, 1);
      f32x16 sn;
      if constexpr (kb < 7) { sn = MFMA(kn, q, cm); PIN(sn); kn = KFRAG(kb + 2); }
      else { sa = MFMA(kn, q, cm); sb = MFMA(kz, q, cm); PIN(sa); PIN(sb); }
      SBAR();
      exp_pack(sc, p0, p1);
      o = MFMA(v0, BF(p0), o);
      o = MFMA(v1, BF(p1), o);
      PIN(o);
      if (kb == 1) {
        STAMP2(10);
        asm volatile("s_waitcnt vmcnt(0)" : "+v"(fl)::"memory");
        check_flags(v.ws, hpoll, v.tag, v.c ? v.tag : fl);
        STAMP2(11);
      }
      if (!v.c) static_for<(kb - 1) * 3, (kb < 7 ? kb * 3 : 20)>([&](auto N_) __attribute__((always_inline)) {
          af[decltype(N_)::v >> 3][decltype(N_)::v & 7] = a_load<(decltype(N_)::v >> 3), (decltype(N_)::v & 7)>(v);
        });
      SBAR();
      if constexpr (kb < 7) sc = sn;
      v0 = n0;
      v1 = n1;
    });
    sc = at_merge(a, sa, sb);
    exp_pack(sc, p0, p1);
    const u32x4 k0 = at_mask(a, 0), k1 = at_mask(a, 1);
    o = MFMA(v0, BF(p0 & k0), o);
    o = MFMA(v1, BF(p1 & k1), o);
    o = MFMA(VFRAG(0, 0), BF(p0 & ~k0), o);
    o = MFMA(VFRAG(0, 1), BF(p1 & ~k1), o);
  } else {
    w3_half(v, 0);
    w3_half(v, 1);
    max16(at_merge(a, MFMA(KFRAG(8), q, zero), splat(NEG)), m, m2);
    for (int kb = 8 - v.w; kb < 8; ++kb) max16(MFMA(KFRAG(kb), q, zero), m, m2);
    cm = splat(-swap32_max(__builtin_fmaxf(m, m2)));
    for (int kb = 8 - v.w; kb < 9; ++kb) {
      sc = MFMA(KFRAG(kb), q, cm);
      if (kb == 8) sc = at_merge(a, sc, splat(NEG));
      exp_pack(sc, p0, p1);
      o = MFMA(VFRAG(kb, 0), BF(p0), o);
      o = MFMA(VFRAG(kb, 1), BF(p1), o);
    }
    PIN(o);
    check_flags(v.ws, hpoll, v.tag, v.c ? v.tag : u32x2{~v.tag.x, 0u});
    if (!v.c) static_for<0, 20>([&](auto N_) __attribute__((always_inline)) {
      af[decltype(N_)::v >> 3][decltype(N_)::v & 7] = a_load<(decltype(N_)::v >> 3), (decltype(N_)::v & 7)>(v);
    });
  }
  o_store(v, v.w, o);
}

DI void c_scatter2(const Cx& v, LAS char* X, const f32x16& a, int nb, int j0) {
  const int t = v.lane & 31;
  LAS char* row = X + v.g * 8192 + t * 256;
  const int sw = ((v.lane >> 5) ^ (t & 15)) * 16;
#pragma unroll
  for (int j = j0; j < j0 + 2; ++j) *(LAS f32x4*)(row + (sw ^ ((4 * nb + j) * 32))) = f32x4{a[4 * j], a[4 * j + 1], a[4 * j + 2], a[4 * j + 3]};
}
DI LAS f32x4* c_cell(const Cx& v, LAS char* X, int half) {
  const int t = 8 * v.g + (v.lane >> 3);
  return (LAS f32x4*)(X + t * 256 + (((2 * (v.lane & 7) + half) ^ (t & 15)) * 16));
}
DI f32x4 c_gather(const Cx& v, LAS char* X, int half) {
  LAS const f32x4* src = c_cell(v, X, half);
  return (src[0] + src[512]) + (src[1024] + src[1536]);
}
DI f32x4 silu4(f32x4 d) {
#pragma unroll
  for (int i = 0; i < 4; ++i) d[i] *= __builtin_amdgcn_rcpf(1.f + __builtin_amdgcn_exp2f(-1.4426950408889634f * d[i]));
  return d;
}
DI u32x2 fin4(u32x2 x, f32x4 s, f32x4 g) {
  return u32x2{pk(__builtin_fmaf(s[0], g[0], __uint_as_float(x.x << 16)), __builtin_fmaf(s[1], g[1], __uint_as_float(x.x & 0xffff0000u))),
               pk(__builtin_fmaf(s[2], g[2], __uint_as_float(x.y << 16)), __builtin_fmaf(s[3], g[3], __uint_as_float(x.y & 0xffff0000u)))};
}
DI LAS char* c_xd(const Cx& v, int half) { return v.L + (1 + half) * REG; }
DI LAS char* c_xs(const Cx& v, int half) { return v.L + (half ? 0 : 4 * REG); }
DI unsigned c_xoff(const Cx& v) { return (64 * v.rt + 8 * v.w + (v.lane >> 3)) * 1024 + v.ct * 128 + (v.lane & 7) * 16; }
DI u32x4 x_load(const Params& P, const Cx& v) {
  u32x4 r;
  asm volatile("buffer_load_dwordx4 %0, %1, %2, 0 offen" : "=v"(r) : "v"(c_xoff(v)), "s"(mk_rsrc(P.x, 2048 * 512 * 2)) : "memory");
  return r;
}
DI void gemm_role(const Params& P, const Cx& v, u32x4 (&af)[4][8], u32x4 (&wp)[8], u32x4& xv) {
  const int lane = v.lane, h = lane >> 5;
  const int qq = (lane & 15) >> 2, p = lane & 3, g1 = (lane >> 4) & 1;
  LAS const char* b0 = v.L + v.g * 8192 + (4 * h + qq) * 128 + (g1 + (qq & 2)) * 32 + 8 * p;
  LAS const char* b1 = v.L + v.g * 8192 + (4 * h + qq) * 128 + (g1 + (2 ^ (qq & 2))) * 32 + 8 * p;
  const unsigned opoll = WS_FO + ((v.rt >> 2) * 32 + 8 * v.g + (lane & 7)) * 64 + ((2 * v.rt + ((lane >> 3) & 1)) & 7) * 8;
  constexpr int RG[4] = {0, 1, 2, 4};
  f32x16 d[2][2] = {}, sm[2][2] = {};
  bf16x8 wf[4][2];
  u32x2 fl;
  auto rd = [&](auto S_) __attribute__((always_inline)) {
    constexpr int s = decltype(S_)::v;
    wf[s & 3][0] = rdW<1024>(b0 + RG[(s >> 2) & 3] * REG + (s & 3) * 2048);
    wf[s & 3][1] = rdW<1024>(b1 + RG[(s >> 2) & 3] * REG + (s & 3) * 2048);
  };
  auto areq = [&](auto G_, auto N_) __attribute__((always_inline)) {
    af[decltype(G_)::v & 3][decltype(N_)::v] = a_load<decltype(G_)::v, decltype(N_)::v>(v);
  };
  rd(IC<0>{});
  rd(IC<1>{});
  static_for<4, 8>([&](auto N_) __attribute__((always_inline)) { areq(IC<2>{}, N_); });
  __builtin_amdgcn_s_setprio(0);
  wait_vm<17>();
  pin8(af[0]);
  auto walk = [&](auto S_) __attribute__((always_inline)) {
    constexpr int s = decltype(S_)::v, gp = s >> 2, k = s & 3;
    if (k == 0) STAMP2(40 + gp);
    if constexpr (k == 2) {
      constexpr int N[8] = {12, 9, 16, 24, 8, 63, 0, 1};
      if constexpr (gp == 4) {
        asm volatile("s_waitcnt vmcnt(8)" : "+v"(fl)::"memory");
        check_flags(v.ws, opoll, v.tag, fl);
      } else wait_vm<N[gp]>();
      pin8(af[(gp + 1) & 3]);
      if (gp == 0) o_flag(v, v.w);
    }
    rd(IC<s + 2>{});
    static_for<0, 4>([&](auto J_) __attribute__((always_inline)) {
      constexpr int j = decltype(J_)::v, n = 4 * k + j;
      f32x16& acc = (gp < 7 ? d : sm)[j >> 1][j & 1];
      acc = MFMA(wf[s & 3][j >> 1], BF(af[gp & 3][2 * k + (j & 1)]), acc);
      PIN(acc);
      SBAR();
      if constexpr (gp == 0 && n < 8) areq(IC<3>{}, IC<n>{});
      if constexpr (gp == 1 && n < 8) *(LAS u32x4*)(v.L + c_dst(v, 0, n) + lane * 16) = wp[n];
      if constexpr (gp >= 2 && gp < 6 && n < 8) c_piece(v, gp + 3, RG[(gp + 3) & 3], n);
      if constexpr (gp >= 1 && gp < 6 && n >= 8) areq(IC<gp + 3>{}, IC<n - 8>{});
      if (s == 15 && j == 3) fl = ld64h(v.ws, opoll);
      SBAR();
    });
  };
  auto close = [&](auto G_, f32x16 (&ac)[2][2], LAS char* x0, LAS char* x1) __attribute__((always_inline)) {
    constexpr int gp = decltype(G_)::v;
    STAMP2(40 + gp);
    rd(IC<4 * gp + 2>{});
    rd(IC<4 * gp + 3>{});
    static_for<0, 5>([&](auto J_) __attribute__((always_inline)) {
      constexpr int j = decltype(J_)::v;
      static_for<0, 4>([&](auto K_) __attribute__((always_inline)) {
        constexpr int k = decltype(K_)::v;
        if constexpr (j < 4) {
          ac[j & 1][j >> 1] = MFMA(wf[k][j & 1], BF(af[gp & 3][2 * k + (j >> 1)]), ac[j & 1][j >> 1]);
          PIN(ac[j & 1][j >> 1]);
          SBAR();
        }
        if constexpr (j > 0 && (k & 1)) c_scatter2(v, j > 2 ? x1 : x0, ac[(j - 1) & 1][(j - 1) >> 1], (j - 1) & 1, k - 1);
        SBAR();
      });
    });
  };
  static_for<0, 24>(walk);
  xv = x_load(P, v);
  close(IC<6>{}, d, c_xd(v, 0), c_xd(v, 1));
  wait_vm<17>();
  pin8(af[3]);
  rd(IC<28>{});
  rd(IC<29>{});
  wait_bar<63, 8>();
  static_for<28, 32>(walk);
  close(IC<8>{}, sm, c_xs(v, 0), c_xs(v, 1));
  STAMP(14);
}
DI void side_role(const Params& P, const Cx& v, f32x4& ga, f32x4& gb, u32x4& xv) {
  wait_vm<0>();
  o_flag(v, v.w);
  __builtin_amdgcn_s_setprio(0);
  xv = x_load(P, v);
  if (blockIdx.x == 0) check_flags(v.ws, WS_FO + (v.g * 64 + v.lane) * 64, v.tag, u32x2{~v.tag.x, 0u});
  wait_bar<63, 0>();
#pragma unroll
  for (int hf = 0; hf < 2; ++hf) *c_cell(v, c_xd(v, 0), hf) = silu4(c_gather(v, c_xd(v, 0), hf));
  ga = silu4(c_gather(v, c_xd(v, 1), 0));
  gb = silu4(c_gather(v, c_xd(v, 1), 1));
}

struct AB {
  bf16x8 X[32];
  bf16x8 wf[3][2];
  bf16x8 sel;
  unsigned nm;
  float r, k1, k2, ir2;
  int gr;
  int rb, cg, strip0, dblA, dblB;
  int kx;
  unsigned voffX, voffQ, voffF;
  rsrc_t rx, rq, rf;
};
DI void x_piece(const Cx& v, const AB& a, int kc, int slot, int i) {
  glds(a.rx, a.voffX ^ (64 * i), (a.rb * 128 + 16 * v.w + 4 * i) * 1024 + (kc ^ a.kx) * 256, slot * SLOT + (4 * v.w + i) * 1024);
}
DI void s_piece(const Cx& v, const AB& a, int kq, int i) {
  glds(a.rq, a.voffQ, (128 * (kq ^ a.kx) + 16 * v.w + 8 * i) * 3072 + a.strip0 * 128, SBUF + (kq & 1) * SUB1 + (2 * v.w + i) * 1024);
}
DI void f_piece(const Cx& v, const AB& a, int dbl, int kq, int slot, int i) {
  glds(a.rf, a.voffF, (128 * (kq ^ a.kx) + 32 * v.g + 8 * i) * 3584 + dbl * 128, slot * SLOT + v.c * SUB1 + v.g * 4096 + i * 1024);
}
template <bool TB> DI void u_piece(const Cx& v, const AB& a, int kq, int i) {
  const int slot = 1 + 2 * (kq & 1);
  if (TB) glds(a.rq, a.voffQ, (128 * (kq ^ a.kx) + 32 * v.g + 8 * i) * 3072 + (a.strip0 + 1 + v.c) * 128,
               slot * SLOT + v.c * SUB1 + v.g * 4096 + i * 1024);
  else f_piece(v, a, a.dblA, kq, slot, i);
}
DI void cs_put(const Cx& v, const f32x4& c0, const f32x4& c1, const f32x4& c2) {
  const bool hi = v.lane & 16;
  *(LAS f32x4*)(v.L + AUX + v.w * 1024 + v.lane * 16) = f32x4{hi ? c0[1] : c0[0], hi ? c1[1] : c1[0], hi ? c2[1] : c2[0], 0.f};
}
DI f32x4 cs_get(const Cx& v) {
  LAS const f32x4* p = (LAS const f32x4*)(v.L + AUX + v.c * 4096 + v.lane * 16);
  return (p[0] + p[64]) + (p[128] + p[192]);
}
DI void q_store(const Cx& v, const AB& a, const f32x16& acc, int strip, int hh) {
  const float sc = strip < 8 ? a.r * QSCALE : a.r;
  u32x4 o;
#pragma unroll
  for (int i = 0; i < 4; ++i) o[i] = pk(acc[8 * hh + 2 * i] * sc, acc[8 * hh + 2 * i + 1] * sc);
  st128h(v.ws, v.lane * 16, WS_QKV + ((strip * 4 + 2 * v.c + hh) * 64 + 4 * a.rb + v.g) * 1024, o);
}
DI void q_flag(const Cx& v, const AB& a, int strip) { put_flag(v.ws, v.lane, WS_FQ + (a.rb * 24 + strip) * 64 + v.w * 8, v.tag); }
DI void put_h(const Cx& v, const AB& a, int dbl, int j, const u32x4& hv) {
  st128h(v.ws, v.lane * 16, WS_H + ((4 * a.rb + v.g) * 112 + 4 * dbl + 2 * v.c + j) * 1024, hv);
}
struct Bases { unsigned b[4]; };
template <bool F> DI Bases mk_bases(const Cx& v, const AB& a, int base) {
  int l = v.lane;
  PIN(l);
  const int qq = (l & 15) >> 2, g1 = (l >> 4) & 1;
  const int o = base + (F ? g1 * SUB1 : 0) + (8 * (l >> 5) + qq) * 128 + ((2 * v.c) ^ (qq & 2) ^ g1) * 32 + 8 * (l & 3);
  Bases r;
#pragma unroll
  for (int i = 0; i < 4; ++i) { r.b[i] = o + (i ^ a.gr) * 4096; PIN(r.b[i]); }
  return r;
}
DI void bump(Bases& B, int d) {
#pragma unroll
  for (int i = 0; i < 4; ++i) { B.b[i] += d; PIN(B.b[i]); }
}
#define WRD(B, off, i) rdW<512>((LAS const char*)(size_t)((B).b[(i) >> 1]) + (off) + ((i) & 1) * 2048)
#define WRX(B, off, i) rdW<512>((LAS const char*)(size_t)((B).b[(i) >> 1] ^ 32u) + (off) + ((i) & 1) * 2048)

DI u32x2 make_tag(u32x2 e) {
  const unsigned long long z = ((unsigned long long)__builtin_amdgcn_readfirstlane(e.y) << 32 | (unsigned)__builtin_amdgcn_readfirstlane(e.x));
  const unsigned long long t = (z ^ (z >> 31)) * 0x9E3779B97F4A7C15ull + 0xD1B54A32D192ED03ull;
  return u32x2{(unsigned)t, (unsigned)(t >> 32)};
}

DI void stats_sum(const u32x4& xr, float& s1, float& s2) {
  s1 = s2 = 0.f;
#pragma unroll
  for (int d = 0; d < 4; ++d) {
    const float lo = __uint_as_float(xr[d] << 16), hi = __uint_as_float(xr[d] & 0xffff0000u);
    s1 += lo + hi;
    s2 = __builtin_fmaf(lo, lo, __builtin_fmaf(hi, hi, s2));
  }
}
DI void stats_put(const Cx& v, const AB& a, float s1, float s2) {
  const float mean = s1 * (1.f / 512.f);
  const float var = __builtin_fmaxf(__builtin_fmaf(-mean, mean, s2 * (1.f / 512.f)), 0.f);
  const float sd = __builtin_amdgcn_sqrtf(rbf(rbf(var) + 1e-5f));
  put_flag(v.ws, v.lane ^ 63, WS_ST + (a.rb * 128 + a.cg * 8 + v.w) * 8, u32x2{pk(-mean, sd), v.tag.x});
}
DI void row_stats(const Cx& v, AB& a, unsigned g) {
  const float sd = __uint_as_float(g & 0xffff0000u);
  const float c1 = -2.f * 0.7978845608028654f * 1.4426950408889634f;
  a.r = __builtin_amdgcn_rcpf(sd);
  a.k1 = c1 * a.r;
  a.k2 = c1 * 0.044715f * a.r * a.r * a.r;
  a.ir2 = sd * sd;
  a.nm = v.lane < 32 ? (g & 0xffffu) | (g << 16) : 0u;
}
DI void rd_step(AB& a, const Bases& B, int s) {
  a.wf[(s + 2) % 3][0] = WRD(B, ((s >> 3) & 1) * SLOT, s & 7);
  a.wf[(s + 2) % 3][1] = WRX(B, ((s >> 3) & 1) * SLOT, s & 7);
}
template <bool TB, int Q>
DI void f_step(Cx& v, AB& a, const Bases& BS, Bases& BU, f32x16& ps, f32x16& a0, f32x16& a1, f32x4& cs, f32x4& c0, f32x4& c1,
               u32x2& st, u32x4& xr) {
  const int t = v.lane & 31;
  const unsigned u = (((v.lane >> 5) ^ (t & 15)) * 16) ^ (64 * a.gr);
  LAS const char* xb = v.L + (32 * v.g + t) * 256;
  bf16x8 ws[3];
  float s1, s2;
  f32x4 cp;
  auto rdx = [&](int q, int i) __attribute__((always_inline)) { a.X[8 * q + i] = *(LAS const bf16x8*)(xb + 2 * (q & 1) * SLOT + (u ^ (32 * i))); };
  auto rd = [&](int i) __attribute__((always_inline)) {
    a.wf[i % 3][0] = WRD(BU, 0, i);
    a.wf[i % 3][1] = TB ? WRD(BU, SUB1, i) : WRX(BU, 0, i);
    ws[i % 3] = WRD(BS, (Q & 1) * SUB1, i);
  };
  auto req = [&](int n) __attribute__((always_inline)) {
    if (Q < 3) { if (n < 2) s_piece(v, a, Q + 1, n); else u_piece<TB>(v, a, Q + 1, n - 2); }
    else if (n < 4) f_piece(v, a, a.dblB, 1, 1, n);
  };
  if (Q == 0) { rdx(0, 0); rdx(0, 1); }
  rd(0);
  rd(1);
  static_for<0, 8>([&](auto I_) __attribute__((always_inline)) {
    constexpr int i = decltype(I_)::v;
    if (i == 4) {
      if (Q == 0) wait_bar<8, 0>();
      else if (Q < 3) wait_bar<Q == 2 ? 8 : 6, 15>();
      else {
        wait_bar<4, 6>();
        cp = cs_get(v);
      }
    }
    const bf16x8 x = a.X[8 * Q + i];
    a0 = MFMA(a.wf[i % 3][0], x, a0);
    PIN(a0);
    SBAR();
    if (Q == 0 && i < 3) { rdx(0, 2 * i + 2); rdx(0, 2 * i + 3); }
    if (Q < 3 && i >= 4) { rdx(Q + 1, 2 * i - 8); rdx(Q + 1, 2 * i - 7); }
    if (i < 2) { COLSUM(a.wf[i % 3][0], c0); PIN(c0); }
    if (i < 3) req(2 * i);
    if (i >= 4) {
      if (Q < 2) x_piece(v, a, Q + 2, 2 * (Q & 1), i - 4);
      else f_piece(v, a, a.dblB, 2 * (Q & 1), 2 * (Q & 1), i - 4);
    }
    if (Q == 3 && i == 5) { ln_fix(a0, cp[0], a.nm); PIN(a0); }
    SBAR();
    a1 = MFMA(a.wf[i % 3][1], x, a1);
    PIN(a1);
    SBAR();
    if (i < 2) { COLSUM(a.wf[i % 3][1], c1); PIN(c1); }
    if (i < 3) req(2 * i + 1);
    if (Q == 3 && i == 6) { ln_fix(a1, cp[1], a.nm); PIN(a1); }
    if (Q == 0 && i == 3) {
      st = ld64h(v.ws, WS_EPOCH);
      asm volatile("buffer_load_dwordx4 %0, %1, %2, %3 offen"
                   : "=v"(xr) : "v"(v.lane * 16), "s"(a.rx), "s"(RFL((a.rb * 128 + a.cg * 8 + v.w) * 1024)) : "memory");
    }
    if (Q == 1) {
      if (i == 3) { PIN(st); PIN(xr); v.tag = make_tag(st); stats_sum(xr, s1, s2); PIN(s1); PIN(s2); }
      if (i == 4) { s1 = wave_sum63(s1); PIN(s1); }
      if (i == 5) { s2 = wave_sum63(s2); PIN(s2); }
      if (i == 6) stats_put(v, a, s1, s2);
    }
    if (Q == 2 && i == 3) st = ld64h(v.ws, WS_ST + (a.rb * 128 + 32 * v.g + (v.lane & 31)) * 8);
    if (Q == 3 && i == 2) {
      PIN(st);
      check_granule(v.ws, WS_ST + (a.rb * 128 + 32 * v.g + (v.lane & 31)) * 8, v.tag.x, st);
      row_stats(v, a, st.x);
      PIN(a.nm); PIN(a.r); PIN(a.k1); PIN(a.k2); PIN(a.ir2);
    }
    SBAR();
    const bf16x8 wsi = ws[i % 3];
    ps = MFMA(wsi, x, ps);
    PIN(ps);
    SBAR();
    if (i < 6) rd(i + 2);
    if (i < 2) { COLSUM(wsi, cs); PIN(cs); }
    if (Q == 3) {
      if (i == 2) cs_put(v, c0, c1, cs);
      if (i == 4) { ln_fix(ps, cp[2], a.nm); PIN(ps); }
      if (i == 6) BU = mk_bases<true>(v, a, 0);
      if (i >= 6) rd_step(a, BU, i - 6);
    } else if (i == 6) bump(BU, (Q & 1) ? -2 * SLOT : 2 * SLOT);
    if (Q == 1 && i == 7) {
      const int per = v.w == 1 ? 16 : 56;
      asm volatile("buffer_load_dword %0, %1, %2, 0 offen"
                   : "=v"(xr.x) : "v"(v.lane < per && v.w < 2 ? (blockIdx.x * per + v.lane) * 128 : 0x40000000u), "s"(v.w == 0 ? v.rwd : v.rwo)
                   : "memory");
    }
    SBAR();
  });
}
template <bool TB> DI void front(Cx& v, AB& a, Bases& BU, f32x16& ps, f32x16& a0, f32x16& a1) {
  const Bases BS = mk_bases<false>(v, a, SBUF);
  BU = mk_bases<!TB>(v, a, SLOT);
  f32x4 cs = {}, c0 = {}, c1 = {};
  u32x2 st;
  u32x4 xr;
  STAMP2(23);
  wait_bar<4, 0>();
  STAMP(1);
  f_step<TB, 0>(v, a, BS, BU, ps, a0, a1, cs, c0, c1, st, xr);
  STAMP2(16); wait_bar<4, 0>(); STAMP2(17);
  f_step<TB, 1>(v, a, BS, BU, ps, a0, a1, cs, c0, c1, st, xr);
  STAMP2(18); wait_bar<6, 0>(); STAMP2(19);
  f_step<TB, 2>(v, a, BS, BU, ps, a0, a1, cs, c0, c1, st, xr);
  STAMP2(20); wait_bar<4, 0>(); STAMP2(21);
  PIN(xr);
  f_step<TB, 3>(v, a, BS, BU, ps, a0, a1, cs, c0, c1, st, xr);
  STAMP2(22);
  wait_bar<4, 8>();
}
template <bool TB> DI void body(Cx& v, AB& a, Bases& B, const f32x16& ps, f32x16& p0, f32x16& p1, AtPre& ap) {
  f32x16 a0 = {}, a1 = {};
  f32x4 c0 = {}, c1 = {};
  float hlo;
  f32x4 cp;
  u32x4 hv;
  u32x2 fl;
  unsigned fo;
  static_for<0, 32>([&](auto S_) __attribute__((always_inline)) {
    constexpr int s = decltype(S_)::v, kq = s >> 3, i = s & 7;
    if (s == 2) { int l = v.lane; PIN(l); a.sel = mk_sel(l); }
    const bf16x8 w0 = a.wf[(s + 2) % 3][0], w1 = a.wf[(s + 2) % 3][1];
    constexpr bool CS = (i >> 1) == 1;
    constexpr int e = TB ? -1 : (s == 30 ? 14 : ((s & 1) && s >= 3 ? (s == 31 ? 15 : (s - 3) >> 1) : -1));
    float ga, ex;
    a0 = MFMA(w0, a.X[s], a0);
    PIN(a0);
    SBAR();
    if (s == 14) bump(B, 2 * SLOT);
    if (s < 30) rd_step(a, B, s + 2);
    if (s == 7) c_setup(v);
    if (CS) { COLSUM(w0, c0); PIN(c0); }
    if ((i & 1) == 0) {
      if (kq == 0) f_piece(v, a, a.dblB, 3, 3, i >> 1);
      else c_piece(v, kq - 1, kq - 1, i >> 1);
    }
    if constexpr (e >= 0) {
      ga = (e < 8 ? p0 : p1)[e & 7];
      ex = __builtin_amdgcn_exp2f(ga * __builtin_fmaf(ga * ga, a.k2, a.k1));
      ga *= (e < 8 ? p0 : p1)[8 + (e & 7)];
      PIN(ga); PIN(ex);
    }
    SBAR();
    a1 = MFMA(w1, a.X[s], a1);
    PIN(a1);
    SBAR();
    if (CS) { COLSUM(w1, c1); PIN(c1); }
    if constexpr (e >= 0) {
      const float hx = ga * __builtin_amdgcn_rcpf(__builtin_fmaf(ex, a.ir2, a.ir2));
      if (e & 1) { hv[(e & 7) >> 1] = pk(hlo, hx); PIN(hv[(e & 7) >> 1]); }
      else { hlo = hx; PIN(hlo); }
      if (e == 15) put_h(v, a, a.dblA, 1, hv);
    }
    if (s < 2) q_store(v, a, ps, a.strip0, s);
    if (s == 8) { wait_vm<4>(); q_flag(v, a, a.strip0); }
    if (!TB && s == 18) put_h(v, a, a.dblA, 0, hv);
    if (TB) {
      if (s == 3 || s == 5) q_store(v, a, p0, a.strip0 + 1, (s - 3) >> 1);
      if (s == 7 || s == 9) q_store(v, a, p1, a.strip0 + 2, (s - 7) >> 1);
      if (s == 14) { wait_vm<3>(); q_flag(v, a, a.strip0 + 1); q_flag(v, a, a.strip0 + 2); }
    }
    if (s == 28) {
      cs_put(v, c0, c1, c1);
      SBAR();
      wait_bar<63, 0>();
      cp = cs_get(v);
    }
    if (s == 30) { ln_fix(a0, cp[0], a.nm); PIN(a0); }
    if (s == 31) { ln_fix(a1, cp[1], a.nm); PIN(a1); }
    if (s == 23) { fo = at_flag_off(v); fl = ld64h(v.ws, fo); }
    if (s == 27) {
      asm volatile("s_waitcnt vmcnt(2)" : "+v"(fl)::"memory");
      check_flags(v.ws, fo, v.tag, fl);
      ap.k0 = at_load(v, 1);
    }
    if (s == 28) ap.k1 = at_load(v, 2);
    if (s == 29) ap.v0 = at_load(v, 3);
    if (s == 30) ap.v1 = at_load(v, 4);
    if (s == 31) ap.q = at_load(v, 0);
    SBAR();
    if (i == 7 && kq < 3) {
      STAMP2(36 + kq);
      if (kq < 2) wait_bar<4, 8>();
      else wait_bar<63, 8>();
      STAMP2(54 + kq);
    }
  });
  p0 = a0;
  p1 = a1;
}
DI void ab_tail(const Cx& v, const AB& a, int dbl, const f32x16& p0, const f32x16& p1, AtPre& ap, u32x4 (&wp)[8]) {
  u32x4 hv;
  static_for<0, 8>([&](auto I_) __attribute__((always_inline)) {
    constexpr int i = decltype(I_)::v, j = 2 * (i & 3);
    const f32x16& p = i < 4 ? p0 : p1;
    if constexpr (i < 4) { if (!v.c) { wp[2 * i] = w_park(v, 2 * i); wp[2 * i + 1] = w_park(v, 2 * i + 1); } }
    hv[i & 3] = pk(geglu(p[j], p[8 + j], a.k1, a.k2, a.ir2), geglu(p[j + 1], p[9 + j], a.k1, a.k2, a.ir2));
    PIN(hv[i & 3]);
    if constexpr ((i & 3) == 3) put_h(v, a, dbl, i >> 2, hv);
    SBAR();
  });
  if (v.c) wait_vm<2>();
  asm volatile("s_waitcnt vmcnt(10)\n\ts_barrier" : "+v"(ap.q), "+v"(ap.k0), "+v"(ap.k1), "+v"(ap.v0), "+v"(ap.v1)::"memory");
  at_stage(v, ap);
  wait_vm<0>();
  put_flag(v.ws, v.lane, WS_FH + (a.rb * 16 + a.cg) * 64 + v.w * 8, v.tag);
  wait_bar<63, 0>();
  PIN4(wp, 0); PIN4(wp, 4);
}

template <bool TB> DI void ab_path(Cx& v, AB a, AtPre& ap, u32x4 (&wp)[8]) {
  int l = v.lane;
  PIN(l);
  a.voffX = (l >> 4) * 1024 + (((l & 15) ^ (l >> 4)) * 16);
  a.voffQ = w_voff(l, 3072);
  a.voffF = w_voff(l, 3584, v.c);
  a.sel = mk_sel(l);
#pragma unroll
  for (int i = 0; i < 4; ++i) u_piece<TB>(v, a, 0, i);
#pragma unroll
  for (int i = 0; i < 4; ++i) x_piece(v, a, 1, 2, i);
  f32x16 ps = {}, p0 = {}, p1 = {};
  Bases B;
  front<TB>(v, a, B, ps, p0, p1);
  STAMP(3);
  body<TB>(v, a, B, ps, p0, p1, ap);
  STAMP(4);
  ab_tail(v, a, a.dblB, p0, p1, ap, wp);
}

__global__ __launch_bounds__(512, 2) void fused_kernel(Params P) {
  extern __shared__ __attribute__((aligned(16))) char smem[];
  Cx v;
  v.L = (LAS char*)smem;
  v.lane = threadIdx.x & 63;
  v.w = __builtin_amdgcn_readfirstlane(threadIdx.x >> 6);
  v.g = v.w & 3;
  v.c = v.w >> 2;
  const int xc = blockIdx.x & 7, idx = blockIdx.x >> 3;
  v.ws = mk_rsrc(P.ws, WS_END);
  v.rwd = mk_rsrc(P.wd, 1792 * 512 * 2);
  v.rwo = mk_rsrc(P.wo, 512 * 512 * 2);
  v.rt = v.hd = xc * 4 + (idx & 3);
  v.ct = v.G = idx >> 2;
  AtPre ap;
  u32x4 wp[8];
  STAMPR(30); STAMP(0);
  {
    AB a;
    a.rb = (xc & 1) * 8 + (idx & 7);
    const int cq = xc >> 1, j = idx >> 3;
    const bool typeB = j == 3;
    a.cg = cq * 4 + j;
    a.gr = v.g ^ (2 * v.c);

    a.strip0 = 6 * cq + j;
    const int swp = typeB ? 0 : (a.rb >> 2) & SWP;
    a.dblA = 7 * cq + 2 * j + swp;
    a.dblB = typeB ? a.dblA : 7 * cq + 2 * j + 1 - swp;
    a.kx = a.rb & KXM;
    a.rx = mk_rsrc(P.x, 2048 * 512 * 2);
    a.rq = mk_rsrc(P.wqkv, 512 * 1536 * 2);
    a.rf = mk_rsrc(v.c ? P.wu : P.wg, 512 * 1792 * 2);
    a.voffX = (v.lane >> 4) * 1024 + (((v.lane & 15) ^ (v.lane >> 4)) * 16);
    a.voffQ = w_voff(v.lane, 3072);
    STAMP2(24);
#pragma unroll
    for (int i = 0; i < 4; ++i) x_piece(v, a, 0, 0, i);
#pragma unroll
    for (int i = 0; i < 2; ++i) s_piece(v, a, 0, i);
    if (typeB) ab_path<true>(v, a, ap, wp);
    else ab_path<false>(v, a, ap, wp);
  }
  STAMP(6);
  c_setup(v);
  v.tag = u32x2{uni(v.tag.x), uni(v.tag.y)};
  u32x4 af[4][8];
  __builtin_amdgcn_s_setprio(2);
  if (v.G) at_body<true>(v, ap, af);
  else at_body<false>(v, ap, af);
  STAMP(7);
  c_setup(v);
  f32x4 ga, gb;
  u32x4 xv;
  if (v.c) side_role(P, v, ga, gb, xv);
  else gemm_role(P, v, af, wp, xv);
  asm volatile("s_waitcnt vmcnt(0) lgkmcnt(0)\n\ts_barrier" : "+v"(xv)::"memory");
  if (blockIdx.x == 0 && v.w == 0) put_flag(v.ws, v.lane, WS_EPOCH, v.tag);
  if (!v.c) {
    ga = *c_cell(v, c_xd(v, 0), 0);
    gb = *c_cell(v, c_xd(v, 0), 1);
  }
  const u32x2 y0 = fin4(u32x2{xv.x, xv.y}, c_gather(v, c_xs(v, v.c), 0), ga), y1 = fin4(u32x2{xv.z, xv.w}, c_gather(v, c_xs(v, v.c), 1), gb);
  __builtin_amdgcn_raw_buffer_store_b128(u32x4{y0.x, y0.y, y1.x, y1.y}, mk_rsrc(P.out, 2048 * 512 * 2), c_xoff(v), 0, 0);
  STAMP(15); STAMPR(31);
}
}

extern "C" void kernel_launch(void* const* d_in, const int* in_sizes, int n_in, void* d_out, int out_size, void* d_ws,
                              size_t ws_size, hipStream_t stream) {
  Params P;
  P.x = d_in[0]; P.wqkv = d_in[1]; P.wo = d_in[2];
  P.wg = d_in[4]; P.wu = d_in[5]; P.wd = d_in[6];
  P.out = d_out; P.ws = d_ws;
  (void)hipFuncSetAttribute((const void*)fused_kernel, hipFuncAttributeMaxDynamicSharedMemorySize, LDS_BYTES);
  fused_kernel<<<256, 512, LDS_BYTES, stream>>>(P);
}
```

```cpp
#define PFCODE
#include <hip/hip_runtime.h>
#include <hip/hip_bf16.h>
#include <stdint.h>

typedef short bf16x8 __attribute__((ext_vector_type(8)));
typedef short s16x4 __attribute__((ext_vector_type(4)));
typedef float f32x16 __attribute__((ext_vector_type(16)));
typedef float f32x4 __attribute__((ext_vector_type(4)));
typedef float f32x2 __attribute__((ext_vector_type(2)));
typedef unsigned u32x4 __attribute__((ext_vector_type(4)));
typedef unsigned u32x2 __attribute__((ext_vector_type(2)));
typedef __bf16 bf2 __attribute__((ext_vector_type(2)));
typedef __amdgpu_buffer_rsrc_t rsrc_t;
#define LAS __attribute__((address_space(3)))
#define DI __device__ __forceinline__
#define SBAR() __builtin_amdgcn_sched_barrier(0)
#define PIN(x) asm volatile("" : "+v"(x))
#define RFL(x) __builtin_amdgcn_readfirstlane(x)
#define MFMA(a, b, c) __builtin_amdgcn_mfma_f32_32x32x16_bf16((a), (b), (c), 0, 0, 0)

#define STAMP(i)
#define STAMPR(i)
#define STAMP_FLUSH()
#define STAMP2(i)

#define KXM 3
#define SWP 0
#define HPOLL 6
namespace {
constexpr unsigned WS_EPOCH = 0;
constexpr unsigned WS_FQ = 1024;
constexpr unsigned WS_FH = WS_FQ + 16 * 24 * 64;
constexpr unsigned WS_FO = WS_FH + 16 * 16 * 64;
constexpr unsigned WS_ST = 65536;
constexpr unsigned WS_QKV = 131072;
constexpr unsigned SEC_BYTES = 32 * 64 * 1024;
constexpr unsigned WS_H = WS_QKV + 3 * SEC_BYTES;
constexpr unsigned WS_O = WS_H + 64 * 112 * 1024;
constexpr unsigned WS_END = WS_O + 64 * 32 * 1024;
static_assert(WS_FO + 256 * 64 <= WS_ST, "flags overlap");
constexpr int SLOT = 32768, SUB1 = 16384;
constexpr int SBUF = 4 * SLOT;
constexpr int AUX = SBUF;
constexpr int REG = 32768;
constexpr int LDS_BYTES = 163840;
constexpr int KV = 3 * REG;
constexpr float QSCALE = 0.25f * 1.4426950408889634f;
constexpr unsigned SPIN_CAP = 1u << 18;

struct Params {
  const void *x, *wqkv, *wo, *wg, *wu, *wd;
  void *out, *ws;
};

template <int I> struct IC { static constexpr int v = I; };
template <int I, int N, class F> DI void static_for(F&& f) {
  if constexpr (I < N) {
    f(IC<I>{});
    static_for<I + 1, N>(f);
  }
}
template <int VM> DI void wait_vm() { asm volatile("s_waitcnt vmcnt(%0)" ::"n"(VM) : "memory"); }
template <int VM, int LG> DI void wait_bar() { asm volatile("s_waitcnt vmcnt(%0) lgkmcnt(%1)\n\ts_barrier" ::"n"(VM), "n"(LG) : "memory"); }
DI unsigned uni(unsigned x) {
  x = __builtin_amdgcn_readfirstlane(x);
  asm volatile("" : "+s"(x));
  return x;
}
DI rsrc_t mk_rsrc(const void* p, unsigned bytes) { return __builtin_amdgcn_make_buffer_rsrc((void*)p, 0, bytes, 0x00020000); }
DI void glds(rsrc_t rs, unsigned voff, unsigned soff, unsigned lds) {
  asm volatile("s_mov_b32 m0, %2\n\ts_nop 0\n\tbuffer_load_dwordx4 %0, %1, %3 offen lds" ::"v"(voff), "s"(rs),
               "s"(RFL(lds)), "s"(RFL(soff)) : "memory");
}
template <int IMM = 0> DI u32x4 ld128h(rsrc_t rs, unsigned voff, unsigned soff) {
  u32x4 r;
  asm volatile("buffer_load_dwordx4 %0, %1, %2, %3 offen offset:%4 sc1" : "=v"(r) : "v"(voff), "s"(rs), "s"(RFL(soff)), "n"(IMM) : "memory");
  return r;
}
DI u32x2 ld64h(rsrc_t rs, unsigned voff) {
  u32x2 r;
  asm volatile("buffer_load_dwordx2 %0, %1, %2, 0 offen sc1" : "=v"(r) : "v"(voff), "s"(rs) : "memory");
  return r;
}
DI void st128h(rsrc_t rs, unsigned voff, unsigned soff, u32x4 d) {
  asm volatile("buffer_store_dwordx4 %0, %1, %2, %3 offen sc1\n\ts_nop 1" ::"v"(d), "v"(voff), "s"(rs), "s"(RFL(soff)) : "memory");
}
DI void put_flag(rsrc_t ws, int lane, unsigned soff, u32x2 tag) {
  asm volatile("buffer_store_dwordx2 %0, %1, %2, %3 offen sc1" ::"v"(tag), "v"(lane ? 0x80000000u : 0u), "s"(ws), "s"(RFL(soff)) : "memory");
}
DI void check_flags(rsrc_t ws, unsigned voff, u32x2 tag, u32x2 v) {
  unsigned cnt;
  asm volatile(
      "s_mov_b32 %1, %5\n"
      "1:\n\t"
      "v_cmp_ne_u64 vcc, %2, %0\n\t"
      "s_cbranch_vccz 2f\n\t"
      "s_sleep 1\n\t"
      "buffer_load_dwordx2 %0, %3, %4, 0 offen sc1\n\t"
      "s_waitcnt vmcnt(0)\n\t"
      "s_sub_u32 %1, %1, 1\n\t"
      "s_cbranch_scc0 1b\n"
      "2:"
      : "+v"(v), "=&s"(cnt)
      : "s"(tag), "v"(voff), "s"(ws), "n"(SPIN_CAP)
      : "vcc", "scc", "memory");
}
DI void check_granule(rsrc_t ws, unsigned voff, unsigned tag, u32x2& v) {
  unsigned cnt, lo = v.x, hi = v.y;
  asm volatile(
      "s_mov_b32 %2, %6\n"
      "1:\n\t"
      "v_cmp_ne_u32 vcc, %3, %1\n\t"
      "s_cbranch_vccz 2f\n\t"
      "s_sleep 1\n\t"
      "buffer_load_dword %1, %4, %5, 0 offen offset:4 sc1\n\t"
      "s_waitcnt vmcnt(0)\n\t"
      "buffer_load_dword %0, %4, %5, 0 offen sc1\n\t"
      "s_waitcnt vmcnt(0)\n\t"
      "s_sub_u32 %2, %2, 1\n\t"
      "s_cbranch_scc0 1b\n"
      "2:"
      : "+v"(lo), "+v"(hi), "=&s"(cnt)
      : "s"(tag), "v"(voff), "s"(ws), "n"(SPIN_CAP)
      : "vcc", "scc", "memory");
  v.x = lo;
}
template <int CTRL> DI float dpp_add(float v) {
  return v + __builtin_bit_cast(float, __builtin_amdgcn_update_dpp(0, __builtin_bit_cast(int, v), CTRL, 0xf, 0xf, true));
}
DI float wave_sum63(float v) {
  v = dpp_add<0x111>(v); v = dpp_add<0x112>(v); v = dpp_add<0x114>(v); v = dpp_add<0x118>(v);
  v = dpp_add<0x142>(v);
  return dpp_add<0x143>(v);
}
DI s16x4 trd(LAS const char* p) { return __builtin_amdgcn_ds_read_tr16_b64_v4i16((LAS s16x4*)p); }
template <int STEP> DI bf16x8 rdW(LAS const char* p) {
  const s16x4 a = trd(p), b = trd(p + STEP);
  return __builtin_shufflevector(a, b, 0, 1, 2, 3, 4, 5, 6, 7);
}
DI unsigned pk(float lo, float hi) {
  const f32x2 v = {lo, hi};
  return __builtin_bit_cast(unsigned, __builtin_convertvector(v, bf2));
}
DI float rbf(float v) { return __builtin_bit_cast(float, pk(0.f, v) & 0xffff0000u); }
#define COLSUM(w, cs) cs = __builtin_amdgcn_mfma_f32_16x16x32_bf16(a.sel, (w), cs, 0, 0, 0)
DI bf16x8 mk_sel(int l) {
  const unsigned sl = ((l ^ (l >> 4)) & 1) ? 0u : 0x3f803f80u;
  return __builtin_bit_cast(bf16x8, u32x4{sl, sl, sl, sl});
}
DI float swap32_max(float m) {
  auto r = __builtin_amdgcn_permlane32_swap(__float_as_uint(m), __float_as_uint(m), false, false);
  return __builtin_fmaxf(__uint_as_float(r[0]), __uint_as_float(r[1]));
}
DI float swap32_add(float m) {
  auto r = __builtin_amdgcn_permlane32_swap(__float_as_uint(m), __float_as_uint(m), false, false);
  return __uint_as_float(r[0]) + __uint_as_float(r[1]);
}
DI f32x16 splat(float v) {
  f32x16 r;
#pragma unroll
  for (int i = 0; i < 16; ++i) r[i] = v;
  return r;
}
DI void ln_fix(f32x16& acc, float c, unsigned nm) {
  const unsigned u0 = __float_as_uint(c);
  const float r1 = c - __uint_as_float(u0 & 0xffff0000u);
  const unsigned u1 = __float_as_uint(r1);
  const float r2 = r1 - __uint_as_float(u1 & 0xffff0000u);
  const u32x2 a = {(u0 >> 16) | (u1 & 0xffff0000u), __float_as_uint(r2) >> 16};
  acc = __builtin_amdgcn_mfma_f32_32x32x8bf16_1k(__builtin_bit_cast(s16x4, a), __builtin_bit_cast(s16x4, u32x2{nm, nm}), acc, 0, 0, 0);
}
DI float geglu(float a, float b, float k1, float k2, float ir2) {
  const float e = __builtin_amdgcn_exp2f(a * __builtin_fmaf(a * a, k2, k1));
  return a * b * __builtin_amdgcn_rcpf(__builtin_fmaf(e, ir2, ir2));
}

DI unsigned w_voff(int lane, int pitch, int x = 0) {
  PIN(lane);
  const int r8 = lane >> 3, p8 = lane & 7;
  return r8 * pitch + (((p8 >> 1) ^ (r8 & 2) ^ x) * 32) + (p8 & 1) * 16;
}
struct Cx {
  int lane, w, g, c;
  int rt, ct, rot;
  int hd, G;
  unsigned voffC;
  unsigned cbd, cbq, cbo, hb, ob;
  rsrc_t ws, rwd, rwo;
  u32x2 tag;
  LAS char* L;
  unsigned junk, junk2;
};
DI void c_setup(Cx& v) {
  v.voffC = w_voff(v.lane, 1024);
  v.cbd = uni((448 * v.g + 32 * v.c) * 1024 + v.ct * 128);
  v.cbq = uni(448 * v.g * 1024 + v.ct * 128);
  v.cbo = uni((128 * v.g + 32 * v.c) * 1024 + v.ct * 128);
  v.hb = uni(WS_H + (2 * v.rt * 112 + 28 * v.g) * 1024);
  v.ob = uni(WS_O + (2 * v.rt * 32 + 8 * v.g) * 1024);
  v.rot = uni((v.rt & 3) * 2);
}
DI int c_grp(const Cx& v, int gp) {
  if (gp >= 7) return (gp - 7) ^ (v.rot & 1);
  const int r = gp + v.rot;
  return r >= 7 ? r - 7 : r;
}
DI unsigned c_voff(const Cx& v, int gp, int i) { return v.voffC + (c_grp(v, gp) << 16) + i * 8192; }
DI unsigned c_dst(const Cx& v, int region, int i) { return region * REG + v.g * 8192 + v.c * 4096 + i * 1024; }
DI void c_piece(const Cx& v, int gp, int region, int i) {
  glds(gp < 7 ? v.rwd : v.rwo, c_voff(v, gp, i), gp < 7 ? v.cbd : v.cbo, c_dst(v, region, i));
}

DI u32x4 w_park(const Cx& v, int gp, int i) {
  u32x4 r;
  asm volatile("buffer_load_dwordx4 %0, %1, %2, %3 offen" : "=v"(r) : "v"(c_voff(v, gp, i)), "s"(v.rwd), "s"(v.cbq) : "memory");
  return r;
}
DI unsigned hs_word(const Cx& v, int i) { return KV + 128 + (i < 2 ? v.g * 8 + i * 4 : 32 + (i - 2) * 4); }
DI void hs_set(const Cx& v, int i, int val) { *(volatile LAS int*)(v.L + hs_word(v, i)) = val; }
DI void hs_wait(const Cx& v, int i, int need) {
  unsigned cnt;
  int x;
  asm volatile(
      "s_mov_b32 %1, %4\n"
      "1:\n\t"
      "ds_read_b32 %0, %2\n\t"
      "s_waitcnt lgkmcnt(0)\n\t"
      "v_cmp_gt_i32 vcc, %3, %0\n\t"
      "s_cbranch_vccz 2f\n\t"
      "s_sleep 2\n\t"
      "s_sub_u32 %1, %1, 1\n\t"
      "s_cbranch_scc0 1b\n"
      "2:"
      : "=&v"(x), "=&s"(cnt)
      : "v"(hs_word(v, i)), "n"(need), "n"(SPIN_CAP * 8)
      : "vcc", "scc", "memory");
}

struct AtPre { u32x4 q, k0, k1, v0, v1; };
DI int at_kb(const Cx& v) {
  const int kb = 8 * v.G - 8 + 2 * v.w;
  return kb < 0 ? 0 : kb;
}
DI u32x4 at_load(const Cx& v, int i) {
  const int blk = i == 0 ? 8 * v.G + v.w : at_kb(v) + ((i - 1) & 1);
  return ld128h(v.ws, v.lane * 16, WS_QKV + ((i + 1) >> 1) * SEC_BYTES + (v.hd * 64 + blk) * 1024);
}
DI unsigned at_flag_off(const Cx& v) {
  const int i = v.lane & 7, isq = i >> 2;
  const int blk = at_kb(v) + (i & 1) + isq * (8 * v.G + v.w - at_kb(v) - (i & 1));
  const int sec = (1 - isq) * (1 + ((i >> 1) & 1));
  return WS_FQ + ((blk >> 2) * 24 + sec * 8 + (v.hd >> 2)) * 64 + (((v.hd >> 1) & 1) * 4 + (blk & 3)) * 8;
}
DI void at_stage(const Cx& v, const AtPre& a) {
  LAS char* kd = v.L + KV + 2 * v.w * 1024 + v.lane * 16;
  LAS char* vd = v.L + KV + 16384 + 2 * v.w * 1024 + (v.lane & 31) * 32 + (v.lane >> 5) * 16;
  const unsigned one = (v.lane & 8) ? 0u : 0x3f803f80u;
  *(LAS u32x4*)kd = v.w == 0 ? u32x4{one, one, one, one} : a.k0;
  *(LAS u32x4*)(kd + 1024) = a.k1;
  *(LAS u32x4*)vd = a.v0;
  *(LAS u32x4*)(vd + 1024) = a.v1;
}
DI void max16(const f32x16& s, float& m, float& m2) {
#pragma unroll
  for (int r = 0; r < 16; r += 4) {
    m = __builtin_fmaxf(__builtin_fmaxf(m, s[r]), s[r + 1]);
    m2 = __builtin_fmaxf(__builtin_fmaxf(m2, s[r + 2]), s[r + 3]);
  }
}
DI void exp_pack(const f32x16& s, u32x4& p0, u32x4& p1) {
#pragma unroll
  for (int i = 0; i < 4; ++i) {
    p0[i] = pk(__builtin_amdgcn_exp2f(s[2 * i]), __builtin_amdgcn_exp2f(s[2 * i + 1]));
    p1[i] = pk(__builtin_amdgcn_exp2f(s[8 + 2 * i]), __builtin_amdgcn_exp2f(s[9 + 2 * i]));
  }
}
#define PIN4(a, o) asm volatile("" : "+v"(a[o]), "+v"(a[o + 1]), "+v"(a[o + 2]), "+v"(a[o + 3]))
#define BF(x) __builtin_bit_cast(bf16x8, x)
DI void pin8(u32x4 (&a)[8]) { PIN4(a, 0); PIN4(a, 4); }
DI void undef8(u32x4 (&a)[8]) {
  asm volatile("" : "=v"(a[0]), "=v"(a[1]), "=v"(a[2]), "=v"(a[3]), "=v"(a[4]), "=v"(a[5]), "=v"(a[6]), "=v"(a[7]));
}
DI void o_flag(const Cx& v, int qb) { put_flag(v.ws, v.lane, WS_FO + (v.G * 32 + v.hd) * 64 + qb * 8, v.tag); }
struct AtLane {
  LAS const char *kp, *vp;
  int vstr;
  int td;
};
#define KFRAG(kb) (*(LAS const bf16x8*)(a.kp + (kb) * 1024))
#define VFRAG(kb, s) rdW<256>(a.vp + (kb) * a.vstr + (s) * 512)
DI AtLane at_lane(const Cx& v, int qb) {
  const int lane = v.lane, h = lane >> 5, qq = (lane & 15) >> 2, p = lane & 3;
  AtLane a;
  a.kp = v.L + KV + qb * 1024 + lane * 16;
  int vreal = KV + 16384 + qb * 1024 + (4 * h + qq) * 32 + (p & 1) * 16 + (p >> 1) * 8, vone = KV + (lane & 15) * 8;
  PIN(vreal); PIN(vone);
  a.vp = v.L + ((lane & 16) ? vone : vreal);
  a.vstr = (lane & 16) ? 0 : 1024;
  a.td = (lane & 31) - 4 * h;
  return a;
}
DI f32x16 at_merge(const AtLane& a, const f32x16& sd, const f32x16& sf) {
  f32x16 r;
#pragma unroll
  for (int i = 0; i < 16; ++i) r[i] = (i & 3) + 8 * (i >> 2) <= a.td ? sd[i] : sf[i];
  return r;
}
DI u32x4 at_mask(const AtLane& a, int half) {
  u32x4 k;
#pragma unroll
  for (int i = 0; i < 4; ++i) {
    const int r0 = 2 * (i & 1) + 8 * (i >> 1) + 16 * half;
    k[i] = (r0 <= a.td ? 0xffffu : 0u) | (r0 + 1 <= a.td ? 0xffff0000u : 0u);
  }
  return k;
}
DI void o_store(const Cx& v, int qb, const f32x16& o) {
  const float il = __builtin_amdgcn_rcpf(o[8]);
  u32x4 ov;
#pragma unroll
  for (int i = 0; i < 4; ++i) ov[i] = pk(o[2 * i] * il, o[2 * i + 1] * il);
  st128h(v.ws, v.lane * 16, WS_O + ((8 * v.G + qb) * 32 + v.hd) * 1024, ov);
}
template <int GP, int N> DI u32x4 a_load(const Cx& v) {
  return ld128h<(N >> 1) * 1024>(v.ws, v.lane * 16 + (c_grp(v, GP) << 12) + (N & 1) * (GP < 7 ? 112 : 32) * 1024, GP < 7 ? v.hb : v.ob);
}
DI void w_early(const Cx& v, int q, u32x4 (&af)[4][8]) {
  if (v.c) {
    af[q >> 2][(2 * q) & 7] = w_park(v, 5 + (q >> 2), (2 * q) & 7);
    af[q >> 2][(2 * q + 1) & 7] = w_park(v, 5 + (q >> 2), (2 * q + 1) & 7);
  } else c_piece(v, 3, 4, q);
}
template <bool FULL> DI void at_body(const Cx& v, const AtPre& ap, u32x4 (&af)[4][8]) {
  const AtLane a = at_lane(v, v.w);
  const bf16x8 q = BF(ap.q);
  const float NEG = -1e30f;
  const f32x16 zero = {};
  const int th = (v.lane >> 3) & 1;
  const unsigned hpoll = WS_FH + ((v.rt >> 1) * 16 + 4 * v.g + (v.lane & 3)) * 64 + (((v.lane >> 2) & 1) * 4 + ((2 * v.rt + th) & 3)) * 8;
  float m = NEG, m2 = NEG;
  f32x16 o = zero, cm, sc;
  u32x4 p0, p1;
  u32x2 fl;
  if (FULL) {
    bf16x8 kf[9];
#pragma unroll
    for (int kb = 0; kb < 9; ++kb) kf[(kb + 8) % 9] = KFRAG((kb + 8) % 9);
    if (v.w == 0) kf[0] = BF(ap.k0);
    f32x16 sa = MFMA(kf[8], q, zero), sb = MFMA(kf[0], q, zero);
    sc = MFMA(kf[1], q, zero);
    PIN(sa); PIN(sb); PIN(sc);
    SBAR();
    w_early(v, 0, af);
    max16(at_merge(a, sa, sb), m, m2);
    static_for<1, 8>([&](auto K_) __attribute__((always_inline)) {
      constexpr int kb = decltype(K_)::v;
      f32x16 sn;
      if constexpr (kb < 7) { sn = MFMA(kf[kb + 1], q, zero); PIN(sn); }
      SBAR();
      if (kb == HPOLL) fl = ld64h(v.ws, hpoll);
      w_early(v, kb, af);
      max16(sc, m, m2);
      PIN(m); PIN(m2);
      SBAR();
      if constexpr (kb < 7) sc = sn;
    });
    m = swap32_max(__builtin_fmaxf(m, m2));
    STAMP2(8);
    cm = splat(-m);
    sc = MFMA(kf[1], q, cm);
    bf16x8 kz = kf[0], kn = kf[2], v0 = VFRAG(1, 0), v1 = VFRAG(1, 1);
    static_for<1, 8>([&](auto K_) __attribute__((always_inline)) {
      constexpr int kb = decltype(K_)::v;
      const bf16x8 n0 = VFRAG(kb + 1, 0), n1 = VFRAG(kb + 1, 1);
      f32x16 sn;
      if constexpr (kb < 7) { sn = MFMA(kn, q, cm); PIN(sn); kn = KFRAG(kb + 2); }
      else { sa = MFMA(kn, q, cm); sb = MFMA(kz, q, cm); PIN(sa); PIN(sb); }
      SBAR();
      exp_pack(sc, p0, p1);
      o = MFMA(v0, BF(p0), o);
      o = MFMA(v1, BF(p1), o);
      PIN(o);
      if (kb == 1) {
        STAMP2(10);
        asm volatile("s_waitcnt vmcnt(%1)" : "+v"(fl) : "n"(8 - HPOLL) : "memory");
        check_flags(v.ws, hpoll, v.tag, v.c ? v.tag : fl);
        STAMP2(11);
      }
      if (!v.c) static_for<(kb - 1) * 3, (kb < 7 ? kb * 3 : 20)>([&](auto N_) __attribute__((always_inline)) {
          af[decltype(N_)::v >> 3][decltype(N_)::v & 7] = a_load<(decltype(N_)::v >> 3), (decltype(N_)::v & 7)>(v);
        });
      SBAR();
      if constexpr (kb < 7) sc = sn;
      v0 = n0;
      v1 = n1;
    });
    sc = at_merge(a, sa, sb);
    exp_pack(sc, p0, p1);
    const u32x4 k0 = at_mask(a, 0), k1 = at_mask(a, 1);
    o = MFMA(v0, BF(p0 & k0), o);
    o = MFMA(v1, BF(p1 & k1), o);
    o = MFMA(VFRAG(0, 0), BF(p0 & ~k0), o);
    o = MFMA(VFRAG(0, 1), BF(p1 & ~k1), o);
  } else {
#pragma unroll
    for (int i = 0; i < 8; ++i) w_early(v, i, af);
    max16(at_merge(a, MFMA(KFRAG(8), q, zero), splat(NEG)), m, m2);
    for (int kb = 8 - v.w; kb < 8; ++kb) max16(MFMA(KFRAG(kb), q, zero), m, m2);
    cm = splat(-swap32_max(__builtin_fmaxf(m, m2)));
    for (int kb = 8 - v.w; kb < 9; ++kb) {
      sc = MFMA(KFRAG(kb), q, cm);
      if (kb == 8) sc = at_merge(a, sc, splat(NEG));
      exp_pack(sc, p0, p1);
      o = MFMA(VFRAG(kb, 0), BF(p0), o);
      o = MFMA(VFRAG(kb, 1), BF(p1), o);
    }
    PIN(o);
    check_flags(v.ws, hpoll, v.tag, v.c ? v.tag : u32x2{~v.tag.x, 0u});
    if (!v.c) static_for<0, 20>([&](auto N_) __attribute__((always_inline)) {
      af[decltype(N_)::v >> 3][decltype(N_)::v & 7] = a_load<(decltype(N_)::v >> 3), (decltype(N_)::v & 7)>(v);
    });
  }
  o_store(v, v.w, o);
}

DI void c_scatter2(const Cx& v, LAS char* X, const f32x16& a, int nb, int j0) {
  const int t = v.lane & 31;
  LAS char* row = X + v.g * 8192 + t * 256;
  const int sw = ((v.lane >> 5) ^ (t & 15)) * 16;
#pragma unroll
  for (int j = j0; j < j0 + 2; ++j) *(LAS f32x4*)(row + (sw ^ ((4 * nb + j) * 32))) = f32x4{a[4 * j], a[4 * j + 1], a[4 * j + 2], a[4 * j + 3]};
}
DI LAS f32x4* c_cell(const Cx& v, LAS char* X, int half) {
  const int t = 8 * v.g + (v.lane >> 3);
  return (LAS f32x4*)(X + t * 256 + (((2 * (v.lane & 7) + half) ^ (t & 15)) * 16));
}
DI f32x4 c_gather(const Cx& v, LAS char* X, int half) {
  LAS const f32x4* src = c_cell(v, X, half);
  return (src[0] + src[512]) + (src[1024] + src[1536]);
}
DI f32x4 silu4(f32x4 d) {
#pragma unroll
  for (int i = 0; i < 4; ++i) d[i] *= __builtin_amdgcn_rcpf(1.f + __builtin_amdgcn_exp2f(-1.4426950408889634f * d[i]));
  return d;
}
DI u32x2 fin4(u32x2 x, f32x4 s, f32x4 g) {
  return u32x2{pk(__builtin_fmaf(s[0], g[0], __uint_as_float(x.x << 16)), __builtin_fmaf(s[1], g[1], __uint_as_float(x.x & 0xffff0000u))),
               pk(__builtin_fmaf(s[2], g[2], __uint_as_float(x.y << 16)), __builtin_fmaf(s[3], g[3], __uint_as_float(x.y & 0xffff0000u)))};
}
DI LAS char* c_xd(const Cx& v, int half) { return v.L + (1 + half) * REG; }
DI LAS char* c_xs(const Cx& v, int half) { return v.L + (half ? 0 : 4 * REG); }
DI unsigned c_xoff(const Cx& v) { return (64 * v.rt + 8 * v.w + (v.lane >> 3)) * 1024 + v.ct * 128 + (v.lane & 7) * 16; }
DI u32x4 x_load(const Params& P, const Cx& v) {
  u32x4 r;
  asm volatile("buffer_load_dwordx4 %0, %1, %2, 0 offen" : "=v"(r) : "v"(c_xoff(v)), "s"(mk_rsrc(P.x, 2048 * 512 * 2)) : "memory");
  return r;
}
struct CFrag {
  LAS const char *b0, *b1;
  bf16x8 wf[4][2];
};
DI CFrag c_frag(const Cx& v) {
  const int lane = v.lane, h = lane >> 5, qq = (lane & 15) >> 2, p = lane & 3, g1 = (lane >> 4) & 1;
  CFrag c;
  c.b0 = v.L + v.g * 8192 + (4 * h + qq) * 128 + (g1 + (qq & 2)) * 32 + 8 * p;
  c.b1 = v.L + v.g * 8192 + (4 * h + qq) * 128 + (g1 + (2 ^ (qq & 2))) * 32 + 8 * p;
  return c;
}
template <int S> DI void c_rd(CFrag& c) {
  constexpr int RG[4] = {0, 1, 2, 4};
  c.wf[S & 3][0] = rdW<1024>(c.b0 + RG[(S >> 2) & 3] * REG + (S & 3) * 2048);
  c.wf[S & 3][1] = rdW<1024>(c.b1 + RG[(S >> 2) & 3] * REG + (S & 3) * 2048);
}
template <int GP> DI void c_close(const Cx& v, CFrag& c, const u32x4 (&af)[8], f32x16 (&ac)[2][2], LAS char* x0, LAS char* x1) {
  c_rd<4 * GP + 2>(c);
  c_rd<4 * GP + 3>(c);
  static_for<0, 5>([&](auto J_) __attribute__((always_inline)) {
    constexpr int j = decltype(J_)::v;
    static_for<0, 4>([&](auto K_) __attribute__((always_inline)) {
      constexpr int k = decltype(K_)::v;
      if constexpr (j < 4) {
        ac[j & 1][j >> 1] = MFMA(c.wf[k][j & 1], BF(af[2 * k + (j >> 1)]), ac[j & 1][j >> 1]);
        PIN(ac[j & 1][j >> 1]);
        SBAR();
      }
      if constexpr (j > 0 && (k & 1)) c_scatter2(v, j > 2 ? x1 : x0, ac[(j - 1) & 1][(j - 1) >> 1], (j - 1) & 1, k - 1);
      SBAR();
    });
  });
}
DI void gemm_role(const Params& P, const Cx& v, u32x4 (&af)[4][8], u32x4 (&wp)[8], u32x4& xv) {
  CFrag c = c_frag(v);
  f32x16 d[2][2] = {};
  c_rd<0>(c);
  c_rd<1>(c);
  static_for<4, 8>([&](auto N_) __attribute__((always_inline)) { af[2][decltype(N_)::v] = a_load<2, decltype(N_)::v>(v); });
  __builtin_amdgcn_s_setprio(0);
  wait_vm<17>();
  pin8(af[0]);
  static_for<0, 24>([&](auto S_) __attribute__((always_inline)) {
    constexpr int s = decltype(S_)::v, gp = s >> 2, k = s & 3;
    if (k == 0) STAMP2(40 + gp);
    if constexpr (k == 2) {
      constexpr int N[6] = {8, 13, 12, 12, 8, 1};
      wait_vm<N[gp]>();
      pin8(af[(gp + 1) & 3]);
      if (gp == 0) o_flag(v, v.w);
      if (gp >= 4) hs_wait(v, 1, gp + 1);
    }
    if (k == 0 && gp >= 2) hs_set(v, 0, gp);
    c_rd<s + 2>(c);
    static_for<0, 4>([&](auto J_) __attribute__((always_inline)) {
      constexpr int j = decltype(J_)::v, n = 4 * k + j;
      d[j >> 1][j & 1] = MFMA(c.wf[k][j >> 1], BF(af[gp & 3][2 * k + (j & 1)]), d[j >> 1][j & 1]);
      PIN(d[j >> 1][j & 1]);
      SBAR();
      if constexpr (gp < 4 && !(n & 1)) af[(gp + 3) & 3][n / 2] = a_load<gp + 3, n / 2>(v);
      if constexpr (gp == 1 && (n & 1)) *(LAS u32x4*)(v.L + c_dst(v, 0, n / 2) + v.lane * 16) = wp[n / 2];
      if (s == 20 && j == 0) xv = x_load(P, v);
      SBAR();
    });
  });
  STAMP2(46);
  c_close<6>(v, c, af[2], d, c_xd(v, 0), c_xd(v, 1));
  asm volatile("s_waitcnt lgkmcnt(0)" ::: "memory");
  hs_set(v, 2 + v.g, 1);
#pragma unroll
  for (int q = 0; q < 4; ++q) hs_wait(v, 2 + q, 1);
#pragma unroll
  for (int i = 0; i < 4; ++i) *c_cell(v, c_xd(v, i >> 1), i & 1) = silu4(c_gather(v, c_xd(v, i >> 1), i & 1));
  STAMP(14);
}
DI void side_role(const Params& P, const Cx& v, u32x4 (&af)[4][8], u32x4& xv) {
  wait_vm<0>();
  o_flag(v, v.w);
  pin8(af[0]);
  pin8(af[1]);
  const unsigned oown = WS_FO + ((v.rt >> 2) * 32 + 8 * v.g + (v.lane & 7)) * 64 + ((2 * v.rt + ((v.lane >> 3) & 1)) & 7) * 8;
  const unsigned opoll = blockIdx.x == 0 && v.lane >= 32 ? WS_FO + (v.g * 64 + v.lane - 32) * 64 : oown;
  auto hand = [&](int j) __attribute__((always_inline)) {
    hs_wait(v, 0, 2 + j);
#pragma unroll
    for (int i = 0; i < 8; ++i) *(LAS u32x4*)(v.L + (1 + j) * REG + v.g * 8192 + i * 1024 + v.lane * 16) = af[j][i];
    asm volatile("s_waitcnt lgkmcnt(0)" ::: "memory");
    hs_set(v, 1, 5 + j);
  };
  hand(0);
  hand(1);
  __builtin_amdgcn_s_setprio(0);
  u32x2 fl;
#pragma unroll
  for (int j = 0; j < 2; ++j) {
    if (j) fl = ld64h(v.ws, opoll);
    hs_wait(v, 0, 4 + j);
#pragma unroll
    for (int i = 0; i < 8; ++i) glds(v.rwo, c_voff(v, 7 + j, i), v.cbo - 32768, (j ? 0 : 4 * REG) + v.g * 8192 + i * 1024);
  }
  asm volatile("s_waitcnt vmcnt(8)" : "+v"(fl)::"memory");
  check_flags(v.ws, opoll, v.tag, fl);
  STAMP2(47);
  static_for<0, 16>([&](auto N_) __attribute__((always_inline)) {
    af[2 + decltype(N_)::v / 8][decltype(N_)::v & 7] = a_load<7 + decltype(N_)::v / 8, (decltype(N_)::v & 7)>(v);
  });
  xv = x_load(P, v);
  if (blockIdx.x == 0) fl = ld64h(v.ws, opoll + (v.lane >= 32 ? 32 * 64 : 0));
  CFrag c = c_frag(v);
  f32x16 sm[2][2] = {};
  c_rd<28>(c);
  c_rd<29>(c);
  wait_vm<9>();
  pin8(af[2]);
  STAMP2(48);
  static_for<28, 32>([&](auto S_) __attribute__((always_inline)) {
    constexpr int s = decltype(S_)::v, k = s & 3;
    c_rd<s + 2>(c);
#pragma unroll
    for (int j = 0; j < 4; ++j) {
      sm[j >> 1][j & 1] = MFMA(c.wf[k][j >> 1], BF(af[2][2 * k + (j & 1)]), sm[j >> 1][j & 1]);
      PIN(sm[j >> 1][j & 1]);
    }
    SBAR();
  });
  asm volatile("s_waitcnt vmcnt(0)" : "+v"(fl)::"memory");
  pin8(af[3]);
  c_close<8>(v, c, af[3], sm, c_xs(v, 0), c_xs(v, 1));
  if (blockIdx.x == 0) check_flags(v.ws, opoll + (v.lane >= 32 ? 32 * 64 : 0), v.tag, fl);
  STAMP(14);
}

struct AB {
  bf16x8 X[32];
  bf16x8 wf[3][2];
  bf16x8 sel;
  unsigned nm;
  float r, k1, k2, ir2;
  int gr;
  int rb, cg, strip0, dblA, dblB;
  int kx;
  unsigned voffX, voffQ, voffF;
  rsrc_t rx, rq, rf;
};
DI void x_piece(const Cx& v, const AB& a, int kc, int slot, int i) {
  glds(a.rx, a.voffX ^ (64 * i), (a.rb * 128 + 16 * v.w + 4 * i) * 1024 + (kc ^ a.kx) * 256, slot * SLOT + (4 * v.w + i) * 1024);
}
DI void s_piece(const Cx& v, const AB& a, int kq, int i) {
  glds(a.rq, a.voffQ, (128 * (kq ^ a.kx) + 16 * v.w + 8 * i) * 3072 + a.strip0 * 128, SBUF + (kq & 1) * SUB1 + (2 * v.w + i) * 1024);
}
DI void f_piece(const Cx& v, const AB& a, int dbl, int kq, int slot, int i) {
  glds(a.rf, a.voffF, (128 * (kq ^ a.kx) + 32 * v.g + 8 * i) * 3584 + dbl * 128, slot * SLOT + v.c * SUB1 + v.g * 4096 + i * 1024);
}
template <bool TB> DI void u_piece(const Cx& v, const AB& a, int kq, int i) {
  const int slot = 1 + 2 * (kq & 1);
  if (TB) glds(a.rq, a.voffQ, (128 * (kq ^ a.kx) + 32 * v.g + 8 * i) * 3072 + (a.strip0 + 1 + v.c) * 128,
               slot * SLOT + v.c * SUB1 + v.g * 4096 + i * 1024);
  else f_piece(v, a, a.dblA, kq, slot, i);
}
DI void cs_put(const Cx& v, const f32x4& c0, const f32x4& c1, const f32x4& c2) {
  const bool hi = v.lane & 16;
  *(LAS f32x4*)(v.L + AUX + v.w * 1024 + v.lane * 16) = f32x4{hi ? c0[1] : c0[0], hi ? c1[1] : c1[0], hi ? c2[1] : c2[0], 0.f};
}
DI f32x4 cs_get(const Cx& v) {
  LAS const f32x4* p = (LAS const f32x4*)(v.L + AUX + v.c * 4096 + v.lane * 16);
  return (p[0] + p[64]) + (p[128] + p[192]);
}
DI void q_store(const Cx& v, const AB& a, const f32x16& acc, int strip, int hh) {
  const float sc = strip < 8 ? a.r * QSCALE : a.r;
  u32x4 o;
#pragma unroll
  for (int i = 0; i < 4; ++i) o[i] = pk(acc[8 * hh + 2 * i] * sc, acc[8 * hh + 2 * i + 1] * sc);
  st128h(v.ws, v.lane * 16, WS_QKV + ((strip * 4 + 2 * v.c + hh) * 64 + 4 * a.rb + v.g) * 1024, o);
}
DI void q_flag(const Cx& v, const AB& a, int strip) { put_flag(v.ws, v.lane, WS_FQ + (a.rb * 24 + strip) * 64 + v.w * 8, v.tag); }
DI void put_h(const Cx& v, const AB& a, int dbl, int j, const u32x4& hv) {
  st128h(v.ws, v.lane * 16, WS_H + ((4 * a.rb + v.g) * 112 + 4 * dbl + 2 * v.c + j) * 1024, hv);
}
struct Bases { unsigned b[4]; };
template <bool F> DI Bases mk_bases(const Cx& v, const AB& a, int base) {
  int l = v.lane;
  PIN(l);
  const int qq = (l & 15) >> 2, g1 = (l >> 4) & 1;
  const int o = base + (F ? g1 * SUB1 : 0) + (8 * (l >> 5) + qq) * 128 + ((2 * v.c) ^ (qq & 2) ^ g1) * 32 + 8 * (l & 3);
  Bases r;
#pragma unroll
  for (int i = 0; i < 4; ++i) { r.b[i] = o + (i ^ a.gr) * 4096; PIN(r.b[i]); }
  return r;
}
DI void bump(Bases& B, int d) {
#pragma unroll
  for (int i = 0; i < 4; ++i) { B.b[i] += d; PIN(B.b[i]); }
}
#define WRD(B, off, i) rdW<512>((LAS const char*)(size_t)((B).b[(i) >> 1]) + (off) + ((i) & 1) * 2048)
#define WRX(B, off, i) rdW<512>((LAS const char*)(size_t)((B).b[(i) >> 1] ^ 32u) + (off) + ((i) & 1) * 2048)

DI u32x2 make_tag(u32x2 e) {
  const unsigned long long z = ((unsigned long long)__builtin_amdgcn_readfirstlane(e.y) << 32 | (unsigned)__builtin_amdgcn_readfirstlane(e.x));
  const unsigned long long t = (z ^ (z >> 31)) * 0x9E3779B97F4A7C15ull + 0xD1B54A32D192ED03ull;
  return u32x2{(unsigned)t, (unsigned)(t >> 32)};
}

DI void stats_sum(const u32x4& xr, float& s1, float& s2) {
  s1 = s2 = 0.f;
#pragma unroll
  for (int d = 0; d < 4; ++d) {
    const float lo = __uint_as_float(xr[d] << 16), hi = __uint_as_float(xr[d] & 0xffff0000u);
    s1 += lo + hi;
    s2 = __builtin_fmaf(lo, lo, __builtin_fmaf(hi, hi, s2));
  }
}
DI void stats_put(const Cx& v, const AB& a, float s1, float s2) {
  const float mean = s1 * (1.f / 512.f);
  const float var = __builtin_fmaxf(__builtin_fmaf(-mean, mean, s2 * (1.f / 512.f)), 0.f);
  const float sd = __builtin_amdgcn_sqrtf(rbf(rbf(var) + 1e-5f));
  put_flag(v.ws, v.lane ^ 63, WS_ST + (a.rb * 128 + a.cg * 8 + v.w) * 8, u32x2{pk(-mean, sd), v.tag.x});
}
DI void row_stats(const Cx& v, AB& a, unsigned g) {
  const float sd = __uint_as_float(g & 0xffff0000u);
  const float c1 = -2.f * 0.7978845608028654f * 1.4426950408889634f;
  a.r = __builtin_amdgcn_rcpf(sd);
  a.k1 = c1 * a.r;
  a.k2 = c1 * 0.044715f * a.r * a.r * a.r;
  a.ir2 = sd * sd;
  a.nm = v.lane < 32 ? (g & 0xffffu) | (g << 16) : 0u;
}
DI void rd_step(AB& a, const Bases& B, int s) {
  a.wf[(s + 2) % 3][0] = WRD(B, ((s >> 3) & 1) * SLOT, s & 7);
  a.wf[(s + 2) % 3][1] = WRX(B, ((s >> 3) & 1) * SLOT, s & 7);
}
template <bool TB, int Q>
DI void f_step(Cx& v, AB& a, const Bases& BS, Bases& BU, f32x16& ps, f32x16& a0, f32x16& a1, f32x4& cs, f32x4& c0, f32x4& c1,
               u32x2& st, u32x4& xr) {
  const int t = v.lane & 31;
  const unsigned u = (((v.lane >> 5) ^ (t & 15)) * 16) ^ (64 * a.gr);
  LAS const char* xb = v.L + (32 * v.g + t) * 256;
  bf16x8 ws[3];
  float s1, s2;
  f32x4 cp;
  auto rdx = [&](int q, int i) __attribute__((always_inline)) { a.X[8 * q + i] = *(LAS const bf16x8*)(xb + 2 * (q & 1) * SLOT + (u ^ (32 * i))); };
  auto rd = [&](int i) __attribute__((always_inline)) {
    a.wf[i % 3][0] = WRD(BU, 0, i);
    a.wf[i % 3][1] = TB ? WRD(BU, SUB1, i) : WRX(BU, 0, i);
    ws[i % 3] = WRD(BS, (Q & 1) * SUB1, i);
  };
  auto req = [&](int n) __attribute__((always_inline)) {
    if (Q < 3) { if (n < 2) s_piece(v, a, Q + 1, n); else u_piece<TB>(v, a, Q + 1, n - 2); }
    else if (n < 4) f_piece(v, a, a.dblB, 1, 1, n);
  };
  if (Q == 0) { rdx(0, 0); rdx(0, 1); }
  rd(0);
  rd(1);
  static_for<0, 8>([&](auto I_) __attribute__((always_inline)) {
    constexpr int i = decltype(I_)::v;
    if (i == 4) {
      if (Q == 0) wait_bar<8, 0>();
      else if (Q < 3) wait_bar<Q == 2 ? 8 : 6, 15>();
      else {
        wait_bar<4, 6>();
        cp = cs_get(v);
      }
    }
    const bf16x8 x = a.X[8 * Q + i];
    a0 = MFMA(a.wf[i % 3][0], x, a0);
    PIN(a0);
    SBAR();
    if (Q == 0 && i < 3) { rdx(0, 2 * i + 2); rdx(0, 2 * i + 3); }
    if (Q < 3 && i >= 4) { rdx(Q + 1, 2 * i - 8); rdx(Q + 1, 2 * i - 7); }
    if (i < 2) { COLSUM(a.wf[i % 3][0], c0); PIN(c0); }
    if (i < 3) req(2 * i);
    if (i >= 4) {
      if (Q < 2) x_piece(v, a, Q + 2, 2 * (Q & 1), i - 4);
      else f_piece(v, a, a.dblB, 2 * (Q & 1), 2 * (Q & 1), i - 4);
    }
    if (Q == 3 && i == 5) { ln_fix(a0, cp[0], a.nm); PIN(a0); }
    SBAR();
    a1 = MFMA(a.wf[i % 3][1], x, a1);
    PIN(a1);
    SBAR();
    if (i < 2) { COLSUM(a.wf[i % 3][1], c1); PIN(c1); }
    if (i < 3) req(2 * i + 1);
    if (Q == 3 && i == 6) { ln_fix(a1, cp[1], a.nm); PIN(a1); }
    if (Q == 0 && i == 3) {
      st = ld64h(v.ws, WS_EPOCH);
      asm volatile("buffer_load_dwordx4 %0, %1, %2, %3 offen"
                   : "=v"(xr) : "v"(v.lane * 16), "s"(a.rx), "s"(RFL((a.rb * 128 + a.cg * 8 + v.w) * 1024)) : "memory");
    }
    if (Q == 1) {
      if (i == 3) { PIN(st); PIN(xr); v.tag = make_tag(st); stats_sum(xr, s1, s2); PIN(s1); PIN(s2); }
      if (i == 4) { s1 = wave_sum63(s1); PIN(s1); }
      if (i == 5) { s2 = wave_sum63(s2); PIN(s2); }
      if (i == 6) stats_put(v, a, s1, s2);
    }
    if (Q == 2 && i == 3) st = ld64h(v.ws, WS_ST + (a.rb * 128 + 32 * v.g + (v.lane & 31)) * 8);
    if (Q == 3 && i == 2) {
      PIN(st);
      check_granule(v.ws, WS_ST + (a.rb * 128 + 32 * v.g + (v.lane & 31)) * 8, v.tag.x, st);
      row_stats(v, a, st.x);
      PIN(a.nm); PIN(a.r); PIN(a.k1); PIN(a.k2); PIN(a.ir2);
    }
    SBAR();
    const bf16x8 wsi = ws[i % 3];
    ps = MFMA(wsi, x, ps);
    PIN(ps);
    SBAR();
    if (i < 6) rd(i + 2);
    if (i < 2) { COLSUM(wsi, cs); PIN(cs); }
    if (Q == 3) {
      if (i == 2) cs_put(v, c0, c1, cs);
      if (i == 4) { ln_fix(ps, cp[2], a.nm); PIN(ps); }
      if (i == 6) BU = mk_bases<true>(v, a, 0);
      if (i >= 6) rd_step(a, BU, i - 6);
    } else if (i == 6) bump(BU, (Q & 1) ? -2 * SLOT : 2 * SLOT);
    if (Q == 1 && i == 7) {
      const int per = v.w == 1 ? 16 : 56;
      asm volatile("buffer_load_dword %0, %1, %2, 0 offen"
                   : "=v"(xr.x) : "v"(v.lane < per && v.w < 2 ? (blockIdx.x * per + v.lane) * 128 : 0x40000000u), "s"(v.w == 0 ? v.rwd : v.rwo)
                   : "memory");
    }
    SBAR();
  });
}
template <bool TB> DI void front(Cx& v, AB& a, Bases& BU, f32x16& ps, f32x16& a0, f32x16& a1) {
  const Bases BS = mk_bases<false>(v, a, SBUF);
  BU = mk_bases<!TB>(v, a, SLOT);
  f32x4 cs = {}, c0 = {}, c1 = {};
  u32x2 st;
  u32x4 xr;
  STAMP2(23);
  wait_bar<4, 0>();
  PIN(v.junk);
  STAMP(1);
  f_step<TB, 0>(v, a, BS, BU, ps, a0, a1, cs, c0, c1, st, xr);
  STAMP2(16); wait_bar<4, 0>(); STAMP2(17);
  f_step<TB, 1>(v, a, BS, BU, ps, a0, a1, cs, c0, c1, st, xr);
  STAMP2(18); wait_bar<6, 0>(); STAMP2(19);
  f_step<TB, 2>(v, a, BS, BU, ps, a0, a1, cs, c0, c1, st, xr);
  STAMP2(20); wait_bar<4, 0>(); STAMP2(21);
  PIN(xr);
  f_step<TB, 3>(v, a, BS, BU, ps, a0, a1, cs, c0, c1, st, xr);
  STAMP2(22);
  wait_bar<4, 8>();
}
template <bool TB> DI void body(Cx& v, AB& a, Bases& B, const f32x16& ps, f32x16& p0, f32x16& p1, AtPre& ap) {
  f32x16 a0 = {}, a1 = {};
  f32x4 c0 = {}, c1 = {};
  float hlo;
  f32x4 cp;
  u32x4 hv;
  u32x2 fl;
  unsigned fo;
  static_for<0, 32>([&](auto S_) __attribute__((always_inline)) {
    constexpr int s = decltype(S_)::v, kq = s >> 3, i = s & 7;
    if (s == 2) { int l = v.lane; PIN(l); a.sel = mk_sel(l); }
    const bf16x8 w0 = a.wf[(s + 2) % 3][0], w1 = a.wf[(s + 2) % 3][1];
    constexpr bool CS = (i >> 1) == 1;
    constexpr int e = TB ? -1 : (s == 30 ? 14 : ((s & 1) && s >= 3 ? (s == 31 ? 15 : (s - 3) >> 1) : -1));
    float ga, ex;
    a0 = MFMA(w0, a.X[s], a0);
    PIN(a0);
    SBAR();
    if (s == 14) bump(B, 2 * SLOT);
    if (s < 30) rd_step(a, B, s + 2);
    if (s == 7) c_setup(v);
    if (CS) { COLSUM(w0, c0); PIN(c0); }
    if ((i & 1) == 0) {
      if (kq == 0) f_piece(v, a, a.dblB, 3, 3, i >> 1);
      else c_piece(v, kq - 1, kq - 1, i >> 1);
    }
    if constexpr (e >= 0) {
      ga = (e < 8 ? p0 : p1)[e & 7];
      ex = __builtin_amdgcn_exp2f(ga * __builtin_fmaf(ga * ga, a.k2, a.k1));
      ga *= (e < 8 ? p0 : p1)[8 + (e & 7)];
      PIN(ga); PIN(ex);
    }
    SBAR();
    a1 = MFMA(w1, a.X[s], a1);
    PIN(a1);
    SBAR();
    if (CS) { COLSUM(w1, c1); PIN(c1); }
    if constexpr (e >= 0) {
      const float hx = ga * __builtin_amdgcn_rcpf(__builtin_fmaf(ex, a.ir2, a.ir2));
      if (e & 1) { hv[(e & 7) >> 1] = pk(hlo, hx); PIN(hv[(e & 7) >> 1]); }
      else { hlo = hx; PIN(hlo); }
      if (e == 15) put_h(v, a, a.dblA, 1, hv);
    }
    if (s < 2) q_store(v, a, ps, a.strip0, s);
    if (s == 8) { wait_vm<4>(); q_flag(v, a, a.strip0); }
    if (!TB && s == 18) put_h(v, a, a.dblA, 0, hv);
    if (TB) {
      if (s == 3 || s == 5) q_store(v, a, p0, a.strip0 + 1, (s - 3) >> 1);
      if (s == 7 || s == 9) q_store(v, a, p1, a.strip0 + 2, (s - 7) >> 1);
      if (s == 14) { wait_vm<3>(); q_flag(v, a, a.strip0 + 1); q_flag(v, a, a.strip0 + 2); }
    }
    if (s == 28) {
      cs_put(v, c0, c1, c1);
      SBAR();
      wait_bar<63, 0>();
      cp = cs_get(v);
    }
    if (s == 30) { ln_fix(a0, cp[0], a.nm); PIN(a0); }
    if (s == 31) { ln_fix(a1, cp[1], a.nm); PIN(a1); }
    if (s == 23) { fo = at_flag_off(v); fl = ld64h(v.ws, fo); }
    if (s == 27) {
      asm volatile("s_waitcnt vmcnt(2)" : "+v"(fl)::"memory");
      check_flags(v.ws, fo, v.tag, fl);
      ap.k0 = at_load(v, 1);
    }
    if (s == 28) ap.k1 = at_load(v, 2);
    if (s == 29) ap.v0 = at_load(v, 3);
    if (s == 30) ap.v1 = at_load(v, 4);
    if (s == 31) ap.q = at_load(v, 0);
    SBAR();
    if (i == 7 && kq < 3) {
      STAMP2(36 + kq);
      if (kq < 2) wait_bar<4, 8>();
      else wait_bar<63, 8>();
      STAMP2(54 + kq);
    }
  });
  p0 = a0;
  p1 = a1;
}
DI void ab_tail(const Cx& v, const AB& a, int dbl, const f32x16& p0, const f32x16& p1, AtPre& ap, u32x4 (&wp)[8]) {
  u32x4 hv;
  static_for<0, 8>([&](auto I_) __attribute__((always_inline)) {
    constexpr int i = decltype(I_)::v, j = 2 * (i & 3);
    const f32x16& p = i < 4 ? p0 : p1;
    if constexpr (i < 4) { if (!v.c) { wp[2 * i] = w_park(v, 4, 2 * i); wp[2 * i + 1] = w_park(v, 4, 2 * i + 1); } }
    hv[i & 3] = pk(geglu(p[j], p[8 + j], a.k1, a.k2, a.ir2), geglu(p[j + 1], p[9 + j], a.k1, a.k2, a.ir2));
    PIN(hv[i & 3]);
    if constexpr ((i & 3) == 3) put_h(v, a, dbl, i >> 2, hv);
    SBAR();
  });
  if (v.c) wait_vm<2>();
  asm volatile("s_waitcnt vmcnt(10)\n\ts_barrier" : "+v"(ap.q), "+v"(ap.k0), "+v"(ap.k1), "+v"(ap.v0), "+v"(ap.v1)::"memory");
  at_stage(v, ap);
  wait_vm<0>();
  put_flag(v.ws, v.lane, WS_FH + (a.rb * 16 + a.cg) * 64 + v.w * 8, v.tag);
  wait_bar<63, 0>();
  PIN4(wp, 0); PIN4(wp, 4);
}

template <bool TB> DI void ab_path(Cx& v, AB a, AtPre& ap, u32x4 (&wp)[8]) {
  int l = v.lane;
  PIN(l);
  a.voffX = (l >> 4) * 1024 + (((l & 15) ^ (l >> 4)) * 16);
  a.voffQ = w_voff(l, 3072);
  a.voffF = w_voff(l, 3584, v.c);
  a.sel = mk_sel(l);
#pragma unroll
  for (int i = 0; i < 4; ++i) u_piece<TB>(v, a, 0, i);
#pragma unroll
  for (int i = 0; i < 4; ++i) x_piece(v, a, 1, 2, i);
  f32x16 ps = {}, p0 = {}, p1 = {};
  Bases B;
  front<TB>(v, a, B, ps, p0, p1);
  STAMP(3);
  body<TB>(v, a, B, ps, p0, p1, ap);
  STAMP(4);
  ab_tail(v, a, a.dblB, p0, p1, ap, wp);
}

__global__ __launch_bounds__(512, 2) void fused_kernel(Params P) {
  extern __shared__ __attribute__((aligned(16))) char smem[];
  Cx v;
  v.L = (LAS char*)smem;
  v.lane = threadIdx.x & 63;
  v.w = __builtin_amdgcn_readfirstlane(threadIdx.x >> 6);
  v.g = v.w & 3;
  v.c = v.w >> 2;
  const int xc = blockIdx.x & 7, idx = blockIdx.x >> 3;
  v.ws = mk_rsrc(P.ws, WS_END);
  v.rwd = mk_rsrc(P.wd, 1792 * 512 * 2);
  v.rwo = mk_rsrc(P.wo, 512 * 512 * 2);
  v.rt = v.hd = xc * 4 + (idx & 3);
  v.ct = v.G = idx >> 2;
  AtPre ap;
  u32x4 wp[8];
  STAMPR(30); STAMP(0);
  {
    AB a;
    a.rb = (xc & 1) * 8 + (idx & 7);
    const int cq = xc >> 1, j = idx >> 3;
    const bool typeB = j == 3;
    a.cg = cq * 4 + j;
    a.gr = v.g ^ (2 * v.c);

    a.strip0 = 6 * cq + j;
    const int swp = typeB ? 0 : (a.rb >> 2) & SWP;
    a.dblA = 7 * cq + 2 * j + swp;
    a.dblB = typeB ? a.dblA : 7 * cq + 2 * j + 1 - swp;
    a.kx = a.rb & KXM;
    a.rx = mk_rsrc(P.x, 2048 * 512 * 2);
    a.rq = mk_rsrc(P.wqkv, 512 * 1536 * 2);
    a.rf = mk_rsrc(v.c ? P.wu : P.wg, 512 * 1792 * 2);
    a.voffX = (v.lane >> 4) * 1024 + (((v.lane & 15) ^ (v.lane >> 4)) * 16);
    a.voffQ = w_voff(v.lane, 3072);
    STAMP2(24);
#pragma unroll
    for (int i = 0; i < 4; ++i) x_piece(v, a, 0, 0, i);
#pragma unroll
    for (int i = 0; i < 2; ++i) s_piece(v, a, 0, i);
    {
      unsigned long long pc;
      unsigned len, junk;
      asm volatile("s_getpc_b64 %0\n.Lfk_begin:\n\ts_mov_b32 %1, .Lfk_end-.Lfk_begin" : "=s"(pc), "=s"(len));
      const unsigned off = (v.w * 64 + v.lane) * 128;
      asm volatile("buffer_load_dword %0, %1, %2, 0 offen" : "=v"(junk) : "v"(off), "s"(mk_rsrc((const void*)pc, len & ~3u)) : "memory");
      v.junk = junk;
    }
    if (typeB) ab_path<true>(v, a, ap, wp);
    else ab_path<false>(v, a, ap, wp);
  }
  STAMP(6);
  c_setup(v);
  v.tag = u32x2{uni(v.tag.x), uni(v.tag.y)};
  u32x4 af[4][8];
  __builtin_amdgcn_s_setprio(2);
  if (v.G) at_body<true>(v, ap, af);
  else at_body<false>(v, ap, af);
  STAMP(7);
  c_setup(v);
  u32x4 xv;
  if (v.c) {
    side_role(P, v, af, xv);
    undef8(af[0]); undef8(af[1]); undef8(af[2]); undef8(wp);
  }
  if (!uni(v.c)) gemm_role(P, v, af, wp, xv);
  asm volatile("s_waitcnt vmcnt(0) lgkmcnt(0)\n\ts_barrier" : "+v"(xv)::"memory");
  if (blockIdx.x == 0 && v.w == 0) put_flag(v.ws, v.lane, WS_EPOCH, v.tag);
  const f32x4 ga = *c_cell(v, c_xd(v, v.c), 0), gb = *c_cell(v, c_xd(v, v.c), 1);
  const u32x2 y0 = fin4(u32x2{xv.x, xv.y}, c_gather(v, c_xs(v, v.c), 0), ga), y1 = fin4(u32x2{xv.z, xv.w}, c_gather(v, c_xs(v, v.c), 1), gb);
  __builtin_amdgcn_raw_buffer_store_b128(u32x4{y0.x, y0.y, y1.x, y1.y}, mk_rsrc(P.out, 2048 * 512 * 2), c_xoff(v), 0, 0);
  asm volatile(".Lfk_end:");
  STAMP(15); STAMPR(31);
}
}

extern "C" void kernel_launch(void* const* d_in, const int* in_sizes, int n_in, void* d_out, int out_size, void* d_ws,
                              size_t ws_size, hipStream_t stream) {
  Params P;
  P.x = d_in[0]; P.wqkv = d_in[1]; P.wo = d_in[2];
  P.wg = d_in[4]; P.wu = d_in[5]; P.wd = d_in[6];
  P.out = d_out; P.ws = d_ws;
  (void)hipFuncSetAttribute((const void*)fused_kernel, hipFuncAttributeMaxDynamicSharedMemorySize, LDS_BYTES);
  fused_kernel<<<256, 512, LDS_BYTES, stream>>>(P);
}
```

```cpp
#include <hip/hip_runtime.h>
#include <hip/hip_bf16.h>
#include <stdint.h>

typedef short bf16x8 __attribute__((ext_vector_type(8)));
typedef short s16x4 __attribute__((ext_vector_type(4)));
typedef float f32x16 __attribute__((ext_vector_type(16)));
typedef float f32x4 __attribute__((ext_vector_type(4)));
typedef float f32x2 __attribute__((ext_vector_type(2)));
typedef unsigned u32x4 __attribute__((ext_vector_type(4)));
typedef unsigned u32x2 __attribute__((ext_vector_type(2)));
typedef __bf16 bf2 __attribute__((ext_vector_type(2)));
typedef __amdgpu_buffer_rsrc_t rsrc_t;
#define LAS __attribute__((address_space(3)))
#define DI __device__ __forceinline__
#define SBAR() __builtin_amdgcn_sched_barrier(0)
#define PIN(x) asm volatile("" : "+v"(x))
#define RFL(x) __builtin_amdgcn_readfirstlane(x)
#define MFMA(a, b, c) __builtin_amdgcn_mfma_f32_32x32x16_bf16((a), (b), (c), 0, 0, 0)

#define STAMP(i)
#define STAMPR(i)
#define JIT(p)
#define STAMP2(i)

namespace {
constexpr unsigned WS_EPOCH = 0;
constexpr unsigned WS_FQ = 1024;
constexpr unsigned WS_FH = WS_FQ + 16 * 24 * 64;
constexpr unsigned WS_FO = WS_FH + 16 * 16 * 64;
constexpr unsigned WS_ST = 65536;
constexpr unsigned WS_QKV = 131072;
constexpr unsigned SEC_BYTES = 32 * 64 * 1024;
constexpr unsigned WS_H = WS_QKV + 3 * SEC_BYTES;
constexpr unsigned WS_O = WS_H + 64 * 112 * 1024;
constexpr unsigned WS_END = WS_O + 64 * 32 * 1024;
static_assert(WS_FO + 256 * 64 <= WS_ST, "flags overlap");
constexpr int SLOT = 32768, SUB1 = 16384;
constexpr int SBUF = 4 * SLOT;
constexpr int AUX = SBUF;
constexpr int REG = 32768;
constexpr int LDS_BYTES = 163840;
constexpr int KV = 3 * REG;
constexpr float QSCALE = 0.25f * 1.4426950408889634f;
constexpr unsigned SPIN_CAP = 1u << 18;

struct Params {
  const void *x, *wqkv, *wo, *wg, *wu, *wd;
  void *out, *ws;
};

template <int I> struct IC { static constexpr int v = I; };
template <int I, int N, class F> DI void static_for(F&& f) {
  if constexpr (I < N) {
    f(IC<I>{});
    static_for<I + 1, N>(f);
  }
}
template <int VM> DI void wait_vm() { asm volatile("s_waitcnt vmcnt(%0)" ::"n"(VM) : "memory"); }
template <int VM, int LG> DI void wait_bar() { asm volatile("s_waitcnt vmcnt(%0) lgkmcnt(%1)\n\ts_barrier" ::"n"(VM), "n"(LG) : "memory"); }
DI unsigned uni(unsigned x) {
  x = __builtin_amdgcn_readfirstlane(x);
  asm volatile("" : "+s"(x));
  return x;
}
DI rsrc_t mk_rsrc(const void* p, unsigned bytes) { return __builtin_amdgcn_make_buffer_rsrc((void*)p, 0, bytes, 0x00020000); }
DI void glds(rsrc_t rs, unsigned voff, unsigned soff, unsigned lds) {
  asm volatile("s_mov_b32 m0, %2\n\ts_nop 0\n\tbuffer_load_dwordx4 %0, %1, %3 offen lds" ::"v"(voff), "s"(rs),
               "s"(RFL(lds)), "s"(RFL(soff)) : "memory");
}
template <int IMM = 0> DI u32x4 ld128h(rsrc_t rs, unsigned voff, unsigned soff) {
  u32x4 r;
  asm volatile("buffer_load_dwordx4 %0, %1, %2, %3 offen offset:%4 sc1" : "=v"(r) : "v"(voff), "s"(rs), "s"(RFL(soff)), "n"(IMM) : "memory");
  return r;
}
DI u32x2 ld64h(rsrc_t rs, unsigned voff) {
  u32x2 r;
  asm volatile("buffer_load_dwordx2 %0, %1, %2, 0 offen sc1" : "=v"(r) : "v"(voff), "s"(rs) : "memory");
  return r;
}
DI void st128h(rsrc_t rs, unsigned voff, unsigned soff, u32x4 d) {
  asm volatile("buffer_store_dwordx4 %0, %1, %2, %3 offen sc1\n\ts_nop 1" ::"v"(d), "v"(voff), "s"(rs), "s"(RFL(soff)) : "memory");
}
DI void put_flag(rsrc_t ws, int lane, unsigned soff, u32x2 tag) {
  asm volatile("buffer_store_dwordx2 %0, %1, %2, %3 offen sc1" ::"v"(tag), "v"(lane ? 0x80000000u : 0u), "s"(ws), "s"(RFL(soff)) : "memory");
}
DI void check_flags(rsrc_t ws, unsigned voff, u32x2 tag, u32x2 v) {
  unsigned cnt;
  asm volatile(
      "s_mov_b32 %1, %5\n"
      "1:\n\t"
      "v_cmp_ne_u64 vcc, %2, %0\n\t"
      "s_cbranch_vccz 2f\n\t"
      "s_sleep 1\n\t"
      "buffer_load_dwordx2 %0, %3, %4, 0 offen sc1\n\t"
      "s_waitcnt vmcnt(0)\n\t"
      "s_sub_u32 %1, %1, 1\n\t"
      "s_cbranch_scc0 1b\n"
      "2:"
      : "+v"(v), "=&s"(cnt)
      : "s"(tag), "v"(voff), "s"(ws), "n"(SPIN_CAP)
      : "vcc", "scc", "memory");
}
DI void check_granule(rsrc_t ws, unsigned voff, unsigned tag, u32x2& v) {
  unsigned cnt, lo = v.x, hi = v.y;
  asm volatile(
      "s_mov_b32 %2, %6\n"
      "1:\n\t"
      "v_cmp_ne_u32 vcc, %3, %1\n\t"
      "s_cbranch_vccz 2f\n\t"
      "s_sleep 1\n\t"
      "buffer_load_dword %1, %4, %5, 0 offen offset:4 sc1\n\t"
      "s_waitcnt vmcnt(0)\n\t"
      "buffer_load_dword %0, %4, %5, 0 offen sc1\n\t"
      "s_waitcnt vmcnt(0)\n\t"
      "s_sub_u32 %2, %2, 1\n\t"
      "s_cbranch_scc0 1b\n"
      "2:"
      : "+v"(lo), "+v"(hi), "=&s"(cnt)
      : "s"(tag), "v"(voff), "s"(ws), "n"(SPIN_CAP)
      : "vcc", "scc", "memory");
  v.x = lo;
}
template <int CTRL> DI float dpp_add(float v) {
  return v + __builtin_bit_cast(float, __builtin_amdgcn_update_dpp(0, __builtin_bit_cast(int, v), CTRL, 0xf, 0xf, true));
}
DI float wave_sum63(float v) {
  v = dpp_add<0x111>(v); v = dpp_add<0x112>(v); v = dpp_add<0x114>(v); v = dpp_add<0x118>(v);
  v = dpp_add<0x142>(v);
  return dpp_add<0x143>(v);
}
DI s16x4 trd(LAS const char* p) { return __builtin_amdgcn_ds_read_tr16_b64_v4i16((LAS s16x4*)p); }
template <int STEP> DI bf16x8 rdW(LAS const char* p) {
  const s16x4 a = trd(p), b = trd(p + STEP);
  return __builtin_shufflevector(a, b, 0, 1, 2, 3, 4, 5, 6, 7);
}
DI unsigned pk(float lo, float hi) {
  const f32x2 v = {lo, hi};
  return __builtin_bit_cast(unsigned, __builtin_convertvector(v, bf2));
}
DI float rbf(float v) { return __builtin_bit_cast(float, pk(0.f, v) & 0xffff0000u); }
#define COLSUM(w, cs) cs = __builtin_amdgcn_mfma_f32_16x16x32_bf16(a.sel, (w), cs, 0, 0, 0)
DI bf16x8 mk_sel(int l) {
  const unsigned sl = ((l ^ (l >> 4)) & 1) ? 0u : 0x3f803f80u;
  return __builtin_bit_cast(bf16x8, u32x4{sl, sl, sl, sl});
}
DI float swap32_max(float m) {
  auto r = __builtin_amdgcn_permlane32_swap(__float_as_uint(m), __float_as_uint(m), false, false);
  return __builtin_fmaxf(__uint_as_float(r[0]), __uint_as_float(r[1]));
}
DI f32x16 splat(float v) {
  f32x16 r;
#pragma unroll
  for (int i = 0; i < 16; ++i) r[i] = v;
  return r;
}
DI void ln_fix(f32x16& acc, float c, unsigned nm) {
  const unsigned u0 = __float_as_uint(c);
  const float r1 = c - __uint_as_float(u0 & 0xffff0000u);
  const unsigned u1 = __float_as_uint(r1);
  const float r2 = r1 - __uint_as_float(u1 & 0xffff0000u);
  const u32x2 a = {(u0 >> 16) | (u1 & 0xffff0000u), __float_as_uint(r2) >> 16};
  acc = __builtin_amdgcn_mfma_f32_32x32x8bf16_1k(__builtin_bit_cast(s16x4, a), __builtin_bit_cast(s16x4, u32x2{nm, nm}), acc, 0, 0, 0);
}
DI float geglu_exp(float t) {
  const float c1 = -2.f * 0.7978845608028654f * 1.4426950408889634f;
  return __builtin_amdgcn_exp2f(t * __builtin_fmaf(t * t, c1 * 0.044715f, c1));
}
DI float geglu_fin(float tb, float e, float sd) { return tb * __builtin_amdgcn_rcpf(__builtin_fmaf(e, sd, sd)); }
DI float geglu(float a, float b, float r, float sd) {
  const float t = a * r;
  return geglu_fin(t * b, geglu_exp(t), sd);
}

DI unsigned w_voff(int lane, int pitch, int x = 0) {
  PIN(lane);
  const int r8 = lane >> 3, p8 = lane & 7;
  return r8 * pitch + (((p8 >> 1) ^ (r8 & 2) ^ x) * 32) + (p8 & 1) * 16;
}
struct Cx {
  int lane, w, g, c;
  int rt, ct, rot;
  int hd, G;
  unsigned voffC;
  unsigned cbd, cbq, cbo, hb, ob;
  rsrc_t ws, rwd, rwo;
  u32x2 tag;
  LAS char* L;
};
DI void c_setup(Cx& v) {
  v.voffC = w_voff(v.lane, 1024);
  v.cbd = uni((448 * v.g + 32 * v.c) * 1024 + v.ct * 128);
  v.cbq = uni(448 * v.g * 1024 + v.ct * 128);
  v.cbo = uni((128 * v.g + 32 * v.c) * 1024 + v.ct * 128);
  v.hb = uni(WS_H + (2 * v.rt * 112 + 28 * v.g) * 1024);
  v.ob = uni(WS_O + (2 * v.rt * 32 + 8 * v.g) * 1024);
  v.rot = uni((v.rt & 3) * 2);
}
DI int c_grp(const Cx& v, int gp) {
  if (gp >= 7) return (gp - 7) ^ (v.rot & 1);
  const int r = gp + v.rot;
  return r >= 7 ? r - 7 : r;
}
DI unsigned c_voff(const Cx& v, int gp, int i) { return v.voffC + (c_grp(v, gp) << 16) + i * 8192; }
DI unsigned c_dst(const Cx& v, int region, int i) { return region * REG + v.g * 8192 + v.c * 4096 + i * 1024; }
DI void c_piece(const Cx& v, int gp, int region, int i) {
  glds(gp < 7 ? v.rwd : v.rwo, c_voff(v, gp, i), gp < 7 ? v.cbd : v.cbo, c_dst(v, region, i));
}

DI unsigned hs_word(const Cx& v, int i) { return KV + 128 + (i < 2 ? v.g * 8 + i * 4 : 32 + (i - 2) * 4); }
DI void hs_set(const Cx& v, int i, int val) { *(volatile LAS int*)(v.L + hs_word(v, i)) = val; }
DI void hs_wait(const Cx& v, int i, int need) {
  unsigned cnt;
  int x;
  asm volatile(
      "s_mov_b32 %1, %4\n"
      "1:\n\t"
      "ds_read_b32 %0, %2\n\t"
      "s_waitcnt lgkmcnt(0)\n\t"
      "v_cmp_gt_i32 vcc, %3, %0\n\t"
      "s_cbranch_vccz 2f\n\t"
      "s_sleep 2\n\t"
      "s_sub_u32 %1, %1, 1\n\t"
      "s_cbranch_scc0 1b\n"
      "2:"
      : "=&v"(x), "=&s"(cnt)
      : "v"(hs_word(v, i) + (i == 2 ? (v.lane & 3) * 4 : 0)), "n"(need), "n"(SPIN_CAP * 8)
      : "vcc", "scc", "memory");
}

struct AtPre { u32x4 q, k0, k1, v0, v1; };
DI int at_kb(const Cx& v) {
  const int kb = 8 * v.G - 8 + 2 * v.w;
  return kb < 0 ? 0 : kb;
}
DI u32x4 at_load(const Cx& v, int i) {
  const int blk = i == 0 ? 8 * v.G + v.w : at_kb(v) + ((i - 1) & 1);
  return ld128h(v.ws, v.lane * 16, WS_QKV + ((i + 1) >> 1) * SEC_BYTES + (v.hd * 64 + blk) * 1024);
}
DI unsigned at_flag_off(const Cx& v) {
  const int i = v.lane & 7, isq = i >> 2;
  const int blk = at_kb(v) + (i & 1) + isq * (8 * v.G + v.w - at_kb(v) - (i & 1));
  const int sec = (1 - isq) * (1 + ((i >> 1) & 1));
  return WS_FQ + ((blk >> 2) * 24 + sec * 8 + (v.hd >> 2)) * 64 + (((v.hd >> 1) & 1) * 4 + (blk & 3)) * 8;
}
DI void at_stage(const Cx& v, const AtPre& a) {
  LAS char* kd = v.L + KV + 2 * v.w * 1024 + v.lane * 16;
  LAS char* vd = v.L + KV + 16384 + 2 * v.w * 1024 + (v.lane & 31) * 32 + (v.lane >> 5) * 16;
  const unsigned one = (v.lane & 8) ? 0u : 0x3f803f80u;
  *(LAS u32x4*)kd = v.w == 0 ? u32x4{one, one, one, one} : a.k0;
  *(LAS u32x4*)(kd + 1024) = a.k1;
  *(LAS u32x4*)vd = a.v0;
  *(LAS u32x4*)(vd + 1024) = a.v1;
}
DI void max16(const f32x16& s, float& m, float& m2) {
#pragma unroll
  for (int r = 0; r < 16; r += 4) {
    m = __builtin_fmaxf(__builtin_fmaxf(m, s[r]), s[r + 1]);
    m2 = __builtin_fmaxf(__builtin_fmaxf(m2, s[r + 2]), s[r + 3]);
  }
}
DI void exp_pack(const f32x16& s, u32x4& p0, u32x4& p1) {
#pragma unroll
  for (int i = 0; i < 4; ++i) {
    p0[i] = pk(__builtin_amdgcn_exp2f(s[2 * i]), __builtin_amdgcn_exp2f(s[2 * i + 1]));
    p1[i] = pk(__builtin_amdgcn_exp2f(s[8 + 2 * i]), __builtin_amdgcn_exp2f(s[9 + 2 * i]));
  }
}
#define PIN4(a, o) asm volatile("" : "+v"(a[o]), "+v"(a[o + 1]), "+v"(a[o + 2]), "+v"(a[o + 3]))
#define BF(x) __builtin_bit_cast(bf16x8, x)
DI void pin8(u32x4 (&a)[8]) { PIN4(a, 0); PIN4(a, 4); }
DI void undef8(u32x4 (&a)[8]) {
  asm volatile("" : "=v"(a[0]), "=v"(a[1]), "=v"(a[2]), "=v"(a[3]), "=v"(a[4]), "=v"(a[5]), "=v"(a[6]), "=v"(a[7]));
}
DI void o_flag(const Cx& v, int qb) { put_flag(v.ws, v.lane, WS_FO + (v.G * 32 + v.hd) * 64 + qb * 8, v.tag); }
struct AtLane {
  LAS const char *kp, *vp;
  int vstr;
  int td;
};
#define KFRAG(kb) (*(LAS const bf16x8*)(a.kp + (kb) * 1024))
#define VFRAG(kb, s) rdW<256>(a.vp + (kb) * a.vstr + (s) * 512)
DI AtLane at_lane(const Cx& v, int qb) {
  const int lane = v.lane, h = lane >> 5, qq = (lane & 15) >> 2, p = lane & 3;
  AtLane a;
  a.kp = v.L + KV + qb * 1024 + lane * 16;
  int vreal = KV + 16384 + qb * 1024 + (4 * h + qq) * 32 + (p & 1) * 16 + (p >> 1) * 8, vone = KV + (lane & 15) * 8;
  PIN(vreal); PIN(vone);
  a.vp = v.L + ((lane & 16) ? vone : vreal);
  a.vstr = (lane & 16) ? 0 : 1024;
  a.td = (lane & 31) - 4 * h;
  return a;
}
DI f32x16 at_merge(const AtLane& a, const f32x16& sd, const f32x16& sf) {
  f32x16 r;
#pragma unroll
  for (int i = 0; i < 16; ++i) r[i] = (i & 3) + 8 * (i >> 2) <= a.td ? sd[i] : sf[i];
  return r;
}
DI u32x4 at_mask(const AtLane& a, int half) {
  u32x4 k;
#pragma unroll
  for (int i = 0; i < 4; ++i) {
    const int r0 = 2 * (i & 1) + 8 * (i >> 1) + 16 * half;
    k[i] = (r0 <= a.td ? 0xffffu : 0u) | (r0 + 1 <= a.td ? 0xffff0000u : 0u);
  }
  return k;
}
DI void o_store(const Cx& v, int qb, const f32x16& o) {
  const float il = __builtin_amdgcn_rcpf(o[8]);
  u32x4 ov;
#pragma unroll
  for (int i = 0; i < 4; ++i) ov[i] = pk(o[2 * i] * il, o[2 * i + 1] * il);
  st128h(v.ws, v.lane * 16, WS_O + ((8 * v.G + qb) * 32 + v.hd) * 1024, ov);
}
template <int GP, int N> DI u32x4 a_load(const Cx& v) {
  return ld128h<(N >> 1) * 1024>(v.ws, v.lane * 16 + (c_grp(v, GP) << 12) + (N & 1) * (GP < 7 ? 112 : 32) * 1024, GP < 7 ? v.hb : v.ob);
}
DI void w3_half(const Cx& v, int hf) {
  if (!v.c) {
#pragma unroll
    for (int i = 4 * hf; i < 4 * hf + 4; ++i) c_piece(v, 3, 4, i);
  }
}
template <bool FULL> DI void at_body(const Cx& v, const AtPre& ap, u32x4 (&af)[4][8]) {
  const AtLane a = at_lane(v, v.w);
  const bf16x8 q = BF(ap.q);
  const float NEG = -1e30f;
  const f32x16 zero = {};
  const int th = (v.lane >> 3) & 1;
  const unsigned hpoll = WS_FH + ((v.rt >> 1) * 16 + 4 * v.g + (v.lane & 3)) * 64 + (((v.lane >> 2) & 1) * 4 + ((2 * v.rt + th) & 3)) * 8;
  float m = NEG, m2 = NEG;
  f32x16 o = zero, cm, sc;
  u32x4 p0, p1;
  u32x2 fl;
  if (FULL) {
    bf16x8 kf[9];
#pragma unroll
    for (int kb = 0; kb < 9; ++kb) kf[(kb + 8) % 9] = KFRAG((kb + 8) % 9);
    if (v.w == 0) kf[0] = BF(ap.k0);
    f32x16 sa = MFMA(kf[8], q, zero), sb = MFMA(kf[0], q, zero);
    sc = MFMA(kf[1], q, zero);
    PIN(sa); PIN(sb); PIN(sc);
    SBAR();
    w3_half(v, 0);
    max16(at_merge(a, sa, sb), m, m2);
    static_for<1, 8>([&](auto K_) __attribute__((always_inline)) {
      constexpr int kb = decltype(K_)::v;
      f32x16 sn;
      if constexpr (kb < 7) { sn = MFMA(kf[kb + 1], q, zero); PIN(sn); }
      SBAR();
      if (kb == 2) w3_half(v, 1);
      if (kb == 6) fl = ld64h(v.ws, hpoll);
      max16(sc, m, m2);
      PIN(m); PIN(m2);
      SBAR();
      if constexpr (kb < 7) sc = sn;
    });
    m = swap32_max(__builtin_fmaxf(m, m2));
    STAMP2(8);
    cm = splat(-m);
    sc = MFMA(kf[1], q, cm);
    bf16x8 kz = kf[0], kn = kf[2], v0 = VFRAG(1, 0), v1 = VFRAG(1, 1);
    static_for<1, 8>([&](auto K_) __attribute__((always_inline)) {
      constexpr int kb = decltype(K_)::v;
      const bf16x8 n0 = VFRAG(kb + 1, 0), n1 = VFRAG(kb + 1, 1);
      f32x16 sn;
      if constexpr (kb < 7) { sn = MFMA(kn, q, cm); PIN(sn); kn = KFRAG(kb + 2); }
      else { sa = MFMA(kn, q, cm); sb = MFMA(kz, q, cm); PIN(sa); PIN(sb); }
      SBAR();
      exp_pack(sc, p0, p1);
      o = MFMA(v0, BF(p0), o);
      o = MFMA(v1, BF(p1), o);
      PIN(o);
      if (kb == 1) {
        STAMP2(10);
        asm volatile("s_waitcnt vmcnt(0)" : "+v"(fl)::"memory");
        check_flags(v.ws, hpoll, v.tag, v.c ? v.tag : fl);
        STAMP2(11);
      }
      if (!v.c) static_for<(kb - 1) * 3, (kb < 7 ? kb * 3 : 20)>([&](auto N_) __attribute__((always_inline)) {
          af[decltype(N_)::v >> 3][decltype(N_)::v & 7] = a_load<(decltype(N_)::v >> 3), (decltype(N_)::v & 7)>(v);
        });
      SBAR();
      if constexpr (kb < 7) sc = sn;
      v0 = n0;
      v1 = n1;
    });
    sc = at_merge(a, sa, sb);
    exp_pack(sc, p0, p1);
    const u32x4 k0 = at_mask(a, 0), k1 = at_mask(a, 1);
    o = MFMA(v0, BF(p0 & k0), o);
    o = MFMA(v1, BF(p1 & k1), o);
    o = MFMA(VFRAG(0, 0), BF(p0 & ~k0), o);
    o = MFMA(VFRAG(0, 1), BF(p1 & ~k1), o);
  } else {
    w3_half(v, 0);
    w3_half(v, 1);
    max16(at_merge(a, MFMA(KFRAG(8), q, zero), splat(NEG)), m, m2);
    for (int kb = 8 - v.w; kb < 8; ++kb) max16(MFMA(KFRAG(kb), q, zero), m, m2);
    cm = splat(-swap32_max(__builtin_fmaxf(m, m2)));
    for (int kb = 8 - v.w; kb < 9; ++kb) {
      sc = MFMA(KFRAG(kb), q, cm);
      if (kb == 8) sc = at_merge(a, sc, splat(NEG));
      exp_pack(sc, p0, p1);
      o = MFMA(VFRAG(kb, 0), BF(p0), o);
      o = MFMA(VFRAG(kb, 1), BF(p1), o);
    }
    PIN(o);
    check_flags(v.ws, hpoll, v.tag, v.c ? v.tag : u32x2{~v.tag.x, 0u});
    if (!v.c) static_for<0, 20>([&](auto N_) __attribute__((always_inline)) {
      af[decltype(N_)::v >> 3][decltype(N_)::v & 7] = a_load<(decltype(N_)::v >> 3), (decltype(N_)::v & 7)>(v);
    });
  }
  o_store(v, v.w, o);
}

DI void c_scatter2(const Cx& v, LAS char* X, const f32x16& a, int nb, int j0) {
  const int t = v.lane & 31;
  LAS char* row = X + v.g * 8192 + t * 256;
  const int sw = ((v.lane >> 5) ^ (t & 15)) * 16;
#pragma unroll
  for (int j = j0; j < j0 + 2; ++j) *(LAS f32x4*)(row + (sw ^ ((4 * nb + j) * 32))) = f32x4{a[4 * j], a[4 * j + 1], a[4 * j + 2], a[4 * j + 3]};
}
DI LAS f32x4* c_cell(const Cx& v, LAS char* X, int half) {
  const int t = 8 * v.g + (v.lane >> 3);
  return (LAS f32x4*)(X + t * 256 + (((2 * (v.lane & 7) + half) ^ (t & 15)) * 16));
}
DI f32x4 c_gather(const Cx& v, LAS char* X, int half) {
  LAS const f32x4* src = c_cell(v, X, half);
  return (src[0] + src[512]) + (src[1024] + src[1536]);
}
DI f32x4 silu4(f32x4 d) {
#pragma unroll
  for (int i = 0; i < 4; ++i) d[i] *= __builtin_amdgcn_rcpf(1.f + __builtin_amdgcn_exp2f(-1.4426950408889634f * d[i]));
  return d;
}
DI u32x2 fin4(u32x2 x, f32x4 s, f32x4 g) {
  return u32x2{pk(__builtin_fmaf(s[0], g[0], __uint_as_float(x.x << 16)), __builtin_fmaf(s[1], g[1], __uint_as_float(x.x & 0xffff0000u))),
               pk(__builtin_fmaf(s[2], g[2], __uint_as_float(x.y << 16)), __builtin_fmaf(s[3], g[3], __uint_as_float(x.y & 0xffff0000u)))};
}
DI LAS char* c_xd(const Cx& v, int half) { return v.L + (1 + half) * REG; }
DI LAS char* c_xs(const Cx& v, int half) { return v.L + (half ? 0 : 4 * REG); }
DI unsigned c_xoff(const Cx& v) { return (64 * v.rt + 8 * v.w + (v.lane >> 3)) * 1024 + v.ct * 128 + (v.lane & 7) * 16; }
DI u32x4 x_load(const Params& P, const Cx& v) {
  u32x4 r;
  asm volatile("buffer_load_dwordx4 %0, %1, %2, 0 offen" : "=v"(r) : "v"(c_xoff(v)), "s"(mk_rsrc(P.x, 2048 * 512 * 2)) : "memory");
  return r;
}
struct CFrag {
  LAS const char *b0, *b1;
  bf16x8 wf[4][2];
};
DI CFrag c_frag(const Cx& v) {
  const int lane = v.lane, h = lane >> 5, qq = (lane & 15) >> 2, p = lane & 3, g1 = (lane >> 4) & 1;
  CFrag c;
  c.b0 = v.L + v.g * 8192 + (4 * h + qq) * 128 + (g1 + (qq & 2)) * 32 + 8 * p;
  c.b1 = v.L + v.g * 8192 + (4 * h + qq) * 128 + (g1 + (2 ^ (qq & 2))) * 32 + 8 * p;
  return c;
}
template <int S> DI void c_rd(CFrag& c) {
  constexpr int RG[4] = {0, 1, 2, 4};
  c.wf[S & 3][0] = rdW<1024>(c.b0 + RG[(S >> 2) & 3] * REG + (S & 3) * 2048);
  c.wf[S & 3][1] = rdW<1024>(c.b1 + RG[(S >> 2) & 3] * REG + (S & 3) * 2048);
}
template <int GP> DI void c_close(const Cx& v, CFrag& c, const u32x4 (&af)[8], f32x16 (&ac)[2][2], LAS char* x0, LAS char* x1) {
  c_rd<4 * GP + 2>(c);
  c_rd<4 * GP + 3>(c);
  static_for<0, 5>([&](auto J_) __attribute__((always_inline)) {
    constexpr int j = decltype(J_)::v;
    static_for<0, 4>([&](auto K_) __attribute__((always_inline)) {
      constexpr int k = decltype(K_)::v;
      if constexpr (j < 4) {
        ac[j & 1][j >> 1] = MFMA(c.wf[k][j & 1], BF(af[2 * k + (j >> 1)]), ac[j & 1][j >> 1]);
        PIN(ac[j & 1][j >> 1]);
        SBAR();
      }
      if constexpr (j > 0 && (k & 1)) c_scatter2(v, j > 2 ? x1 : x0, ac[(j - 1) & 1][(j - 1) >> 1], (j - 1) & 1, k - 1);
      SBAR();
    });
  });
}
DI void gemm_role(const Params& P, const Cx& v, u32x4 (&af)[4][8], u32x4& xv) {
  CFrag c = c_frag(v);
  f32x16 d[2][2] = {};
  c_rd<0>(c);
  c_rd<1>(c);
  static_for<4, 8>([&](auto N_) __attribute__((always_inline)) { af[2][decltype(N_)::v] = a_load<2, decltype(N_)::v>(v); });
  __builtin_amdgcn_s_setprio(0);
  wait_vm<17>();
  pin8(af[0]);
  static_for<0, 24>([&](auto S_) __attribute__((always_inline)) {
    constexpr int s = decltype(S_)::v, gp = s >> 2, k = s & 3;
    if (k == 0) { STAMP2(40 + gp); JIT(10 + gp); }
    if constexpr (k == 2) {
      constexpr int N[6] = {8, 17, 20, 12, 8, 1};
      wait_vm<N[gp]>();
      pin8(af[(gp + 1) & 3]);
      if (gp == 0) o_flag(v, v.w);
      if (gp >= 4) hs_wait(v, 1, gp + 1);
    }
    if (k == 0 && gp >= 2) hs_set(v, 0, gp);
    c_rd<s + 2>(c);
    static_for<0, 4>([&](auto J_) __attribute__((always_inline)) {
      constexpr int j = decltype(J_)::v, n = 4 * k + j;
      d[j >> 1][j & 1] = MFMA(c.wf[k][j >> 1], BF(af[gp & 3][2 * k + (j & 1)]), d[j >> 1][j & 1]);
      PIN(d[j >> 1][j & 1]);
      SBAR();
      if constexpr (gp < 4 && !(n & 1)) af[(gp + 3) & 3][n / 2] = a_load<gp + 3, n / 2>(v);
      if constexpr (gp == 1 && (n & 1)) c_piece(v, 4, 0, n / 2);
      if (s == 20 && j == 0) xv = x_load(P, v);
      SBAR();
    });
  });
  STAMP2(46);
  c_close<6>(v, c, af[2], d, c_xd(v, 0), c_xd(v, 1));
  asm volatile("s_waitcnt lgkmcnt(0)" ::: "memory");
  hs_set(v, 2 + v.g, 1);
  hs_wait(v, 2, 1);
#pragma unroll
  for (int i = 0; i < 4; ++i) *c_cell(v, c_xd(v, i >> 1), i & 1) = silu4(c_gather(v, c_xd(v, i >> 1), i & 1));
  STAMP(14);
}
DI void side_role(const Params& P, const Cx& v, u32x4 (&af)[4][8], u32x4& xv) {
  wait_vm<0>();
  o_flag(v, v.w);
  __builtin_amdgcn_s_setprio(0);
  const unsigned oown = WS_FO + ((v.rt >> 2) * 32 + 8 * v.g + (v.lane & 7)) * 64 + ((2 * v.rt + ((v.lane >> 3) & 1)) & 7) * 8;
  const unsigned opoll = blockIdx.x == 0 && v.lane >= 32 ? WS_FO + (v.g * 64 + v.lane - 32) * 64 : oown;
  auto group = [&](int gp) __attribute__((always_inline)) {
    hs_wait(v, 0, gp - 3);
    JIT(30 + gp);
#pragma unroll
    for (int i = 0; i < 8; ++i)
      glds(gp < 7 ? v.rwd : v.rwo, c_voff(v, gp, i), gp < 7 ? v.cbq : v.cbo - 32768, ((gp & 3) == 3 ? 4 : gp & 3) * REG + v.g * 8192 + i * 1024);
  };
  group(5);
  wait_vm<0>();
  hs_set(v, 1, 5);
  group(6);
  u32x2 fl = ld64h(v.ws, opoll);
  wait_vm<1>();
  hs_set(v, 1, 6);
  group(7);
  group(8);
  asm volatile("s_waitcnt vmcnt(16)" : "+v"(fl)::"memory");
  check_flags(v.ws, opoll, v.tag, fl);
  STAMP2(47);
  static_for<0, 16>([&](auto N_) __attribute__((always_inline)) {
    af[2 + decltype(N_)::v / 8][decltype(N_)::v & 7] = a_load<7 + decltype(N_)::v / 8, (decltype(N_)::v & 7)>(v);
  });
  xv = x_load(P, v);
  if (blockIdx.x == 0) fl = ld64h(v.ws, opoll + (v.lane >= 32 ? 32 * 64 : 0));
  CFrag c = c_frag(v);
  f32x16 sm[2][2] = {};
  wait_vm<9>();
  pin8(af[2]);
  c_rd<28>(c);
  c_rd<29>(c);
  STAMP2(48);
  JIT(9);
  static_for<28, 32>([&](auto S_) __attribute__((always_inline)) {
    constexpr int s = decltype(S_)::v, k = s & 3;
    c_rd<s + 2>(c);
#pragma unroll
    for (int j = 0; j < 4; ++j) {
      sm[j >> 1][j & 1] = MFMA(c.wf[k][j >> 1], BF(af[2][2 * k + (j & 1)]), sm[j >> 1][j & 1]);
      PIN(sm[j >> 1][j & 1]);
    }
    SBAR();
  });
  asm volatile("s_waitcnt vmcnt(0)" : "+v"(fl)::"memory");
  pin8(af[3]);
  c_close<8>(v, c, af[3], sm, c_xs(v, 0), c_xs(v, 1));
  if (blockIdx.x == 0) check_flags(v.ws, opoll + (v.lane >= 32 ? 32 * 64 : 0), v.tag, fl);
  STAMP(14);
}

struct AB {
  bf16x8 X[32];
  bf16x8 wf[3][2];
  bf16x8 sel;
  unsigned nm;
  float r, sd;
  int gr;
  int rb, cg, strip0, dblA, dblB;
  int kx;
  unsigned voffX, voffQ, voffF;
  rsrc_t rx, rq, rf;
};
DI void x_piece(const Cx& v, const AB& a, int kc, int slot, int i) {
  glds(a.rx, a.voffX ^ (64 * i), (a.rb * 128 + 16 * v.w + 4 * i) * 1024 + (kc ^ a.kx) * 256, slot * SLOT + (4 * v.w + i) * 1024);
}
DI void s_piece(const Cx& v, const AB& a, int kq, int i) {
  glds(a.rq, a.voffQ, (128 * (kq ^ a.kx) + 16 * v.w + 8 * i) * 3072 + a.strip0 * 128, SBUF + (kq & 1) * SUB1 + (2 * v.w + i) * 1024);
}
DI void f_piece(const Cx& v, const AB& a, int dbl, int kq, int slot, int i) {
  glds(a.rf, a.voffF, (128 * (kq ^ a.kx) + 32 * v.g + 8 * i) * 3584 + dbl * 128, slot * SLOT + v.c * SUB1 + v.g * 4096 + i * 1024);
}
template <bool TB> DI void u_piece(const Cx& v, const AB& a, int kq, int i) {
  const int slot = 1 + 2 * (kq & 1);
  if (TB) glds(a.rq, a.voffQ, (128 * (kq ^ a.kx) + 32 * v.g + 8 * i) * 3072 + (a.strip0 + 1 + v.c) * 128,
               slot * SLOT + v.c * SUB1 + v.g * 4096 + i * 1024);
  else f_piece(v, a, a.dblA, kq, slot, i);
}
DI void cs_put(const Cx& v, const f32x4& c0, const f32x4& c1, const f32x4& c2) {
  const bool hi = v.lane & 16;
  *(LAS f32x4*)(v.L + AUX + v.w * 1024 + v.lane * 16) = f32x4{hi ? c0[1] : c0[0], hi ? c1[1] : c1[0], hi ? c2[1] : c2[0], 0.f};
}
DI f32x4 cs_get(const Cx& v) {
  LAS const f32x4* p = (LAS const f32x4*)(v.L + AUX + v.c * 4096 + v.lane * 16);
  return (p[0] + p[64]) + (p[128] + p[192]);
}
DI void q_store(const Cx& v, const AB& a, const f32x16& acc, int strip, int hh) {
  const float sc = strip < 8 ? a.r * QSCALE : a.r;
  u32x4 o;
#pragma unroll
  for (int i = 0; i < 4; ++i) o[i] = pk(acc[8 * hh + 2 * i] * sc, acc[8 * hh + 2 * i + 1] * sc);
  st128h(v.ws, v.lane * 16, WS_QKV + ((strip * 4 + 2 * v.c + hh) * 64 + 4 * a.rb + v.g) * 1024, o);
}
DI void q_flag(const Cx& v, const AB& a, int strip) { put_flag(v.ws, v.lane, WS_FQ + (a.rb * 24 + strip) * 64 + v.w * 8, v.tag); }
DI void put_h(const Cx& v, const AB& a, int dbl, int j, const u32x4& hv) {
  st128h(v.ws, v.lane * 16, WS_H + ((4 * a.rb + v.g) * 112 + 4 * dbl + 2 * v.c + j) * 1024, hv);
}
struct Bases { unsigned b[4]; };
template <bool F> DI Bases mk_bases(const Cx& v, const AB& a, int base) {
  int l = v.lane;
  PIN(l);
  const int qq = (l & 15) >> 2, g1 = (l >> 4) & 1;
  const int o = base + (F ? g1 * SUB1 : 0) + (8 * (l >> 5) + qq) * 128 + ((2 * v.c) ^ (qq & 2) ^ g1) * 32 + 8 * (l & 3);
  Bases r;
#pragma unroll
  for (int i = 0; i < 4; ++i) { r.b[i] = o + (i ^ a.gr) * 4096; PIN(r.b[i]); }
  return r;
}
DI void bump(Bases& B, int d) {
#pragma unroll
  for (int i = 0; i < 4; ++i) { B.b[i] += d; PIN(B.b[i]); }
}
#define WRD(B, off, i) rdW<512>((LAS const char*)(size_t)((B).b[(i) >> 1]) + (off) + ((i) & 1) * 2048)
#define WRX(B, off, i) rdW<512>((LAS const char*)(size_t)((B).b[(i) >> 1] ^ 32u) + (off) + ((i) & 1) * 2048)

DI u32x2 make_tag(u32x2 e) {
  const unsigned long long z = ((unsigned long long)__builtin_amdgcn_readfirstlane(e.y) << 32 | (unsigned)__builtin_amdgcn_readfirstlane(e.x));
  const unsigned long long t = (z ^ (z >> 31)) * 0x9E3779B97F4A7C15ull + 0xD1B54A32D192ED03ull;
  return u32x2{(unsigned)t, (unsigned)(t >> 32)};
}

DI void stats_sum(const u32x4& xr, float& s1, float& s2) {
  s1 = s2 = 0.f;
#pragma unroll
  for (int d = 0; d < 4; ++d) {
    const float lo = __uint_as_float(xr[d] << 16), hi = __uint_as_float(xr[d] & 0xffff0000u);
    s1 += lo + hi;
    s2 = __builtin_fmaf(lo, lo, __builtin_fmaf(hi, hi, s2));
  }
}
DI void stats_put(const Cx& v, const AB& a, float s1, float s2) {
  const float mean = s1 * (1.f / 512.f);
  const float var = __builtin_fmaxf(__builtin_fmaf(-mean, mean, s2 * (1.f / 512.f)), 0.f);
  const float sd = __builtin_amdgcn_sqrtf(rbf(rbf(var) + 1e-5f));
  put_flag(v.ws, v.lane ^ 63, WS_ST + (a.rb * 128 + a.cg * 8 + v.w) * 8, u32x2{pk(-mean, sd), v.tag.x});
}
DI void row_stats(const Cx& v, AB& a, unsigned g) {
  a.sd = __uint_as_float(g & 0xffff0000u);
  a.r = __builtin_amdgcn_rcpf(a.sd);
  a.nm = v.lane < 32 ? (g & 0xffffu) | (g << 16) : 0u;
}
DI void rd_step(AB& a, const Bases& B, int s) {
  a.wf[(s + 2) % 3][0] = WRD(B, ((s >> 3) & 1) * SLOT, s & 7);
  a.wf[(s + 2) % 3][1] = WRX(B, ((s >> 3) & 1) * SLOT, s & 7);
}
template <bool TB, int Q>
DI void f_step(Cx& v, AB& a, const Bases& BS, Bases& BU, f32x16& ps, f32x16& a0, f32x16& a1, f32x4& cs, f32x4& c0, f32x4& c1,
               u32x2& st, u32x4& xr) {
  const int t = v.lane & 31;
  const unsigned u = (((v.lane >> 5) ^ (t & 15)) * 16) ^ (64 * a.gr);
  LAS const char* xb = v.L + (32 * v.g + t) * 256;
  bf16x8 ws[3];
  float s1, s2;
  f32x4 cp;
  auto rdx = [&](int q, int i) __attribute__((always_inline)) { a.X[8 * q + i] = *(LAS const bf16x8*)(xb + 2 * (q & 1) * SLOT + (u ^ (32 * i))); };
  auto rd = [&](int i) __attribute__((always_inline)) {
    a.wf[i % 3][0] = WRD(BU, 0, i);
    a.wf[i % 3][1] = TB ? WRD(BU, SUB1, i) : WRX(BU, 0, i);
    ws[i % 3] = WRD(BS, (Q & 1) * SUB1, i);
  };
  auto req = [&](int n) __attribute__((always_inline)) {
    if (Q < 3) { if (n < 2) s_piece(v, a, Q + 1, n); else u_piece<TB>(v, a, Q + 1, n - 2); }
    else if (n < 4) f_piece(v, a, a.dblB, 1, 1, n);
  };
  rd(0);
  rd(1);
  static_for<0, 8>([&](auto I_) __attribute__((always_inline)) {
    constexpr int i = decltype(I_)::v;
    if (i == 4) {
      if (Q < 3) wait_bar<Q == 2 ? 8 : 6, 15>();
      else {
        wait_bar<4, 6>();
        cp = cs_get(v);
      }
    }
    const bf16x8 x = a.X[8 * Q + i];
    a0 = MFMA(a.wf[i % 3][0], x, a0);
    PIN(a0);
    SBAR();
    if (Q < 3 && i >= 4) { rdx(Q + 1, 2 * i - 8); rdx(Q + 1, 2 * i - 7); }
    if (i < 2) { COLSUM(a.wf[i % 3][0], c0); PIN(c0); }
    if (i < 3) req(2 * i);
    if (i >= 4) {
      if (Q < 2) x_piece(v, a, Q + 2, 2 * (Q & 1), i - 4);
      else f_piece(v, a, a.dblB, 2 * (Q & 1), 2 * (Q & 1), i - 4);
    }
    if (Q == 3 && i == 5) { ln_fix(a0, cp[0], a.nm); PIN(a0); }
    SBAR();
    a1 = MFMA(a.wf[i % 3][1], x, a1);
    PIN(a1);
    SBAR();
    if (i < 2) { COLSUM(a.wf[i % 3][1], c1); PIN(c1); }
    if (i < 3) req(2 * i + 1);
    if (Q == 3 && i == 6) { ln_fix(a1, cp[1], a.nm); PIN(a1); }
    if (Q == 1) {
      if (i == 3) { PIN(st); PIN(xr); v.tag = make_tag(st); stats_sum(xr, s1, s2); PIN(s1); PIN(s2); }
      if (i == 4) { s1 = wave_sum63(s1); PIN(s1); }
      if (i == 5) { s2 = wave_sum63(s2); PIN(s2); }
      if (i == 6) { JIT(1); stats_put(v, a, s1, s2); }
    }
    if (Q == 2 && i == 3) st = ld64h(v.ws, WS_ST + (a.rb * 128 + 32 * v.g + (v.lane & 31)) * 8);
    if (Q == 3 && i == 2) {
      PIN(st);
      check_granule(v.ws, WS_ST + (a.rb * 128 + 32 * v.g + (v.lane & 31)) * 8, v.tag.x, st);
      row_stats(v, a, st.x);
      PIN(a.nm); PIN(a.r); PIN(a.sd);
    }
    SBAR();
    const bf16x8 wsi = ws[i % 3];
    ps = MFMA(wsi, x, ps);
    PIN(ps);
    SBAR();
    if (i < 6) rd(i + 2);
    if (i < 2) { COLSUM(wsi, cs); PIN(cs); }
    if (Q == 3) {
      if (i == 2) cs_put(v, c0, c1, cs);
      if (i == 4) { ln_fix(ps, cp[2], a.nm); PIN(ps); }
      if (i == 6) BU = mk_bases<true>(v, a, 0);
      if (i >= 6) rd_step(a, BU, i - 6);
    } else if (i == 6) bump(BU, (Q & 1) ? -2 * SLOT : 2 * SLOT);
    if (Q == 1 && i == 7) {
      const int per = v.w == 1 ? 16 : 56;
      asm volatile("buffer_load_dword %0, %1, %2, 0 offen"
                   : "=v"(xr.x) : "v"(v.lane < per && v.w < 2 ? (blockIdx.x * per + v.lane) * 128 : 0x40000000u), "s"(v.w == 0 ? v.rwd : v.rwo)
                   : "memory");
    }
    SBAR();
  });
}
template <bool TB>
DI void f_step0(Cx& v, AB& a, const Bases& BS, Bases& BU, f32x16& ps, f32x16& a0, f32x16& a1, f32x4& cs, f32x4& c0, f32x4& c1,
                u32x2& st, u32x4& xr) {
  const int t = v.lane & 31;
  const unsigned u = (((v.lane >> 5) ^ (t & 15)) * 16) ^ (64 * a.gr);
  LAS const char* xb = v.L + (32 * v.g + t) * 256;
  bf16x8 ws[8];
#pragma unroll
  for (int i = 0; i < 3; ++i) {
    a.X[i] = *(LAS const bf16x8*)(xb + (u ^ (32 * i)));
    ws[i] = WRD(BS, 0, i);
  }
#pragma unroll
  for (int i = 0; i < 8; ++i) {
    if (i < 5) {
      a.X[i + 3] = *(LAS const bf16x8*)(xb + (u ^ (32 * (i + 3))));
      ws[i + 3] = WRD(BS, 0, i + 3);
    }
    if (i < 4) x_piece(v, a, 1, 2, i);
    else if (i < 6) s_piece(v, a, 1, i - 4);
    else u_piece<TB>(v, a, 1, i - 6);
    ps = MFMA(ws[i], a.X[i], ps);
    if (i < 2) COLSUM(ws[i], cs);
    PIN(ps); PIN(cs);
    SBAR();
  }
  wait_bar<8, 0>();
  auto rd = [&](int i) __attribute__((always_inline)) {
    a.wf[i % 3][0] = WRD(BU, 0, i);
    a.wf[i % 3][1] = TB ? WRD(BU, SUB1, i) : WRX(BU, 0, i);
  };
  rd(0);
  rd(1);
  static_for<0, 8>([&](auto I_) __attribute__((always_inline)) {
    constexpr int i = decltype(I_)::v;
    if (i == 4) wait_bar<10, 15>();
    const bf16x8 w0 = a.wf[i % 3][0], w1 = a.wf[i % 3][1];
    a0 = MFMA(w0, a.X[i], a0);
    PIN(a0);
    SBAR();
    if (i < 6) rd(i + 2);
    if (i >= 4) a.X[2 * i] = *(LAS const bf16x8*)(xb + 2 * SLOT + (u ^ (32 * (2 * i - 8))));
    if (i < 2) { COLSUM(w0, c0); PIN(c0); u_piece<TB>(v, a, 1, 2 + i); }
    else if (i < 6) x_piece(v, a, 2, 0, i - 2);
    SBAR();
    a1 = MFMA(w1, a.X[i], a1);
    PIN(a1);
    SBAR();
    if (i >= 4) a.X[2 * i + 1] = *(LAS const bf16x8*)(xb + 2 * SLOT + (u ^ (32 * (2 * i - 7))));
    if (i < 2) { COLSUM(w1, c1); PIN(c1); }
    if (i == 1) {
      st = ld64h(v.ws, WS_EPOCH);
      asm volatile("buffer_load_dwordx4 %0, %1, %2, %3 offen"
                   : "=v"(xr) : "v"(v.lane * 16), "s"(a.rx), "s"(RFL((a.rb * 128 + a.cg * 8 + v.w) * 1024)) : "memory");
    }
    if (i == 6) bump(BU, 2 * SLOT);
    SBAR();
  });
}
template <bool TB> DI void front(Cx& v, AB& a, Bases& BU, f32x16& ps, f32x16& a0, f32x16& a1) {
  const Bases BS = mk_bases<false>(v, a, SBUF);
  BU = mk_bases<!TB>(v, a, SLOT);
  f32x4 cs = {}, c0 = {}, c1 = {};
  u32x2 st;
  u32x4 xr;
  STAMP2(23);
  wait_bar<4, 0>();
  STAMP(1);
  f_step0<TB>(v, a, BS, BU, ps, a0, a1, cs, c0, c1, st, xr);
  STAMP2(16); wait_bar<4, 0>(); STAMP2(17); JIT(24);
  f_step<TB, 1>(v, a, BS, BU, ps, a0, a1, cs, c0, c1, st, xr);
  STAMP2(18); wait_bar<6, 0>(); STAMP2(19); JIT(25);
  f_step<TB, 2>(v, a, BS, BU, ps, a0, a1, cs, c0, c1, st, xr);
  STAMP2(20); wait_bar<4, 0>(); STAMP2(21); JIT(26);
  PIN(xr);
  f_step<TB, 3>(v, a, BS, BU, ps, a0, a1, cs, c0, c1, st, xr);
  STAMP2(22);
  wait_bar<4, 8>();
}
template <bool TB> DI void body(Cx& v, AB& a, Bases& B, const f32x16& ps, f32x16& p0, f32x16& p1, AtPre& ap) {
  f32x16 a0 = {}, a1 = {};
  f32x4 c0 = {}, c1 = {};
  float hlo;
  f32x4 cp;
  u32x4 hv;
  u32x2 fl;
  unsigned fo;
  static_for<0, 32>([&](auto S_) __attribute__((always_inline)) {
    constexpr int s = decltype(S_)::v, kq = s >> 3, i = s & 7;
    if (s == 2) { int l = v.lane; PIN(l); a.sel = mk_sel(l); }
    const bf16x8 w0 = a.wf[(s + 2) % 3][0], w1 = a.wf[(s + 2) % 3][1];
    constexpr bool CS = (i >> 1) == 1;
    constexpr int e = TB ? -1 : (s == 30 ? 14 : ((s & 1) && s >= 3 ? (s == 31 ? 15 : (s - 3) >> 1) : -1));
    float ga, ex;
    a0 = MFMA(w0, a.X[s], a0);
    PIN(a0);
    SBAR();
    if (s == 14) bump(B, 2 * SLOT);
    if (s < 30) rd_step(a, B, s + 2);
    if (s == 7) c_setup(v);
    if (CS) { COLSUM(w0, c0); PIN(c0); }
    if ((i & 1) == 0) {
      if (kq == 0) f_piece(v, a, a.dblB, 3, 3, i >> 1);
      else c_piece(v, kq - 1, kq - 1, i >> 1);
    }
    if constexpr (e >= 0) {
      ga = (e < 8 ? p0 : p1)[e & 7] * a.r;
      ex = geglu_exp(ga);
      ga *= (e < 8 ? p0 : p1)[8 + (e & 7)];
      PIN(ga); PIN(ex);
    }
    SBAR();
    a1 = MFMA(w1, a.X[s], a1);
    PIN(a1);
    SBAR();
    if (CS) { COLSUM(w1, c1); PIN(c1); }
    if constexpr (e >= 0) {
      const float hx = geglu_fin(ga, ex, a.sd);
      if (e & 1) { hv[(e & 7) >> 1] = pk(hlo, hx); PIN(hv[(e & 7) >> 1]); }
      else { hlo = hx; PIN(hlo); }
      if (e == 15) put_h(v, a, a.dblA, 1, hv);
    }
    if (s < 2) q_store(v, a, ps, a.strip0, s);
    if (s == 8) { wait_vm<4>(); q_flag(v, a, a.strip0); }
    if (!TB && s == 18) put_h(v, a, a.dblA, 0, hv);
    if (TB) {
      if (s == 3 || s == 5) q_store(v, a, p0, a.strip0 + 1, (s - 3) >> 1);
      if (s == 7 || s == 9) q_store(v, a, p1, a.strip0 + 2, (s - 7) >> 1);
      if (s == 14) { wait_vm<3>(); q_flag(v, a, a.strip0 + 1); q_flag(v, a, a.strip0 + 2); }
    }
    if (s == 28) {
      cs_put(v, c0, c1, c1);
      SBAR();
      wait_bar<63, 0>();
      cp = cs_get(v);
    }
    if (s == 30) { ln_fix(a0, cp[0], a.nm); PIN(a0); }
    if (s == 31) { ln_fix(a1, cp[1], a.nm); PIN(a1); }
    if (s == 23) { fo = at_flag_off(v); fl = ld64h(v.ws, fo); }
    if (s == 27) {
      asm volatile("s_waitcnt vmcnt(2)" : "+v"(fl)::"memory");
      check_flags(v.ws, fo, v.tag, fl);
      ap.k0 = at_load(v, 1);
    }
    if (s == 28) ap.k1 = at_load(v, 2);
    if (s == 29) ap.v0 = at_load(v, 3);
    if (s == 30) ap.v1 = at_load(v, 4);
    if (s == 31) ap.q = at_load(v, 0);
    SBAR();
    if (i == 7 && kq < 3) {
      STAMP2(36 + kq);
      if (kq < 2) wait_bar<4, 8>();
      else wait_bar<63, 8>();
      STAMP2(54 + kq);
      JIT(20 + kq);
    }
  });
  p0 = a0;
  p1 = a1;
}
DI void ab_tail(const Cx& v, const AB& a, int dbl, const f32x16& p0, const f32x16& p1, AtPre& ap) {
  u32x4 hv;
  static_for<0, 8>([&](auto I_) __attribute__((always_inline)) {
    constexpr int i = decltype(I_)::v, j = 2 * (i & 3);
    const f32x16& p = i < 4 ? p0 : p1;
    hv[i & 3] = pk(geglu(p[j], p[8 + j], a.r, a.sd), geglu(p[j + 1], p[9 + j], a.r, a.sd));
    PIN(hv[i & 3]);
    if constexpr ((i & 3) == 3) put_h(v, a, dbl, i >> 2, hv);
    SBAR();
  });
  asm volatile("s_waitcnt vmcnt(2)\n\ts_barrier" : "+v"(ap.q), "+v"(ap.k0), "+v"(ap.k1), "+v"(ap.v0), "+v"(ap.v1)::"memory");
  JIT(4);
  at_stage(v, ap);
  wait_vm<0>();
  put_flag(v.ws, v.lane, WS_FH + (a.rb * 16 + a.cg) * 64 + v.w * 8, v.tag);
  wait_bar<63, 0>();
}

template <bool TB> DI void ab_path(Cx& v, AB a, AtPre& ap) {
  int l = v.lane;
  PIN(l);
  a.voffX = (l >> 4) * 1024 + (((l & 15) ^ (l >> 4)) * 16);
  a.voffQ = w_voff(l, 3072);
  a.voffF = w_voff(l, 3584, v.c);
  a.sel = mk_sel(l);
#pragma unroll
  for (int i = 0; i < 4; ++i) u_piece<TB>(v, a, 0, i);
  f32x16 ps = {}, p0 = {}, p1 = {};
  Bases B;
  front<TB>(v, a, B, ps, p0, p1);
  STAMP(3);
  JIT(2);
  body<TB>(v, a, B, ps, p0, p1, ap);
  STAMP(4);
  JIT(3);
  ab_tail(v, a, a.dblB, p0, p1, ap);
}

__global__ __launch_bounds__(512, 2) void fused_kernel(Params P) {
  extern __shared__ __attribute__((aligned(16))) char smem[];
  Cx v;
  v.L = (LAS char*)smem;
  v.lane = threadIdx.x & 63;
  v.w = __builtin_amdgcn_readfirstlane(threadIdx.x >> 6);
  v.g = v.w & 3;
  v.c = v.w >> 2;
  const int xc = blockIdx.x & 7, idx = blockIdx.x >> 3;
  v.ws = mk_rsrc(P.ws, WS_END);
  v.rwd = mk_rsrc(P.wd, 1792 * 512 * 2);
  v.rwo = mk_rsrc(P.wo, 512 * 512 * 2);
  v.rt = v.hd = xc * 4 + (idx & 3);
  v.ct = v.G = idx >> 2;
  AtPre ap;
  STAMPR(30); STAMP(0);
  JIT(0);
  {
    AB a;
    a.rb = (xc & 1) * 8 + (idx & 7);
    const int cq = xc >> 1, j = idx >> 3;
    const bool typeB = j == 3;
    a.cg = cq * 4 + j;
    a.gr = v.g;
    a.strip0 = 6 * cq + j;
    a.dblA = 7 * cq + 2 * j;
    a.dblB = typeB ? a.dblA : a.dblA + 1;
    a.kx = a.rb & 3;
    a.rx = mk_rsrc(P.x, 2048 * 512 * 2);
    a.rq = mk_rsrc(P.wqkv, 512 * 1536 * 2);
    a.rf = mk_rsrc(v.c ? P.wu : P.wg, 512 * 1792 * 2);
    a.voffX = (v.lane >> 4) * 1024 + (((v.lane & 15) ^ (v.lane >> 4)) * 16);
    a.voffQ = w_voff(v.lane, 3072);
    STAMP2(24);
#pragma unroll
    for (int i = 0; i < 4; ++i) x_piece(v, a, 0, 0, i);
#pragma unroll
    for (int i = 0; i < 2; ++i) s_piece(v, a, 0, i);
    if (typeB) ab_path<true>(v, a, ap);
    else ab_path<false>(v, a, ap);
  }
  STAMP(6);
  JIT(5);
  c_setup(v);
  v.tag = u32x2{uni(v.tag.x), uni(v.tag.y)};
  u32x4 af[4][8];
  __builtin_amdgcn_s_setprio(2);
  if (v.G) at_body<true>(v, ap, af);
  else at_body<false>(v, ap, af);
  STAMP(7);
  JIT(6);
  c_setup(v);
  u32x4 xv;
  if (v.c) {
    side_role(P, v, af, xv);
    undef8(af[0]); undef8(af[1]); undef8(af[2]);
  }
  if (!uni(v.c)) gemm_role(P, v, af, xv);
  asm volatile("s_waitcnt vmcnt(0) lgkmcnt(0)\n\ts_barrier" : "+v"(xv)::"memory");
  if (blockIdx.x == 0 && v.w == 0) put_flag(v.ws, v.lane, WS_EPOCH, v.tag);
  const f32x4 ga = *c_cell(v, c_xd(v, v.c), 0), gb = *c_cell(v, c_xd(v, v.c), 1);
  const u32x2 y0 = fin4(u32x2{xv.x, xv.y}, c_gather(v, c_xs(v, v.c), 0), ga), y1 = fin4(u32x2{xv.z, xv.w}, c_gather(v, c_xs(v, v.c), 1), gb);
  __builtin_amdgcn_raw_buffer_store_b128(u32x4{y0.x, y0.y, y1.x, y1.y}, mk_rsrc(P.out, 2048 * 512 * 2), c_xoff(v), 0, 16);
  STAMP(15); STAMPR(31);
}
}

extern "C" void kernel_launch(void* const* d_in, const int* in_sizes, int n_in, void* d_out, int out_size, void* d_ws,
                              size_t ws_size, hipStream_t stream) {
  Params P;
  P.x = d_in[0]; P.wqkv = d_in[1]; P.wo = d_in[2];
  P.wg = d_in[4]; P.wu = d_in[5]; P.wd = d_in[6];
  P.out = d_out; P.ws = d_ws;
  (void)hipFuncSetAttribute((const void*)fused_kernel, hipFuncAttributeMaxDynamicSharedMemorySize, LDS_BYTES);
  fused_kernel<<<256, 512, LDS_BYTES, stream>>>(P);
}
```

```cpp
#include <hip/hip_runtime.h>
#include <hip/hip_bf16.h>
#include <stdint.h>

typedef short bf16x8 __attribute__((ext_vector_type(8)));
typedef short s16x4 __attribute__((ext_vector_type(4)));
typedef float f32x16 __attribute__((ext_vector_type(16)));
typedef float f32x4 __attribute__((ext_vector_type(4)));
typedef float f32x2 __attribute__((ext_vector_type(2)));
typedef unsigned u32x4 __attribute__((ext_vector_type(4)));
typedef unsigned u32x2 __attribute__((ext_vector_type(2)));
typedef __bf16 bf2 __attribute__((ext_vector_type(2)));
typedef __amdgpu_buffer_rsrc_t rsrc_t;
#define LAS __attribute__((address_space(3)))
#define DI __device__ __forceinline__
#define SBAR() __builtin_amdgcn_sched_barrier(0)
#define PIN(x) asm volatile("" : "+v"(x))
#define RFL(x) __builtin_amdgcn_readfirstlane(x)
#define MFMA(a, b, c) __builtin_amdgcn_mfma_f32_32x32x16_bf16((a), (b), (c), 0, 0, 0)

#define STAMP(i)
#define STAMPR(i)
#define JIT(p)
#define STAMP2(i)

namespace {
constexpr unsigned WS_EPOCH = 0;
constexpr unsigned WS_FQ = 1024;
constexpr unsigned WS_FH = WS_FQ + 16 * 24 * 64;
constexpr unsigned WS_FO = WS_FH + 16 * 16 * 64;
constexpr unsigned WS_ST = 65536;
constexpr unsigned WS_QKV = 131072;
constexpr unsigned SEC_BYTES = 32 * 64 * 1024;
constexpr unsigned WS_H = WS_QKV + 3 * SEC_BYTES;
constexpr unsigned WS_O = WS_H + 64 * 112 * 1024;
constexpr unsigned WS_END = WS_O + 64 * 32 * 1024;
static_assert(WS_FO + 256 * 64 <= WS_ST, "flags overlap");
constexpr int SLOT = 32768, SUB1 = 16384;
constexpr int SBUF = 4 * SLOT;
constexpr int AUX = SBUF;
constexpr int REG = 32768;
constexpr int LDS_BYTES = 163840;
constexpr int KV = 3 * REG;
constexpr float QSCALE = 0.25f * 1.4426950408889634f;
constexpr unsigned SPIN_CAP = 1u << 18;

struct Params {
  const void *x, *wqkv, *wo, *wg, *wu, *wd;
  void *out, *ws;
};

template <int I> struct IC { static constexpr int v = I; };
template <int I, int N, class F> DI void static_for(F&& f) {
  if constexpr (I < N) {
    f(IC<I>{});
    static_for<I + 1, N>(f);
  }
}
template <int VM> DI void wait_vm() { asm volatile("s_waitcnt vmcnt(%0)" ::"n"(VM) : "memory"); }
template <int VM, int LG> DI void wait_bar() { asm volatile("s_waitcnt vmcnt(%0) lgkmcnt(%1)\n\ts_barrier" ::"n"(VM), "n"(LG) : "memory"); }
DI unsigned uni(unsigned x) {
  x = __builtin_amdgcn_readfirstlane(x);
  asm volatile("" : "+s"(x));
  return x;
}
DI rsrc_t mk_rsrc(const void* p, unsigned bytes) { return __builtin_amdgcn_make_buffer_rsrc((void*)p, 0, bytes, 0x00020000); }
DI void glds(rsrc_t rs, unsigned voff, unsigned soff, unsigned lds) {
  asm volatile("s_mov_b32 m0, %2\n\ts_nop 0\n\tbuffer_load_dwordx4 %0, %1, %3 offen lds" ::"v"(voff), "s"(rs),
               "s"(RFL(lds)), "s"(RFL(soff)) : "memory");
}
template <int IMM = 0> DI u32x4 ld128h(rsrc_t rs, unsigned voff, unsigned soff) {
  u32x4 r;
  asm volatile("buffer_load_dwordx4 %0, %1, %2, %3 offen offset:%4 sc1" : "=v"(r) : "v"(voff), "s"(rs), "s"(RFL(soff)), "n"(IMM) : "memory");
  return r;
}
DI u32x2 ld64h(rsrc_t rs, unsigned voff) {
  u32x2 r;
  asm volatile("buffer_load_dwordx2 %0, %1, %2, 0 offen sc1" : "=v"(r) : "v"(voff), "s"(rs) : "memory");
  return r;
}
DI void st128h(rsrc_t rs, unsigned voff, unsigned soff, u32x4 d) {
  asm volatile("buffer_store_dwordx4 %0, %1, %2, %3 offen sc1\n\ts_nop 1" ::"v"(d), "v"(voff), "s"(rs), "s"(RFL(soff)) : "memory");
}
DI void put_flag(rsrc_t ws, int lane, unsigned soff, u32x2 tag) {
  asm volatile("buffer_store_dwordx2 %0, %1, %2, %3 offen sc1" ::"v"(tag), "v"(lane ? 0x80000000u : 0u), "s"(ws), "s"(RFL(soff)) : "memory");
}
DI void check_flags(rsrc_t ws, unsigned voff, u32x2 tag, u32x2 v) {
  unsigned cnt;
  asm volatile(
      "s_mov_b32 %1, %5\n"
      "1:\n\t"
      "v_cmp_ne_u64 vcc, %2, %0\n\t"
      "s_cbranch_vccz 2f\n\t"
      "s_sleep 1\n\t"
      "buffer_load_dwordx2 %0, %3, %4, 0 offen sc1\n\t"
      "s_waitcnt vmcnt(0)\n\t"
      "s_sub_u32 %1, %1, 1\n\t"
      "s_cbranch_scc0 1b\n"
      "2:"
      : "+v"(v), "=&s"(cnt)
      : "s"(tag), "v"(voff), "s"(ws), "n"(SPIN_CAP)
      : "vcc", "scc", "memory");
}
DI void check_granule(rsrc_t ws, unsigned voff, unsigned tag, u32x2& v) {
  unsigned cnt, lo = v.x, hi = v.y;
  asm volatile(
      "s_mov_b32 %2, %6\n"
      "1:\n\t"
      "v_cmp_ne_u32 vcc, %3, %1\n\t"
      "s_cbranch_vccz 2f\n\t"
      "s_sleep 1\n\t"
      "buffer_load_dword %1, %4, %5, 0 offen offset:4 sc1\n\t"
      "s_waitcnt vmcnt(0)\n\t"
      "buffer_load_dword %0, %4, %5, 0 offen sc1\n\t"
      "s_waitcnt vmcnt(0)\n\t"
      "s_sub_u32 %2, %2, 1\n\t"
      "s_cbranch_scc0 1b\n"
      "2:"
      : "+v"(lo), "+v"(hi), "=&s"(cnt)
      : "s"(tag), "v"(voff), "s"(ws), "n"(SPIN_CAP)
      : "vcc", "scc", "memory");
  v.x = lo;
}
template <int CTRL> DI float dpp_add(float v) {
  return v + __builtin_bit_cast(float, __builtin_amdgcn_update_dpp(0, __builtin_bit_cast(int, v), CTRL, 0xf, 0xf, true));
}
DI float wave_sum63(float v) {
  v = dpp_add<0x111>(v); v = dpp_add<0x112>(v); v = dpp_add<0x114>(v); v = dpp_add<0x118>(v);
  v = dpp_add<0x142>(v);
  return dpp_add<0x143>(v);
}
DI s16x4 trd(LAS const char* p) { return __builtin_amdgcn_ds_read_tr16_b64_v4i16((LAS s16x4*)p); }
template <int STEP> DI bf16x8 rdW(LAS const char* p) {
  const s16x4 a = trd(p), b = trd(p + STEP);
  return __builtin_shufflevector(a, b, 0, 1, 2, 3, 4, 5, 6, 7);
}
DI unsigned pk(float lo, float hi) {
  const f32x2 v = {lo, hi};
  return __builtin_bit_cast(unsigned, __builtin_convertvector(v, bf2));
}
DI float rbf(float v) { return __builtin_bit_cast(float, pk(0.f, v) & 0xffff0000u); }
#define COLSUM(w, cs) cs = __builtin_amdgcn_mfma_f32_16x16x32_bf16(a.sel, (w), cs, 0, 0, 0)
DI bf16x8 mk_sel(int l) {
  const unsigned sl = ((l ^ (l >> 4)) & 1) ? 0u : 0x3f803f80u;
  return __builtin_bit_cast(bf16x8, u32x4{sl, sl, sl, sl});
}
DI float swap32_max(float m) {
  auto r = __builtin_amdgcn_permlane32_swap(__float_as_uint(m), __float_as_uint(m), false, false);
  return __builtin_fmaxf(__uint_as_float(r[0]), __uint_as_float(r[1]));
}
DI f32x16 splat(float v) {
  f32x16 r;
#pragma unroll
  for (int i = 0; i < 16; ++i) r[i] = v;
  return r;
}
DI void ln_fix(f32x16& acc, float c, unsigned nm) {
  const unsigned u0 = __float_as_uint(c);
  const float r1 = c - __uint_as_float(u0 & 0xffff0000u);
  const unsigned u1 = __float_as_uint(r1);
  const float r2 = r1 - __uint_as_float(u1 & 0xffff0000u);
  const u32x2 a = {(u0 >> 16) | (u1 & 0xffff0000u), __float_as_uint(r2) >> 16};
  acc = __builtin_amdgcn_mfma_f32_32x32x8bf16_1k(__builtin_bit_cast(s16x4, a), __builtin_bit_cast(s16x4, u32x2{nm, nm}), acc, 0, 0, 0);
}
DI float geglu_exp(float t) {
  const float c1 = -2.f * 0.7978845608028654f * 1.4426950408889634f;
  return __builtin_amdgcn_exp2f(t * __builtin_fmaf(t * t, c1 * 0.044715f, c1));
}
DI float geglu_fin(float tb, float e, float sd) { return tb * __builtin_amdgcn_rcpf(__builtin_fmaf(e, sd, sd)); }
DI float geglu(float a, float b, float r, float sd) {
  const float t = a * r;
  return geglu_fin(t * b, geglu_exp(t), sd);
}

DI unsigned w_voff(int lane, int pitch, int x = 0) {
  PIN(lane);
  const int r8 = lane >> 3, p8 = lane & 7;
  return r8 * pitch + (((p8 >> 1) ^ (r8 & 2) ^ x) * 32) + (p8 & 1) * 16;
}
struct Cx {
  int lane, w, g, c;
  int rt, ct, rot;
  int hd, G;
  unsigned voffC;
  unsigned cbd, cbq, cbo, hb, ob;
  rsrc_t ws, rwd, rwo;
  u32x2 tag;
  LAS char* L;
};
DI void c_setup(Cx& v) {
  v.voffC = w_voff(v.lane, 1024);
  v.cbd = uni((448 * v.g + 32 * v.c) * 1024 + v.ct * 128);
  v.cbq = uni(448 * v.g * 1024 + v.ct * 128);
  v.cbo = uni((128 * v.g + 32 * v.c) * 1024 + v.ct * 128);
  v.hb = uni(WS_H + (2 * v.rt * 112 + 28 * v.g) * 1024);
  v.ob = uni(WS_O + (2 * v.rt * 32 + 8 * v.g) * 1024);
  v.rot = uni((v.rt & 3) * 2);
}
DI int c_grp(const Cx& v, int gp) {
  if (gp >= 7) return (gp - 7) ^ (v.rot & 1);
  const int r = gp + v.rot;
  return r >= 7 ? r - 7 : r;
}
DI unsigned c_voff(const Cx& v, int gp, int i) { return v.voffC + (c_grp(v, gp) << 16) + i * 8192; }
DI unsigned c_dst(const Cx& v, int region, int i) { return region * REG + v.g * 8192 + v.c * 4096 + i * 1024; }
DI void c_piece(const Cx& v, int gp, int region, int i) {
  glds(gp < 7 ? v.rwd : v.rwo, c_voff(v, gp, i), gp < 7 ? v.cbd : v.cbo, c_dst(v, region, i));
}

DI unsigned hs_word(const Cx& v, int i) { return KV + 128 + (i < 2 ? v.g * 8 + i * 4 : 32 + (i - 2) * 4); }
DI void hs_set(const Cx& v, int i, int val) { *(volatile LAS int*)(v.L + hs_word(v, i)) = val; }
DI void hs_wait(const Cx& v, int i, int need) {
  unsigned cnt;
  int x;
  asm volatile(
      "s_mov_b32 %1, %4\n"
      "1:\n\t"
      "ds_read_b32 %0, %2\n\t"
      "s_waitcnt lgkmcnt(0)\n\t"
      "v_cmp_gt_i32 vcc, %3, %0\n\t"
      "s_cbranch_vccz 2f\n\t"
      "s_sleep 2\n\t"
      "s_sub_u32 %1, %1, 1\n\t"
      "s_cbranch_scc0 1b\n"
      "2:"
      : "=&v"(x), "=&s"(cnt)
      : "v"(hs_word(v, i) + (i == 2 ? (v.lane & 3) * 4 : 0)), "n"(need), "n"(SPIN_CAP * 8)
      : "vcc", "scc", "memory");
}

struct AtPre { u32x4 q, k0, k1, v0, v1; };
DI int at_kb(const Cx& v) {
  const int kb = 8 * v.G - 8 + 2 * v.w;
  return kb < 0 ? 0 : kb;
}
DI u32x4 at_load(const Cx& v, int i) {
  const int blk = i == 0 ? 8 * v.G + v.w : at_kb(v) + ((i - 1) & 1);
  return ld128h(v.ws, v.lane * 16, WS_QKV + ((i + 1) >> 1) * SEC_BYTES + (v.hd * 64 + blk) * 1024);
}
DI unsigned at_flag_off(const Cx& v) {
  const int i = v.lane & 7, isq = i >> 2;
  const int blk = at_kb(v) + (i & 1) + isq * (8 * v.G + v.w - at_kb(v) - (i & 1));
  const int sec = (1 - isq) * (1 + ((i >> 1) & 1));
  return WS_FQ + ((blk >> 2) * 24 + sec * 8 + (v.hd >> 2)) * 64 + (((v.hd >> 1) & 1) * 4 + (blk & 3)) * 8;
}
DI void at_stage(const Cx& v, const AtPre& a) {
  LAS char* kd = v.L + KV + 2 * v.w * 1024 + v.lane * 16;
  LAS char* vd = v.L + KV + 16384 + 2 * v.w * 1024 + (v.lane & 31) * 32 + (v.lane >> 5) * 16;
  const unsigned one = (v.lane & 8) ? 0u : 0x3f803f80u;
  *(LAS u32x4*)kd = v.w == 0 ? u32x4{one, one, one, one} : a.k0;
  *(LAS u32x4*)(kd + 1024) = a.k1;
  *(LAS u32x4*)vd = a.v0;
  *(LAS u32x4*)(vd + 1024) = a.v1;
}
DI void max16(const f32x16& s, float& m, float& m2) {
#pragma unroll
  for (int r = 0; r < 16; r += 4) {
    m = __builtin_fmaxf(__builtin_fmaxf(m, s[r]), s[r + 1]);
    m2 = __builtin_fmaxf(__builtin_fmaxf(m2, s[r + 2]), s[r + 3]);
  }
}
DI void exp_pack(const f32x16& s, u32x4& p0, u32x4& p1) {
#pragma unroll
  for (int i = 0; i < 4; ++i) {
    p0[i] = pk(__builtin_amdgcn_exp2f(s[2 * i]), __builtin_amdgcn_exp2f(s[2 * i + 1]));
    p1[i] = pk(__builtin_amdgcn_exp2f(s[8 + 2 * i]), __builtin_amdgcn_exp2f(s[9 + 2 * i]));
  }
}
#define PIN4(a, o) asm volatile("" : "+v"(a[o]), "+v"(a[o + 1]), "+v"(a[o + 2]), "+v"(a[o + 3]))
#define BF(x) __builtin_bit_cast(bf16x8, x)
DI void pin8(u32x4 (&a)[8]) { PIN4(a, 0); PIN4(a, 4); }
DI void undef8(u32x4 (&a)[8]) {
  asm volatile("" : "=v"(a[0]), "=v"(a[1]), "=v"(a[2]), "=v"(a[3]), "=v"(a[4]), "=v"(a[5]), "=v"(a[6]), "=v"(a[7]));
}
DI void o_flag(const Cx& v, int qb) { put_flag(v.ws, v.lane, WS_FO + (v.G * 32 + v.hd) * 64 + qb * 8, v.tag); }
struct AtLane {
  LAS const char *kp, *vp;
  int vstr;
  int td;
};
#define KFRAG(kb) (*(LAS const bf16x8*)(a.kp + (kb) * 1024))
#define VFRAG(kb, s) rdW<256>(a.vp + (kb) * a.vstr + (s) * 512)
DI AtLane at_lane(const Cx& v, int qb) {
  const int lane = v.lane, h = lane >> 5, qq = (lane & 15) >> 2, p = lane & 3;
  AtLane a;
  a.kp = v.L + KV + qb * 1024 + lane * 16;
  int vreal = KV + 16384 + qb * 1024 + (4 * h + qq) * 32 + (p & 1) * 16 + (p >> 1) * 8, vone = KV + (lane & 15) * 8;
  PIN(vreal); PIN(vone);
  a.vp = v.L + ((lane & 16) ? vone : vreal);
  a.vstr = (lane & 16) ? 0 : 1024;
  a.td = (lane & 31) - 4 * h;
  return a;
}
DI f32x16 at_merge(const AtLane& a, const f32x16& sd, const f32x16& sf) {
  f32x16 r;
#pragma unroll
  for (int i = 0; i < 16; ++i) r[i] = (i & 3) + 8 * (i >> 2) <= a.td ? sd[i] : sf[i];
  return r;
}
DI u32x4 at_mask(const AtLane& a, int half) {
  u32x4 k;
#pragma unroll
  for (int i = 0; i < 4; ++i) {
    const int r0 = 2 * (i & 1) + 8 * (i >> 1) + 16 * half;
    k[i] = (r0 <= a.td ? 0xffffu : 0u) | (r0 + 1 <= a.td ? 0xffff0000u : 0u);
  }
  return k;
}
DI void o_store(const Cx& v, int qb, const f32x16& o) {
  const float il = __builtin_amdgcn_rcpf(o[8]);
  u32x4 ov;
#pragma unroll
  for (int i = 0; i < 4; ++i) ov[i] = pk(o[2 * i] * il, o[2 * i + 1] * il);
  st128h(v.ws, v.lane * 16, WS_O + ((8 * v.G + qb) * 32 + v.hd) * 1024, ov);
}
template <int GP, int N> DI u32x4 a_load(const Cx& v) {
  return ld128h<(N >> 1) * 1024>(v.ws, v.lane * 16 + (c_grp(v, GP) << 12) + (N & 1) * (GP < 7 ? 112 : 32) * 1024, GP < 7 ? v.hb : v.ob);
}
DI void w_early(const Cx& v, int q, u32x4 (&af)[4][8]) {
  if (v.c) {
#pragma unroll
    for (int i = (2 * q) & 7; i < ((2 * q) & 7) + 2; ++i)
      asm volatile("buffer_load_dwordx4 %0, %1, %2, %3 offen"
                   : "=v"(af[q >> 2][i]) : "v"(c_voff(v, 5 + (q >> 2), i)), "s"(v.rwd), "s"(v.cbq) : "memory");
  } else c_piece(v, 3, 4, q);
}
template <bool FULL> DI void at_body(const Cx& v, const AtPre& ap, u32x4 (&af)[4][8]) {
  const AtLane a = at_lane(v, v.w);
  const bf16x8 q = BF(ap.q);
  const float NEG = -1e30f;
  const f32x16 zero = {};
  const int th = (v.lane >> 3) & 1;
  const unsigned hpoll = WS_FH + ((v.rt >> 1) * 16 + 4 * v.g + (v.lane & 3)) * 64 + (((v.lane >> 2) & 1) * 4 + ((2 * v.rt + th) & 3)) * 8;
  float m = NEG, m2 = NEG;
  f32x16 o = zero, cm, sc;
  u32x4 p0, p1;
  u32x2 fl;
  if (FULL) {
    bf16x8 kf[9];
#pragma unroll
    for (int kb = 0; kb < 9; ++kb) kf[(kb + 8) % 9] = KFRAG((kb + 8) % 9);
    if (v.w == 0) kf[0] = BF(ap.k0);
    f32x16 sa = MFMA(kf[8], q, zero), sb = MFMA(kf[0], q, zero);
    sc = MFMA(kf[1], q, zero);
    PIN(sa); PIN(sb); PIN(sc);
    SBAR();
    w_early(v, 0, af);
    max16(at_merge(a, sa, sb), m, m2);
    static_for<1, 8>([&](auto K_) __attribute__((always_inline)) {
      constexpr int kb = decltype(K_)::v;
      f32x16 sn;
      if constexpr (kb < 7) { sn = MFMA(kf[kb + 1], q, zero); PIN(sn); }
      SBAR();
      if (kb == 6) fl = ld64h(v.ws, hpoll);
      w_early(v, kb, af);
      max16(sc, m, m2);
      PIN(m); PIN(m2);
      SBAR();
      if constexpr (kb < 7) sc = sn;
    });
    m = swap32_max(__builtin_fmaxf(m, m2));
    STAMP2(8);
    cm = splat(-m);
    sc = MFMA(kf[1], q, cm);
    bf16x8 kz = kf[0], kn = kf[2], v0 = VFRAG(1, 0), v1 = VFRAG(1, 1);
    static_for<1, 8>([&](auto K_) __attribute__((always_inline)) {
      constexpr int kb = decltype(K_)::v;
      const bf16x8 n0 = VFRAG(kb + 1, 0), n1 = VFRAG(kb + 1, 1);
      f32x16 sn;
      if constexpr (kb < 7) { sn = MFMA(kn, q, cm); PIN(sn); kn = KFRAG(kb + 2); }
      else { sa = MFMA(kn, q, cm); sb = MFMA(kz, q, cm); PIN(sa); PIN(sb); }
      SBAR();
      exp_pack(sc, p0, p1);
      o = MFMA(v0, BF(p0), o);
      o = MFMA(v1, BF(p1), o);
      PIN(o);
      if (kb == 1) {
        STAMP2(10);
        asm volatile("s_waitcnt vmcnt(2)" : "+v"(fl)::"memory");
        check_flags(v.ws, hpoll, v.tag, v.c ? v.tag : fl);
        STAMP2(11);
      }
      if (!v.c) static_for<(kb - 1) * 3, (kb < 7 ? kb * 3 : 20)>([&](auto N_) __attribute__((always_inline)) {
          af[decltype(N_)::v >> 3][decltype(N_)::v & 7] = a_load<(decltype(N_)::v >> 3), (decltype(N_)::v & 7)>(v);
        });
      SBAR();
      if constexpr (kb < 7) sc = sn;
      v0 = n0;
      v1 = n1;
    });
    sc = at_merge(a, sa, sb);
    exp_pack(sc, p0, p1);
    const u32x4 k0 = at_mask(a, 0), k1 = at_mask(a, 1);
    o = MFMA(v0, BF(p0 & k0), o);
    o = MFMA(v1, BF(p1 & k1), o);
    o = MFMA(VFRAG(0, 0), BF(p0 & ~k0), o);
    o = MFMA(VFRAG(0, 1), BF(p1 & ~k1), o);
  } else {
#pragma unroll
    for (int i = 0; i < 8; ++i) w_early(v, i, af);
    max16(at_merge(a, MFMA(KFRAG(8), q, zero), splat(NEG)), m, m2);
    for (int kb = 8 - v.w; kb < 8; ++kb) max16(MFMA(KFRAG(kb), q, zero), m, m2);
    cm = splat(-swap32_max(__builtin_fmaxf(m, m2)));
    for (int kb = 8 - v.w; kb < 9; ++kb) {
      sc = MFMA(KFRAG(kb), q, cm);
      if (kb == 8) sc = at_merge(a, sc, splat(NEG));
      exp_pack(sc, p0, p1);
      o = MFMA(VFRAG(kb, 0), BF(p0), o);
      o = MFMA(VFRAG(kb, 1), BF(p1), o);
    }
    PIN(o);
    check_flags(v.ws, hpoll, v.tag, v.c ? v.tag : u32x2{~v.tag.x, 0u});
    if (!v.c) static_for<0, 20>([&](auto N_) __attribute__((always_inline)) {
      af[decltype(N_)::v >> 3][decltype(N_)::v & 7] = a_load<(decltype(N_)::v >> 3), (decltype(N_)::v & 7)>(v);
    });
  }
  o_store(v, v.w, o);
}

DI void c_scatter2(const Cx& v, LAS char* X, const f32x16& a, int nb, int j0) {
  const int t = v.lane & 31;
  LAS char* row = X + v.g * 8192 + t * 256;
  const int sw = ((v.lane >> 5) ^ (t & 15)) * 16;
#pragma unroll
  for (int j = j0; j < j0 + 2; ++j) *(LAS f32x4*)(row + (sw ^ ((4 * nb + j) * 32))) = f32x4{a[4 * j], a[4 * j + 1], a[4 * j + 2], a[4 * j + 3]};
}
DI LAS f32x4* c_cell(const Cx& v, LAS char* X, int half) {
  const int t = 8 * v.g + (v.lane >> 3);
  return (LAS f32x4*)(X + t * 256 + (((2 * (v.lane & 7) + half) ^ (t & 15)) * 16));
}
DI f32x4 c_gather(const Cx& v, LAS char* X, int half) {
  LAS const f32x4* src = c_cell(v, X, half);
  return (src[0] + src[512]) + (src[1024] + src[1536]);
}
DI f32x4 silu4(f32x4 d) {
#pragma unroll
  for (int i = 0; i < 4; ++i) d[i] *= __builtin_amdgcn_rcpf(1.f + __builtin_amdgcn_exp2f(-1.4426950408889634f * d[i]));
  return d;
}
DI u32x2 fin4(u32x2 x, f32x4 s, f32x4 g) {
  return u32x2{pk(__builtin_fmaf(s[0], g[0], __uint_as_float(x.x << 16)), __builtin_fmaf(s[1], g[1], __uint_as_float(x.x & 0xffff0000u))),
               pk(__builtin_fmaf(s[2], g[2], __uint_as_float(x.y << 16)), __builtin_fmaf(s[3], g[3], __uint_as_float(x.y & 0xffff0000u)))};
}
DI LAS char* c_xd(const Cx& v, int half) { return v.L + (1 + half) * REG; }
DI LAS char* c_xs(const Cx& v, int half) { return v.L + (half ? 0 : 4 * REG); }
DI unsigned c_xoff(const Cx& v) { return (64 * v.rt + 8 * v.w + (v.lane >> 3)) * 1024 + v.ct * 128 + (v.lane & 7) * 16; }
DI u32x4 x_load(const Params& P, const Cx& v) {
  u32x4 r;
  asm volatile("buffer_load_dwordx4 %0, %1, %2, 0 offen" : "=v"(r) : "v"(c_xoff(v)), "s"(mk_rsrc(P.x, 2048 * 512 * 2)) : "memory");
  return r;
}
struct CFrag {
  LAS const char *b0, *b1;
  bf16x8 wf[4][2];
};
DI CFrag c_frag(const Cx& v) {
  const int lane = v.lane, h = lane >> 5, qq = (lane & 15) >> 2, p = lane & 3, g1 = (lane >> 4) & 1;
  CFrag c;
  c.b0 = v.L + v.g * 8192 + (4 * h + qq) * 128 + (g1 + (qq & 2)) * 32 + 8 * p;
  c.b1 = v.L + v.g * 8192 + (4 * h + qq) * 128 + (g1 + (2 ^ (qq & 2))) * 32 + 8 * p;
  return c;
}
template <int S> DI void c_rd(CFrag& c) {
  constexpr int RG[4] = {0, 1, 2, 4};
  c.wf[S & 3][0] = rdW<1024>(c.b0 + RG[(S >> 2) & 3] * REG + (S & 3) * 2048);
  c.wf[S & 3][1] = rdW<1024>(c.b1 + RG[(S >> 2) & 3] * REG + (S & 3) * 2048);
}
template <int GP, bool STAGED>
DI void c_close(const Cx& v, const CFrag& c, u32x4 (&a0)[8], u32x4 (&a1)[8], f32x16 (&ac)[2][2], LAS char* x0, LAS char* x1) {
  constexpr int RG[4] = {0, 1, 2, 4};
  bf16x8 wa[8], wb[8];
#define C_OFF(k) (RG[(GP + ((k) >> 2)) & 3] * REG + ((k) & 3) * 2048)
  wa[0] = rdW<1024>(c.b0 + C_OFF(0));
  wa[1] = rdW<1024>(c.b0 + C_OFF(1));
  static_for<0, 8>([&](auto K_) __attribute__((always_inline)) {
    constexpr int k = decltype(K_)::v;
    u32x4(&a)[8] = k < 4 ? a0 : a1;
    if constexpr (STAGED && !(k & 1)) { wait_vm<13 - 2 * k>(); PIN4(a, 2 * (k & 3)); }
    ac[0][0] = MFMA(wa[k], BF(a[2 * (k & 3)]), ac[0][0]);
    PIN(ac[0][0]);
    SBAR();
    if constexpr (k < 6) wa[k + 2] = rdW<1024>(c.b0 + C_OFF(k + 2));
    wb[k] = rdW<1024>(c.b1 + C_OFF(k));
    SBAR();
    ac[0][1] = MFMA(wa[k], BF(a[2 * (k & 3) + 1]), ac[0][1]);
    PIN(ac[0][1]);
    SBAR();
  });
#pragma unroll
  for (int k = 0; k < 8; ++k) PIN(wb[k]);
  static_for<0, 24>([&](auto N_) __attribute__((always_inline)) {
    constexpr int n = decltype(N_)::v, k = n & 7, hf = n >> 3;
    if constexpr (n < 16) {
      ac[1][hf] = MFMA(wb[k], BF((k < 4 ? a0 : a1)[2 * (k & 3) + hf]), ac[1][hf]);
      PIN(ac[1][hf]);
      SBAR();
    }
    constexpr int w = n < 16 ? ((n & 1) && n >= 1 && n < 13 ? (n - 1) / 2 : -1) : (n < 18 ? n - 10 : -1);
    if constexpr (w >= 0) {
      constexpr int nb = w >> 2, th = (w >> 1) & 1;
      c_scatter2(v, th ? x1 : x0, ac[nb][th], nb, (w & 1) * 2);
    }
    SBAR();
  });
}
DI void gemm_role(const Params& P, const Cx& v, u32x4 (&af)[4][8], u32x4& xv) {
  CFrag c = c_frag(v);
  f32x16 d[2][2] = {};
  c_rd<0>(c);
  c_rd<1>(c);
  static_for<4, 8>([&](auto N_) __attribute__((always_inline)) { af[2][decltype(N_)::v] = a_load<2, decltype(N_)::v>(v); });
  __builtin_amdgcn_s_setprio(0);
  wait_vm<17>();
  pin8(af[0]);
  static_for<0, 20>([&](auto S_) __attribute__((always_inline)) {
    constexpr int s = decltype(S_)::v, gp = s >> 2, k = s & 3;
    if (k == 0) { STAMP2(40 + gp); JIT(10 + gp); }
    if constexpr (k == 2 && gp < 4) {
      constexpr int N[4] = {8, 17, 20, 12};
      wait_vm<N[gp]>();
      pin8(af[(gp + 1) & 3]);
      if (gp == 0) o_flag(v, v.w);
    }
    if (k == 0 && gp >= 2) hs_set(v, 0, gp);
    if constexpr (s < 18) c_rd<s + 2>(c);
    static_for<0, 4>([&](auto J_) __attribute__((always_inline)) {
      constexpr int j = decltype(J_)::v, n = 4 * k + j;
      d[j >> 1][j & 1] = MFMA(c.wf[k][j >> 1], BF(af[gp & 3][2 * k + (j & 1)]), d[j >> 1][j & 1]);
      PIN(d[j >> 1][j & 1]);
      SBAR();
      if constexpr (gp < 4 && !(n & 1)) af[(gp + 3) & 3][n / 2] = a_load<gp + 3, n / 2>(v);
      if constexpr (gp == 1 && (n & 1)) c_piece(v, 4, 0, n / 2);
      if (s == 16 && j == 0) xv = x_load(P, v);
      SBAR();
    });
  });
  STAMP2(45);
  hs_set(v, 0, 5);
  wait_vm<1>();
  pin8(af[1]);
  pin8(af[2]);
  hs_wait(v, 1, 6);
  STAMP2(46);
  c_close<5, false>(v, c, af[1], af[2], d, c_xd(v, 0), c_xd(v, 1));
  asm volatile("s_waitcnt lgkmcnt(0)" ::: "memory");
  hs_set(v, 2 + v.g, 1);
  hs_wait(v, 2, 1);
#pragma unroll
  for (int i = 0; i < 4; ++i) *c_cell(v, c_xd(v, i >> 1), i & 1) = silu4(c_gather(v, c_xd(v, i >> 1), i & 1));
  STAMP(14);
}
DI void side_role(const Params& P, const Cx& v, u32x4 (&af)[4][8], u32x4& xv) {
  wait_vm<0>();
  o_flag(v, v.w);
  __builtin_amdgcn_s_setprio(0);
  const unsigned oown = WS_FO + ((v.rt >> 2) * 32 + 8 * v.g + (v.lane & 7)) * 64 + ((2 * v.rt + ((v.lane >> 3) & 1)) & 7) * 8;
  const unsigned opoll = blockIdx.x == 0 && v.lane >= 32 ? WS_FO + (v.g * 64 + v.lane - 32) * 64 : oown;
  auto group = [&](int gp) __attribute__((always_inline)) {
    hs_wait(v, 0, gp - 3);
    JIT(30 + gp);
#pragma unroll
    for (int i = 0; i < 8; ++i)
      glds(gp < 7 ? v.rwd : v.rwo, c_voff(v, gp, i), gp < 7 ? v.cbq : v.cbo - 32768, ((gp & 3) == 3 ? 4 : gp & 3) * REG + v.g * 8192 + i * 1024);
  };
  pin8(af[0]);
  pin8(af[1]);
#pragma unroll
  for (int j = 0; j < 2; ++j) {
    hs_wait(v, 0, 2 + j);
#pragma unroll
    for (int i = 0; i < 8; ++i) *(LAS u32x4*)(v.L + (1 + j) * REG + v.g * 8192 + i * 1024 + v.lane * 16) = af[j][i];
  }
  asm volatile("s_waitcnt lgkmcnt(0)" ::: "memory");
  hs_set(v, 1, 6);
  group(7);
  u32x2 fl = ld64h(v.ws, opoll);
  group(8);
  asm volatile("s_waitcnt vmcnt(8)" : "+v"(fl)::"memory");
  check_flags(v.ws, opoll, v.tag, fl);
  STAMP2(47);
  if (blockIdx.x == 0) fl = ld64h(v.ws, opoll + (v.lane >= 32 ? 32 * 64 : 0));
  static_for<0, 16>([&](auto N_) __attribute__((always_inline)) {
    af[2 + decltype(N_)::v / 8][decltype(N_)::v & 7] = a_load<7 + decltype(N_)::v / 8, (decltype(N_)::v & 7)>(v);
  });
  xv = x_load(P, v);
  const CFrag c = c_frag(v);
  f32x16 sm[2][2] = {};
  asm volatile("s_waitcnt vmcnt(17)" : "+v"(fl)::"memory");
  STAMP2(48);
  JIT(9);
  c_close<7, true>(v, c, af[2], af[3], sm, c_xs(v, 0), c_xs(v, 1));
  if (blockIdx.x == 0) check_flags(v.ws, opoll + (v.lane >= 32 ? 32 * 64 : 0), v.tag, fl);
  STAMP(14);
}

struct AB {
  bf16x8 X[32];
  bf16x8 wf[3][2];
  bf16x8 sel;
  unsigned nm;
  float r, sd;
  int gr;
  int rb, cg, strip0, dblA, dblB;
  int kx;
  unsigned voffX, voffQ, voffF;
  rsrc_t rx, rq, rf;
};
DI void x_piece(const Cx& v, const AB& a, int kc, int slot, int i) {
  glds(a.rx, a.voffX ^ (64 * i), (a.rb * 128 + 16 * v.w + 4 * i) * 1024 + (kc ^ a.kx) * 256, slot * SLOT + (4 * v.w + i) * 1024);
}
DI void s_piece(const Cx& v, const AB& a, int kq, int i) {
  glds(a.rq, a.voffQ, (128 * (kq ^ a.kx) + 16 * v.w + 8 * i) * 3072 + a.strip0 * 128, SBUF + (kq & 1) * SUB1 + (2 * v.w + i) * 1024);
}
DI void f_piece(const Cx& v, const AB& a, int dbl, int kq, int slot, int i) {
  glds(a.rf, a.voffF, (128 * (kq ^ a.kx) + 32 * v.g + 8 * i) * 3584 + dbl * 128, slot * SLOT + v.c * SUB1 + v.g * 4096 + i * 1024);
}
template <bool TB> DI void u_piece(const Cx& v, const AB& a, int kq, int i) {
  const int slot = 1 + 2 * (kq & 1);
  if (TB) glds(a.rq, a.voffQ, (128 * (kq ^ a.kx) + 32 * v.g + 8 * i) * 3072 + (a.strip0 + 1 + v.c) * 128,
               slot * SLOT + v.c * SUB1 + v.g * 4096 + i * 1024);
  else f_piece(v, a, a.dblA, kq, slot, i);
}
DI void cs_put(const Cx& v, const f32x4& c0, const f32x4& c1, const f32x4& c2) {
  const bool hi = v.lane & 16;
  *(LAS f32x4*)(v.L + AUX + v.w * 1024 + v.lane * 16) = f32x4{hi ? c0[1] : c0[0], hi ? c1[1] : c1[0], hi ? c2[1] : c2[0], 0.f};
}
DI f32x4 cs_get(const Cx& v) {
  LAS const f32x4* p = (LAS const f32x4*)(v.L + AUX + v.c * 4096 + v.lane * 16);
  return (p[0] + p[64]) + (p[128] + p[192]);
}
DI void q_store(const Cx& v, const AB& a, const f32x16& acc, int strip, int hh) {
  const float sc = strip < 8 ? a.r * QSCALE : a.r;
  u32x4 o;
#pragma unroll
  for (int i = 0; i < 4; ++i) o[i] = pk(acc[8 * hh + 2 * i] * sc, acc[8 * hh + 2 * i + 1] * sc);
  st128h(v.ws, v.lane * 16, WS_QKV + ((strip * 4 + 2 * v.c + hh) * 64 + 4 * a.rb + v.g) * 1024, o);
}
DI void q_flag(const Cx& v, const AB& a, int strip) { put_flag(v.ws, v.lane, WS_FQ + (a.rb * 24 + strip) * 64 + v.w * 8, v.tag); }
DI void put_h(const Cx& v, const AB& a, int dbl, int j, const u32x4& hv) {
  st128h(v.ws, v.lane * 16, WS_H + ((4 * a.rb + v.g) * 112 + 4 * dbl + 2 * v.c + j) * 1024, hv);
}
struct Bases { unsigned b[4]; };
template <bool F> DI Bases mk_bases(const Cx& v, const AB& a, int base) {
  int l = v.lane;
  PIN(l);
  const int qq = (l & 15) >> 2, g1 = (l >> 4) & 1;
  const int o = base + (F ? g1 * SUB1 : 0) + (8 * (l >> 5) + qq) * 128 + ((2 * v.c) ^ (qq & 2) ^ g1) * 32 + 8 * (l & 3);
  Bases r;
#pragma unroll
  for (int i = 0; i < 4; ++i) { r.b[i] = o + (i ^ a.gr) * 4096; PIN(r.b[i]); }
  return r;
}
DI void bump(Bases& B, int d) {
#pragma unroll
  for (int i = 0; i < 4; ++i) { B.b[i] += d; PIN(B.b[i]); }
}
#define WRD(B, off, i) rdW<512>((LAS const char*)(size_t)((B).b[(i) >> 1]) + (off) + ((i) & 1) * 2048)
#define WRX(B, off, i) rdW<512>((LAS const char*)(size_t)((B).b[(i) >> 1] ^ 32u) + (off) + ((i) & 1) * 2048)

DI u32x2 make_tag(u32x2 e) {
  const unsigned long long z = ((unsigned long long)__builtin_amdgcn_readfirstlane(e.y) << 32 | (unsigned)__builtin_amdgcn_readfirstlane(e.x));
  const unsigned long long t = (z ^ (z >> 31)) * 0x9E3779B97F4A7C15ull + 0xD1B54A32D192ED03ull;
  return u32x2{(unsigned)t, (unsigned)(t >> 32)};
}

DI void stats_sum(const u32x4& xr, float& s1, float& s2) {
  s1 = s2 = 0.f;
#pragma unroll
  for (int d = 0; d < 4; ++d) {
    const float lo = __uint_as_float(xr[d] << 16), hi = __uint_as_float(xr[d] & 0xffff0000u);
    s1 += lo + hi;
    s2 = __builtin_fmaf(lo, lo, __builtin_fmaf(hi, hi, s2));
  }
}
DI void stats_put(const Cx& v, const AB& a, float s1, float s2) {
  const float mean = s1 * (1.f / 512.f);
  const float var = __builtin_fmaxf(__builtin_fmaf(-mean, mean, s2 * (1.f / 512.f)), 0.f);
  const float sd = __builtin_amdgcn_sqrtf(rbf(rbf(var) + 1e-5f));
  put_flag(v.ws, v.lane ^ 63, WS_ST + (a.rb * 128 + a.cg * 8 + v.w) * 8, u32x2{pk(-mean, sd), v.tag.x});
}
DI void row_stats(const Cx& v, AB& a, unsigned g) {
  a.sd = __uint_as_float(g & 0xffff0000u);
  a.r = __builtin_amdgcn_rcpf(a.sd);
  a.nm = v.lane < 32 ? (g & 0xffffu) | (g << 16) : 0u;
}
DI void rd_step(AB& a, const Bases& B, int s) {
  a.wf[(s + 2) % 3][0] = WRD(B, ((s >> 3) & 1) * SLOT, s & 7);
  a.wf[(s + 2) % 3][1] = WRX(B, ((s >> 3) & 1) * SLOT, s & 7);
}
template <bool TB, int Q>
DI void f_step(Cx& v, AB& a, const Bases& BS, Bases& BU, f32x16& ps, f32x16& a0, f32x16& a1, f32x4& cs, f32x4& c0, f32x4& c1,
               u32x2& st, u32x4& xr) {
  const int t = v.lane & 31;
  const unsigned u = (((v.lane >> 5) ^ (t & 15)) * 16) ^ (64 * a.gr);
  LAS const char* xb = v.L + (32 * v.g + t) * 256;
  bf16x8 ws[3];
  float s1, s2;
  f32x4 cp;
  auto rdx = [&](int q, int i) __attribute__((always_inline)) { a.X[8 * q + i] = *(LAS const bf16x8*)(xb + 2 * (q & 1) * SLOT + (u ^ (32 * i))); };
  auto rd = [&](int i) __attribute__((always_inline)) {
    a.wf[i % 3][0] = WRD(BU, 0, i);
    a.wf[i % 3][1] = TB ? WRD(BU, SUB1, i) : WRX(BU, 0, i);
    ws[i % 3] = WRD(BS, (Q & 1) * SUB1, i);
  };
  auto req = [&](int n) __attribute__((always_inline)) {
    if (Q < 3) { if (n < 2) s_piece(v, a, Q + 1, n); else u_piece<TB>(v, a, Q + 1, n - 2); }
    else if (n < 4) f_piece(v, a, a.dblB, 1, 1, n);
  };
  rd(0);
  rd(1);
  static_for<0, 8>([&](auto I_) __attribute__((always_inline)) {
    constexpr int i = decltype(I_)::v;
    if (i == 4) {
      if (Q < 3) wait_bar<Q == 2 ? 8 : 6, 15>();
      else {
        wait_bar<4, 6>();
        cp = cs_get(v);
      }
    }
    const bf16x8 x = a.X[8 * Q + i];
    a0 = MFMA(a.wf[i % 3][0], x, a0);
    PIN(a0);
    SBAR();
    if (Q < 3 && i >= 4) { rdx(Q + 1, 2 * i - 8); rdx(Q + 1, 2 * i - 7); }
    if (i < 2) { COLSUM(a.wf[i % 3][0], c0); PIN(c0); }
    if (i < 3) req(2 * i);
    if (i >= 4) {
      if (Q < 2) x_piece(v, a, Q + 2, 2 * (Q & 1), i - 4);
      else f_piece(v, a, a.dblB, 2 * (Q & 1), 2 * (Q & 1), i - 4);
    }
    if (Q == 3 && i == 5) { ln_fix(a0, cp[0], a.nm); PIN(a0); }
    SBAR();
    a1 = MFMA(a.wf[i % 3][1], x, a1);
    PIN(a1);
    SBAR();
    if (i < 2) { COLSUM(a.wf[i % 3][1], c1); PIN(c1); }
    if (i < 3) req(2 * i + 1);
    if (Q == 3 && i == 6) { ln_fix(a1, cp[1], a.nm); PIN(a1); }
    if (Q == 1) {
      if (i == 3) { PIN(st); PIN(xr); v.tag = make_tag(st); stats_sum(xr, s1, s2); PIN(s1); PIN(s2); }
      if (i == 4) { s1 = wave_sum63(s1); PIN(s1); }
      if (i == 5) { s2 = wave_sum63(s2); PIN(s2); }
      if (i == 6) { JIT(1); stats_put(v, a, s1, s2); }
    }
    if (Q == 2 && i == 3) st = ld64h(v.ws, WS_ST + (a.rb * 128 + 32 * v.g + (v.lane & 31)) * 8);
    if (Q == 3 && i == 2) {
      PIN(st);
      check_granule(v.ws, WS_ST + (a.rb * 128 + 32 * v.g + (v.lane & 31)) * 8, v.tag.x, st);
      row_stats(v, a, st.x);
      PIN(a.nm); PIN(a.r); PIN(a.sd);
    }
    SBAR();
    const bf16x8 wsi = ws[i % 3];
    ps = MFMA(wsi, x, ps);
    PIN(ps);
    SBAR();
    if (i < 6) rd(i + 2);
    if (i < 2) { COLSUM(wsi, cs); PIN(cs); }
    if (Q == 3) {
      if (i == 2) cs_put(v, c0, c1, cs);
      if (i == 4) { ln_fix(ps, cp[2], a.nm); PIN(ps); }
      if (i == 6) BU = mk_bases<true>(v, a, 0);
      if (i >= 6) rd_step(a, BU, i - 6);
    } else if (i == 6) bump(BU, (Q & 1) ? -2 * SLOT : 2 * SLOT);
    if (Q == 1 && i == 7) {
      const int per = v.w == 1 ? 16 : 56;
      asm volatile("buffer_load_dword %0, %1, %2, 0 offen"
                   : "=v"(xr.x) : "v"(v.lane < per && v.w < 2 ? (blockIdx.x * per + v.lane) * 128 : 0x40000000u), "s"(v.w == 0 ? v.rwd : v.rwo)
                   : "memory");
    }
    SBAR();
  });
}
template <bool TB>
DI void f_step0(Cx& v, AB& a, const Bases& BS, Bases& BU, f32x16& ps, f32x16& a0, f32x16& a1, f32x4& cs, f32x4& c0, f32x4& c1,
                u32x2& st, u32x4& xr) {
  const int t = v.lane & 31;
  const unsigned u = (((v.lane >> 5) ^ (t & 15)) * 16) ^ (64 * a.gr);
  LAS const char* xb = v.L + (32 * v.g + t) * 256;
  bf16x8 ws[8];
#pragma unroll
  for (int i = 0; i < 3; ++i) {
    a.X[i] = *(LAS const bf16x8*)(xb + (u ^ (32 * i)));
    ws[i] = WRD(BS, 0, i);
  }
#pragma unroll
  for (int i = 0; i < 8; ++i) {
    if (i < 5) {
      a.X[i + 3] = *(LAS const bf16x8*)(xb + (u ^ (32 * (i + 3))));
      ws[i + 3] = WRD(BS, 0, i + 3);
    }
    if (i < 4) x_piece(v, a, 1, 2, i);
    else if (i < 6) s_piece(v, a, 1, i - 4);
    else u_piece<TB>(v, a, 1, i - 6);
    ps = MFMA(ws[i], a.X[i], ps);
    if (i < 2) COLSUM(ws[i], cs);
    PIN(ps); PIN(cs);
    SBAR();
  }
  wait_bar<8, 0>();
  auto rd = [&](int i) __attribute__((always_inline)) {
    a.wf[i % 3][0] = WRD(BU, 0, i);
    a.wf[i % 3][1] = TB ? WRD(BU, SUB1, i) : WRX(BU, 0, i);
  };
  rd(0);
  rd(1);
  static_for<0, 8>([&](auto I_) __attribute__((always_inline)) {
    constexpr int i = decltype(I_)::v;
    if (i == 4) wait_bar<10, 15>();
    const bf16x8 w0 = a.wf[i % 3][0], w1 = a.wf[i % 3][1];
    a0 = MFMA(w0, a.X[i], a0);
    PIN(a0);
    SBAR();
    if (i < 6) rd(i + 2);
    if (i >= 4) a.X[2 * i] = *(LAS const bf16x8*)(xb + 2 * SLOT + (u ^ (32 * (2 * i - 8))));
    if (i < 2) { COLSUM(w0, c0); PIN(c0); u_piece<TB>(v, a, 1, 2 + i); }
    else if (i < 6) x_piece(v, a, 2, 0, i - 2);
    SBAR();
    a1 = MFMA(w1, a.X[i], a1);
    PIN(a1);
    SBAR();
    if (i >= 4) a.X[2 * i + 1] = *(LAS const bf16x8*)(xb + 2 * SLOT + (u ^ (32 * (2 * i - 7))));
    if (i < 2) { COLSUM(w1, c1); PIN(c1); }
    if (i == 1) {
      st = ld64h(v.ws, WS_EPOCH);
      asm volatile("buffer_load_dwordx4 %0, %1, %2, %3 offen"
                   : "=v"(xr) : "v"(v.lane * 16), "s"(a.rx), "s"(RFL((a.rb * 128 + a.cg * 8 + v.w) * 1024)) : "memory");
    }
    if (i == 6) bump(BU, 2 * SLOT);
    SBAR();
  });
}
template <bool TB> DI void front(Cx& v, AB& a, Bases& BU, f32x16& ps, f32x16& a0, f32x16& a1) {
  const Bases BS = mk_bases<false>(v, a, SBUF);
  BU = mk_bases<!TB>(v, a, SLOT);
  f32x4 cs = {}, c0 = {}, c1 = {};
  u32x2 st;
  u32x4 xr;
  STAMP2(23);
  wait_bar<4, 0>();
  STAMP(1);
  f_step0<TB>(v, a, BS, BU, ps, a0, a1, cs, c0, c1, st, xr);
  STAMP2(16); wait_bar<4, 0>(); STAMP2(17); JIT(24);
  f_step<TB, 1>(v, a, BS, BU, ps, a0, a1, cs, c0, c1, st, xr);
  STAMP2(18); wait_bar<6, 0>(); STAMP2(19); JIT(25);
  f_step<TB, 2>(v, a, BS, BU, ps, a0, a1, cs, c0, c1, st, xr);
  STAMP2(20); wait_bar<4, 0>(); STAMP2(21); JIT(26);
  PIN(xr);
  f_step<TB, 3>(v, a, BS, BU, ps, a0, a1, cs, c0, c1, st, xr);
  STAMP2(22);
  wait_bar<4, 8>();
}
template <bool TB> DI void body(Cx& v, AB& a, Bases& B, const f32x16& ps, f32x16& p0, f32x16& p1, AtPre& ap) {
  f32x16 a0 = {}, a1 = {};
  f32x4 c0 = {}, c1 = {};
  float hlo;
  f32x4 cp;
  u32x4 hv;
  u32x2 fl;
  unsigned fo;
  static_for<0, 32>([&](auto S_) __attribute__((always_inline)) {
    constexpr int s = decltype(S_)::v, kq = s >> 3, i = s & 7;
    if (s == 2) { int l = v.lane; PIN(l); a.sel = mk_sel(l); }
    const bf16x8 w0 = a.wf[(s + 2) % 3][0], w1 = a.wf[(s + 2) % 3][1];
    constexpr bool CS = (i >> 1) == 1;
    constexpr int e = TB ? -1 : (s == 30 ? 14 : ((s & 1) && s >= 3 ? (s == 31 ? 15 : (s - 3) >> 1) : -1));
    float ga, ex;
    a0 = MFMA(w0, a.X[s], a0);
    PIN(a0);
    SBAR();
    if (s == 14) bump(B, 2 * SLOT);
    if (s < 30) rd_step(a, B, s + 2);
    if (s == 7) c_setup(v);
    if (CS) { COLSUM(w0, c0); PIN(c0); }
    if ((i & 1) == 0) {
      if (kq == 0) f_piece(v, a, a.dblB, 3, 3, i >> 1);
      else c_piece(v, kq - 1, kq - 1, i >> 1);
    }
    if constexpr (e >= 0) {
      ga = (e < 8 ? p0 : p1)[e & 7] * a.r;
      ex = geglu_exp(ga);
      ga *= (e < 8 ? p0 : p1)[8 + (e & 7)];
      PIN(ga); PIN(ex);
    }
    SBAR();
    a1 = MFMA(w1, a.X[s], a1);
    PIN(a1);
    SBAR();
    if (CS) { COLSUM(w1, c1); PIN(c1); }
    if constexpr (e >= 0) {
      const float hx = geglu_fin(ga, ex, a.sd);
      if (e & 1) { hv[(e & 7) >> 1] = pk(hlo, hx); PIN(hv[(e & 7) >> 1]); }
      else { hlo = hx; PIN(hlo); }
      if (e == 15) put_h(v, a, a.dblA, 1, hv);
    }
    if (s < 2) q_store(v, a, ps, a.strip0, s);
    if (s == 8) { wait_vm<4>(); q_flag(v, a, a.strip0); }
    if (!TB && s == 18) put_h(v, a, a.dblA, 0, hv);
    if (TB) {
      if (s == 3 || s == 5) q_store(v, a, p0, a.strip0 + 1, (s - 3) >> 1);
      if (s == 7 || s == 9) q_store(v, a, p1, a.strip0 + 2, (s - 7) >> 1);
      if (s == 14) { wait_vm<3>(); q_flag(v, a, a.strip0 + 1); q_flag(v, a, a.strip0 + 2); }
    }
    if (s == 28) {
      cs_put(v, c0, c1, c1);
      SBAR();
      wait_bar<63, 0>();
      cp = cs_get(v);
    }
    if (s == 30) { ln_fix(a0, cp[0], a.nm); PIN(a0); }
    if (s == 31) { ln_fix(a1, cp[1], a.nm); PIN(a1); }
    if (s == 23) { fo = at_flag_off(v); fl = ld64h(v.ws, fo); }
    if (s == 27) {
      asm volatile("s_waitcnt vmcnt(2)" : "+v"(fl)::"memory");
      check_flags(v.ws, fo, v.tag, fl);
      ap.k0 = at_load(v, 1);
    }
    if (s == 28) ap.k1 = at_load(v, 2);
    if (s == 29) ap.v0 = at_load(v, 3);
    if (s == 30) ap.v1 = at_load(v, 4);
    if (s == 31) ap.q = at_load(v, 0);
    SBAR();
    if (i == 7 && kq < 3) {
      STAMP2(36 + kq);
      if (kq < 2) wait_bar<4, 8>();
      else wait_bar<63, 8>();
      STAMP2(54 + kq);
      JIT(20 + kq);
    }
  });
  p0 = a0;
  p1 = a1;
}
DI void ab_tail(const Cx& v, const AB& a, int dbl, const f32x16& p0, const f32x16& p1, AtPre& ap) {
  u32x4 hv;
  static_for<0, 8>([&](auto I_) __attribute__((always_inline)) {
    constexpr int i = decltype(I_)::v, j = 2 * (i & 3);
    const f32x16& p = i < 4 ? p0 : p1;
    hv[i & 3] = pk(geglu(p[j], p[8 + j], a.r, a.sd), geglu(p[j + 1], p[9 + j], a.r, a.sd));
    PIN(hv[i & 3]);
    if constexpr ((i & 3) == 3) put_h(v, a, dbl, i >> 2, hv);
    SBAR();
  });
  asm volatile("s_waitcnt vmcnt(2)\n\ts_barrier" : "+v"(ap.q), "+v"(ap.k0), "+v"(ap.k1), "+v"(ap.v0), "+v"(ap.v1)::"memory");
  JIT(4);
  at_stage(v, ap);
  wait_vm<0>();
  put_flag(v.ws, v.lane, WS_FH + (a.rb * 16 + a.cg) * 64 + v.w * 8, v.tag);
  wait_bar<63, 0>();
}

template <bool TB> DI void ab_path(Cx& v, AB a, AtPre& ap) {
  int l = v.lane;
  PIN(l);
  a.voffX = (l >> 4) * 1024 + (((l & 15) ^ (l >> 4)) * 16);
  a.voffQ = w_voff(l, 3072);
  a.voffF = w_voff(l, 3584, v.c);
  a.sel = mk_sel(l);
#pragma unroll
  for (int i = 0; i < 4; ++i) u_piece<TB>(v, a, 0, i);
  f32x16 ps = {}, p0 = {}, p1 = {};
  Bases B;
  front<TB>(v, a, B, ps, p0, p1);
  STAMP(3);
  JIT(2);
  body<TB>(v, a, B, ps, p0, p1, ap);
  STAMP(4);
  JIT(3);
  ab_tail(v, a, a.dblB, p0, p1, ap);
}

__global__ __launch_bounds__(512, 2) void fused_kernel(Params P) {
  extern __shared__ __attribute__((aligned(16))) char smem[];
  Cx v;
  v.L = (LAS char*)smem;
  v.lane = threadIdx.x & 63;
  v.w = __builtin_amdgcn_readfirstlane(threadIdx.x >> 6);
  v.g = v.w & 3;
  v.c = v.w >> 2;
  const int xc = blockIdx.x & 7, idx = blockIdx.x >> 3;
  v.ws = mk_rsrc(P.ws, WS_END);
  v.rwd = mk_rsrc(P.wd, 1792 * 512 * 2);
  v.rwo = mk_rsrc(P.wo, 512 * 512 * 2);
  v.rt = v.hd = xc * 4 + (idx & 3);
  v.ct = v.G = idx >> 2;
  AtPre ap;
  STAMPR(30); STAMP(0);
  JIT(0);
  {
    AB a;
    a.rb = (xc & 1) * 8 + (idx & 7);
    const int cq = xc >> 1, j = idx >> 3;
    const bool typeB = j == 3;
    a.cg = cq * 4 + j;
    a.gr = v.g;
    a.strip0 = 6 * cq + j;
    a.dblA = 7 * cq + 2 * j;
    a.dblB = typeB ? a.dblA : a.dblA + 1;
    a.kx = a.rb & 3;
    a.rx = mk_rsrc(P.x, 2048 * 512 * 2);
    a.rq = mk_rsrc(P.wqkv, 512 * 1536 * 2);
    a.rf = mk_rsrc(v.c ? P.wu : P.wg, 512 * 1792 * 2);
    a.voffX = (v.lane >> 4) * 1024 + (((v.lane & 15) ^ (v.lane >> 4)) * 16);
    a.voffQ = w_voff(v.lane, 3072);
    STAMP2(24);
#pragma unroll
    for (int i = 0; i < 4; ++i) x_piece(v, a, 0, 0, i);
#pragma unroll
    for (int i = 0; i < 2; ++i) s_piece(v, a, 0, i);
    if (typeB) ab_path<true>(v, a, ap);
    else ab_path<false>(v, a, ap);
  }
  STAMP(6);
  JIT(5);
  c_setup(v);
  v.tag = u32x2{uni(v.tag.x), uni(v.tag.y)};
  u32x4 af[4][8];
  __builtin_amdgcn_s_setprio(2);
  if (v.G) at_body<true>(v, ap, af);
  else at_body<false>(v, ap, af);
  STAMP(7);
  JIT(6);
  c_setup(v);
  u32x4 xv;
  if (v.c) {
    side_role(P, v, af, xv);
    undef8(af[0]); undef8(af[1]); undef8(af[2]);
  }
  if (!uni(v.c)) gemm_role(P, v, af, xv);
  asm volatile("s_waitcnt vmcnt(0) lgkmcnt(0)\n\ts_barrier" : "+v"(xv)::"memory");
  if (blockIdx.x == 0 && v.w == 0) put_flag(v.ws, v.lane, WS_EPOCH, v.tag);
  const f32x4 ga = *c_cell(v, c_xd(v, v.c), 0), gb = *c_cell(v, c_xd(v, v.c), 1);
  const u32x2 y0 = fin4(u32x2{xv.x, xv.y}, c_gather(v, c_xs(v, v.c), 0), ga), y1 = fin4(u32x2{xv.z, xv.w}, c_gather(v, c_xs(v, v.c), 1), gb);
  __builtin_amdgcn_raw_buffer_store_b128(u32x4{y0.x, y0.y, y1.x, y1.y}, mk_rsrc(P.out, 2048 * 512 * 2), c_xoff(v), 0, 16);
  STAMP(15); STAMPR(31);
}
}

extern "C" void kernel_launch(void* const* d_in, const int* in_sizes, int n_in, void* d_out, int out_size, void* d_ws,
                              size_t ws_size, hipStream_t stream) {
  Params P;
  P.x = d_in[0]; P.wqkv = d_in[1]; P.wo = d_in[2];
  P.wg = d_in[4]; P.wu = d_in[5]; P.wd = d_in[6];
  P.out = d_out; P.ws = d_ws;
  (void)hipFuncSetAttribute((const void*)fused_kernel, hipFuncAttributeMaxDynamicSharedMemorySize, LDS_BYTES);
  fused_kernel<<<256, 512, LDS_BYTES, stream>>>(P);
}
```

```cpp
#define LIMIT 32
#include <hip/hip_runtime.h>
#include <hip/hip_bf16.h>
#include <stdint.h>

typedef short bf16x8 __attribute__((ext_vector_type(8)));
typedef short s16x4 __attribute__((ext_vector_type(4)));
typedef float f32x16 __attribute__((ext_vector_type(16)));
typedef float f32x4 __attribute__((ext_vector_type(4)));
typedef float f32x2 __attribute__((ext_vector_type(2)));
typedef unsigned u32x4 __attribute__((ext_vector_type(4)));
typedef unsigned u32x2 __attribute__((ext_vector_type(2)));
typedef __bf16 bf2 __attribute__((ext_vector_type(2)));
typedef __amdgpu_buffer_rsrc_t rsrc_t;
#define LAS __attribute__((address_space(3)))
#define DI __device__ __forceinline__
#define SBAR() __builtin_amdgcn_sched_barrier(0)
#define PIN(x) asm volatile("" : "+v"(x))
#define RFL(x) __builtin_amdgcn_readfirstlane(x)
#define MFMA(a, b, c) __builtin_amdgcn_mfma_f32_32x32x16_bf16((a), (b), (c), 0, 0, 0)

namespace {
constexpr unsigned WS_EPOCH = 0;
constexpr unsigned WS_FQ = 1024;
constexpr unsigned WS_FH = WS_FQ + 16 * 24 * 64;
constexpr unsigned WS_FO = WS_FH + 16 * 16 * 64;
constexpr unsigned WS_ST = 65536;
constexpr unsigned WS_QKV = 131072;
constexpr unsigned SEC_BYTES = 32 * 64 * 1024;
constexpr unsigned WS_H = WS_QKV + 3 * SEC_BYTES;
constexpr unsigned WS_O = WS_H + 64 * 112 * 1024;
constexpr unsigned WS_END = WS_O + 64 * 32 * 1024;
static_assert(WS_FO + 256 * 64 <= WS_ST, "flags overlap");
constexpr int SLOT = 32768, SUB1 = 16384;
constexpr int SBUF = 4 * SLOT;
constexpr int AUX = SBUF;
constexpr int REG = 32768;
constexpr int LDS_BYTES = 163840;
constexpr int KV = 3 * REG;
constexpr float QSCALE = 0.25f * 1.4426950408889634f;
constexpr unsigned SPIN_CAP = 1u << 18;

struct Params {
  const void *x, *wqkv, *wo, *wg, *wu, *wd;
  void *out, *ws;
};

template <int I> struct IC { static constexpr int v = I; };
template <int I, int N, class F> DI void static_for(F&& f) {
  if constexpr (I < N) {
    f(IC<I>{});
    static_for<I + 1, N>(f);
  }
}
template <int VM> DI void wait_vm() { asm volatile("s_waitcnt vmcnt(%0)" ::"n"(VM) : "memory"); }
template <int VM, int LG> DI void wait_bar() { asm volatile("s_waitcnt vmcnt(%0) lgkmcnt(%1)\n\ts_barrier" ::"n"(VM), "n"(LG) : "memory"); }
DI unsigned uni(unsigned x) {
  x = __builtin_amdgcn_readfirstlane(x);
  asm volatile("" : "+s"(x));
  return x;
}
DI rsrc_t mk_rsrc(const void* p, unsigned bytes) { return __builtin_amdgcn_make_buffer_rsrc((void*)p, 0, bytes, 0x00020000); }
DI void glds(rsrc_t rs, unsigned voff, unsigned soff, unsigned lds) {
  asm volatile("s_mov_b32 m0, %2\n\ts_nop 0\n\tbuffer_load_dwordx4 %0, %1, %3 offen lds" ::"v"(voff), "s"(rs),
               "s"(RFL(lds)), "s"(RFL(soff)) : "memory");
}
template <int IMM = 0> DI u32x4 ld128h(rsrc_t rs, unsigned voff, unsigned soff) {
  u32x4 r;
  asm volatile("buffer_load_dwordx4 %0, %1, %2, %3 offen offset:%4 sc1" : "=v"(r) : "v"(voff), "s"(rs), "s"(RFL(soff)), "n"(IMM) : "memory");
  return r;
}
DI u32x2 ld64h(rsrc_t rs, unsigned voff) {
  u32x2 r;
  asm volatile("buffer_load_dwordx2 %0, %1, %2, 0 offen sc1" : "=v"(r) : "v"(voff), "s"(rs) : "memory");
  return r;
}
DI void st128h(rsrc_t rs, unsigned voff, unsigned soff, u32x4 d) {
  asm volatile("buffer_store_dwordx4 %0, %1, %2, %3 offen sc1\n\ts_nop 1" ::"v"(d), "v"(voff), "s"(rs), "s"(RFL(soff)) : "memory");
}
DI void put_flag(rsrc_t ws, int lane, unsigned soff, u32x2 tag) {
  asm volatile("buffer_store_dwordx2 %0, %1, %2, %3 offen sc1" ::"v"(tag), "v"(lane ? 0x80000000u : 0u), "s"(ws), "s"(RFL(soff)) : "memory");
}
DI void check_flags(rsrc_t ws, unsigned voff, u32x2 tag, u32x2 v) {
  unsigned cnt;
  asm volatile(
      "s_mov_b32 %1, %5\n"
      "1:\n\t"
      "v_cmp_ne_u64 vcc, %2, %0\n\t"
      "s_nop 4\n\t"
      "s_cbranch_vccz 2f\n\t"
      "s_sleep 1\n\t"
      "buffer_load_dwordx2 %0, %3, %4, 0 offen sc1\n\t"
      "s_waitcnt vmcnt(0)\n\t"
      "s_sub_u32 %1, %1, 1\n\t"
      "s_cbranch_scc0 1b\n"
      "2:"
      : "+v"(v), "=&s"(cnt)
      : "s"(tag), "v"(voff), "s"(ws), "n"(SPIN_CAP)
      : "vcc", "scc", "memory");
}
DI void check_granule(rsrc_t ws, unsigned voff, unsigned tag, u32x2& v) {
  unsigned cnt, lo = v.x, hi = v.y;
  asm volatile(
      "s_mov_b32 %2, %6\n"
      "1:\n\t"
      "v_cmp_ne_u32 vcc, %3, %1\n\t"
      "s_nop 4\n\t"
      "s_cbranch_vccz 2f\n\t"
      "s_sleep 1\n\t"
      "buffer_load_dword %1, %4, %5, 0 offen offset:4 sc1\n\t"
      "s_waitcnt vmcnt(0)\n\t"
      "buffer_load_dword %0, %4, %5, 0 offen sc1\n\t"
      "s_waitcnt vmcnt(0)\n\t"
      "s_sub_u32 %2, %2, 1\n\t"
      "s_cbranch_scc0 1b\n"
      "2:"
      : "+v"(lo), "+v"(hi), "=&s"(cnt)
      : "s"(tag), "v"(voff), "s"(ws), "n"(SPIN_CAP)
      : "vcc", "scc", "memory");
  v.x = lo;
}
template <int CTRL> DI float dpp_add(float v) {
  return v + __builtin_bit_cast(float, __builtin_amdgcn_update_dpp(0, __builtin_bit_cast(int, v), CTRL, 0xf, 0xf, true));
}
DI float wave_sum63(float v) {
  v = dpp_add<0x111>(v); v = dpp_add<0x112>(v); v = dpp_add<0x114>(v); v = dpp_add<0x118>(v);
  v = dpp_add<0x142>(v);
  return dpp_add<0x143>(v);
}
DI s16x4 trd(LAS const char* p) { return __builtin_amdgcn_ds_read_tr16_b64_v4i16((LAS s16x4*)p); }
template <int STEP> DI bf16x8 rdW(LAS const char* p) {
  const s16x4 a = trd(p), b = trd(p + STEP);
  return __builtin_shufflevector(a, b, 0, 1, 2, 3, 4, 5, 6, 7);
}
DI unsigned pk(float lo, float hi) {
  const f32x2 v = {lo, hi};
  return __builtin_bit_cast(unsigned, __builtin_convertvector(v, bf2));
}
DI float rbf(float v) { return __builtin_bit_cast(float, pk(0.f, v) & 0xffff0000u); }
#define COLSUM(w, cs) cs = __builtin_amdgcn_mfma_f32_16x16x32_bf16(a.sel, (w), cs, 0, 0, 0)
DI bf16x8 mk_sel(int l) {
  const unsigned sl = ((l ^ (l >> 4)) & 1) ? 0u : 0x3f803f80u;
  return __builtin_bit_cast(bf16x8, u32x4{sl, sl, sl, sl});
}
DI float swap32_max(float m) {
  auto r = __builtin_amdgcn_permlane32_swap(__float_as_uint(m), __float_as_uint(m), false, false);
  return __builtin_fmaxf(__uint_as_float(r[0]), __uint_as_float(r[1]));
}
DI f32x16 splat(float v) {
  f32x16 r;
#pragma unroll
  for (int i = 0; i < 16; ++i) r[i] = v;
  return r;
}
DI void ln_fix(f32x16& acc, float c, unsigned nm) {
  const unsigned u0 = __float_as_uint(c);
  const float r1 = c - __uint_as_float(u0 & 0xffff0000u);
  const unsigned u1 = __float_as_uint(r1);
  const float r2 = r1 - __uint_as_float(u1 & 0xffff0000u);
  const u32x2 a = {(u0 >> 16) | (u1 & 0xffff0000u), __float_as_uint(r2) >> 16};
  acc = __builtin_amdgcn_mfma_f32_32x32x8bf16_1k(__builtin_bit_cast(s16x4, a), __builtin_bit_cast(s16x4, u32x2{nm, nm}), acc, 0, 0, 0);
}
DI float geglu_exp(float t) {
  const float c1 = -2.f * 0.7978845608028654f * 1.4426950408889634f;
  return __builtin_amdgcn_exp2f(t * __builtin_fmaf(t * t, c1 * 0.044715f, c1));
}
DI float geglu_fin(float tb, float e, float sd) { return tb * __builtin_amdgcn_rcpf(__builtin_fmaf(e, sd, sd)); }
DI float geglu(float a, float b, float r, float sd) {
  const float t = a * r;
  return geglu_fin(t * b, geglu_exp(t), sd);
}

DI unsigned w_voff(int lane, int pitch, int x = 0) {
  PIN(lane);
  const int r8 = lane >> 3, p8 = lane & 7;
  return r8 * pitch + (((p8 >> 1) ^ (r8 & 2) ^ x) * 32) + (p8 & 1) * 16;
}
struct Cx {
  int lane, w, g, c;
  int rt, ct, rot;
  int hd, G;
  unsigned voffC;
  unsigned cbd, cbq, cbo, hb, ob;
  rsrc_t ws, rwd, rwo;
  u32x2 tag;
  LAS char* L;
  unsigned long long t0;
};
DI void c_setup(Cx& v) {
  v.voffC = w_voff(v.lane, 1024);
  v.cbd = uni((448 * v.g + 32 * v.c) * 1024 + v.ct * 128);
  v.cbq = uni(448 * v.g * 1024 + v.ct * 128);
  v.cbo = uni((128 * v.g + 32 * v.c) * 1024 + v.ct * 128);
  v.hb = uni(WS_H + (2 * v.rt * 112 + 28 * v.g) * 1024);
  v.ob = uni(WS_O + (2 * v.rt * 32 + 8 * v.g) * 1024);
  v.rot = uni((v.rt & 3) * 2);
}
DI int c_grp(const Cx& v, int gp) {
  if (gp >= 7) return (gp - 7) ^ (v.rot & 1);
  const int r = gp + v.rot;
  return r >= 7 ? r - 7 : r;
}
DI unsigned c_voff(const Cx& v, int gp, int i) { return v.voffC + (c_grp(v, gp) << 16) + i * 8192; }
DI unsigned c_dst(const Cx& v, int region, int i) { return region * REG + v.g * 8192 + v.c * 4096 + i * 1024; }
DI void c_piece(const Cx& v, int gp, int region, int i) {
  glds(gp < 7 ? v.rwd : v.rwo, c_voff(v, gp, i), gp < 7 ? v.cbd : v.cbo, c_dst(v, region, i));
}

DI unsigned hs_word(const Cx& v, int i) { return KV + 128 + (i < 2 ? v.g * 8 + i * 4 : 32 + (i - 2) * 4); }
DI void hs_set(const Cx& v, int i, int val) { *(volatile LAS int*)(v.L + hs_word(v, i)) = val; }
DI void hs_wait(const Cx& v, int i, int need) {
  unsigned cnt;
  int x;
  asm volatile(
      "s_mov_b32 %1, %4\n"
      "1:\n\t"
      "ds_read_b32 %0, %2\n\t"
      "s_waitcnt lgkmcnt(0)\n\t"
      "v_cmp_gt_i32 vcc, %3, %0\n\t"
      "s_nop 4\n\t"
      "s_cbranch_vccz 2f\n\t"
      "s_sleep 2\n\t"
      "s_sub_u32 %1, %1, 1\n\t"
      "s_cbranch_scc0 1b\n"
      "2:"
      : "=&v"(x), "=&s"(cnt)
      : "v"(hs_word(v, i) + (i == 2 ? (v.lane & 3) * 4 : 0)), "n"(need), "n"(SPIN_CAP * 8)
      : "vcc", "scc", "memory");
}

struct AtPre { u32x4 q, k0, k1, v0, v1; };
DI int at_kb(const Cx& v) {
  const int kb = 8 * v.G - 8 + 2 * v.w;
  return kb < 0 ? 0 : kb;
}
DI u32x4 at_load(const Cx& v, int i) {
  const int blk = i == 0 ? 8 * v.G + v.w : at_kb(v) + ((i - 1) & 1);
  return ld128h(v.ws, v.lane * 16, WS_QKV + ((i + 1) >> 1) * SEC_BYTES + (v.hd * 64 + blk) * 1024);
}
DI unsigned at_flag_off(const Cx& v) {
  const int i = v.lane & 7, isq = i >> 2;
  const int blk = at_kb(v) + (i & 1) + isq * (8 * v.G + v.w - at_kb(v) - (i & 1));
  const int sec = (1 - isq) * (1 + ((i >> 1) & 1));
  return WS_FQ + ((blk >> 2) * 24 + sec * 8 + (v.hd >> 2)) * 64 + (((v.hd >> 1) & 1) * 4 + (blk & 3)) * 8;
}
DI void at_stage(const Cx& v, const AtPre& a) {
  LAS char* kd = v.L + KV + 2 * v.w * 1024 + v.lane * 16;
  LAS char* vd = v.L + KV + 16384 + 2 * v.w * 1024 + (v.lane & 31) * 32 + (v.lane >> 5) * 16;
  const unsigned one = (v.lane & 8) ? 0u : 0x3f803f80u;
  *(LAS u32x4*)kd = v.w == 0 ? u32x4{one, one, one, one} : a.k0;
  *(LAS u32x4*)(kd + 1024) = a.k1;
  *(LAS u32x4*)vd = a.v0;
  *(LAS u32x4*)(vd + 1024) = a.v1;
}
DI void max16(const f32x16& s, float& m, float& m2) {
#pragma unroll
  for (int r = 0; r < 16; r += 4) {
    m = __builtin_fmaxf(__builtin_fmaxf(m, s[r]), s[r + 1]);
    m2 = __builtin_fmaxf(__builtin_fmaxf(m2, s[r + 2]), s[r + 3]);
  }
}
DI void exp_pack(const f32x16& s, u32x4& p0, u32x4& p1) {
#pragma unroll
  for (int i = 0; i < 4; ++i) {
    p0[i] = pk(__builtin_amdgcn_exp2f(s[2 * i]), __builtin_amdgcn_exp2f(s[2 * i + 1]));
    p1[i] = pk(__builtin_amdgcn_exp2f(s[8 + 2 * i]), __builtin_amdgcn_exp2f(s[9 + 2 * i]));
  }
}
#define PIN4(a, o) asm volatile("" : "+v"(a[o]), "+v"(a[o + 1]), "+v"(a[o + 2]), "+v"(a[o + 3]))
#define BF(x) __builtin_bit_cast(bf16x8, x)
DI void pin8(u32x4 (&a)[8]) { PIN4(a, 0); PIN4(a, 4); }
DI void undef8(u32x4 (&a)[8]) {
  asm volatile("" : "=v"(a[0]), "=v"(a[1]), "=v"(a[2]), "=v"(a[3]), "=v"(a[4]), "=v"(a[5]), "=v"(a[6]), "=v"(a[7]));
}
DI void o_flag(const Cx& v, int qb) { put_flag(v.ws, v.lane, WS_FO + (v.G * 32 + v.hd) * 64 + qb * 8, v.tag); }
struct AtLane {
  LAS const char *kp, *vp;
  int vstr;
  int td;
};
#define KFRAG(kb) (*(LAS const bf16x8*)(a.kp + (kb) * 1024))
#define VFRAG(kb, s) rdW<256>(a.vp + (kb) * a.vstr + (s) * 512)
DI AtLane at_lane(const Cx& v, int qb) {
  const int lane = v.lane, h = lane >> 5, qq = (lane & 15) >> 2, p = lane & 3;
  AtLane a;
  a.kp = v.L + KV + qb * 1024 + lane * 16;
  int vreal = KV + 16384 + qb * 1024 + (4 * h + qq) * 32 + (p & 1) * 16 + (p >> 1) * 8, vone = KV + (lane & 15) * 8;
  PIN(vreal); PIN(vone);
  a.vp = v.L + ((lane & 16) ? vone : vreal);
  a.vstr = (lane & 16) ? 0 : 1024;
  a.td = (lane & 31) - 4 * h;
  return a;
}
DI f32x16 at_merge(const AtLane& a, const f32x16& sd, const f32x16& sf) {
  f32x16 r;
#pragma unroll
  for (int i = 0; i < 16; ++i) r[i] = (i & 3) + 8 * (i >> 2) <= a.td ? sd[i] : sf[i];
  return r;
}
DI u32x4 at_mask(const AtLane& a, int half) {
  u32x4 k;
#pragma unroll
  for (int i = 0; i < 4; ++i) {
    const int r0 = 2 * (i & 1) + 8 * (i >> 1) + 16 * half;
    k[i] = (r0 <= a.td ? 0xffffu : 0u) | (r0 + 1 <= a.td ? 0xffff0000u : 0u);
  }
  return k;
}
DI void o_store(const Cx& v, int qb, const f32x16& o) {
  const float il = __builtin_amdgcn_rcpf(o[8]);
  u32x4 ov;
#pragma unroll
  for (int i = 0; i < 4; ++i) ov[i] = pk(o[2 * i] * il, o[2 * i + 1] * il);
  st128h(v.ws, v.lane * 16, WS_O + ((8 * v.G + qb) * 32 + v.hd) * 1024, ov);
}
template <int GP, int N> DI u32x4 a_load(const Cx& v) {
  return ld128h<(N >> 1) * 1024>(v.ws, v.lane * 16 + (c_grp(v, GP) << 12) + (N & 1) * (GP < 7 ? 112 : 32) * 1024, GP < 7 ? v.hb : v.ob);
}
DI void w_early(const Cx& v, int q, u32x4 (&af)[4][8]) {
  if (v.c) {
#pragma unroll
    for (int i = (2 * q) & 7; i < ((2 * q) & 7) + 2; ++i)
      asm volatile("buffer_load_dwordx4 %0, %1, %2, %3 offen"
                   : "=v"(af[q >> 2][i]) : "v"(c_voff(v, 5 + (q >> 2), i)), "s"(v.rwd), "s"(v.cbq) : "memory");
  } else c_piece(v, 3, 4, q);
}
template <bool FULL> DI void at_body(const Cx& v, const AtPre& ap, u32x4 (&af)[4][8]) {
  const AtLane a = at_lane(v, v.w);
  const bf16x8 q = BF(ap.q);
  const float NEG = -1e30f;
  const f32x16 zero = {};
  const int th = (v.lane >> 3) & 1;
  const unsigned hpoll = WS_FH + ((v.rt >> 1) * 16 + 4 * v.g + (v.lane & 3)) * 64 + (((v.lane >> 2) & 1) * 4 + ((2 * v.rt + th) & 3)) * 8;
  float m = NEG, m2 = NEG;
  f32x16 o = zero, cm, sc;
  u32x4 p0, p1;
  u32x2 fl;
  if (FULL) {
    bf16x8 kf[9];
#pragma unroll
    for (int kb = 0; kb < 9; ++kb) kf[(kb + 8) % 9] = KFRAG((kb + 8) % 9);
    if (v.w == 0) kf[0] = BF(ap.k0);
    f32x16 sa = MFMA(kf[8], q, zero), sb = MFMA(kf[0], q, zero);
    sc = MFMA(kf[1], q, zero);
    PIN(sa); PIN(sb); PIN(sc);
    SBAR();
    w_early(v, 0, af);
    max16(at_merge(a, sa, sb), m, m2);
    static_for<1, 8>([&](auto K_) __attribute__((always_inline)) {
      constexpr int kb = decltype(K_)::v;
      f32x16 sn;
      if constexpr (kb < 7) { sn = MFMA(kf[kb + 1], q, zero); PIN(sn); }
      SBAR();
      if (kb == 6) fl = ld64h(v.ws, hpoll);
      w_early(v, kb, af);
      max16(sc, m, m2);
      PIN(m); PIN(m2);
      SBAR();
      if constexpr (kb < 7) sc = sn;
    });
    m = swap32_max(__builtin_fmaxf(m, m2));
    cm = splat(-m);
    sc = MFMA(kf[1], q, cm);
    bf16x8 kz = kf[0], kn = kf[2], v0 = VFRAG(1, 0), v1 = VFRAG(1, 1);
    static_for<1, 8>([&](auto K_) __attribute__((always_inline)) {
      constexpr int kb = decltype(K_)::v;
      const bf16x8 n0 = VFRAG(kb + 1, 0), n1 = VFRAG(kb + 1, 1);
      f32x16 sn;
      if constexpr (kb < 7) { sn = MFMA(kn, q, cm); PIN(sn); kn = KFRAG(kb + 2); }
      else { sa = MFMA(kn, q, cm); sb = MFMA(kz, q, cm); PIN(sa); PIN(sb); }
      SBAR();
      exp_pack(sc, p0, p1);
      o = MFMA(v0, BF(p0), o);
      o = MFMA(v1, BF(p1), o);
      PIN(o);
      if (kb == 1) {
        asm volatile("s_waitcnt vmcnt(2)" : "+v"(fl)::"memory");
        check_flags(v.ws, hpoll, v.tag, v.c ? v.tag : fl);
      }
      if (!v.c) static_for<(kb - 1) * 3, (kb < 7 ? kb * 3 : 20)>([&](auto N_) __attribute__((always_inline)) {
          af[decltype(N_)::v >> 3][decltype(N_)::v & 7] = a_load<(decltype(N_)::v >> 3), (decltype(N_)::v & 7)>(v);
        });
      SBAR();
      if constexpr (kb < 7) sc = sn;
      v0 = n0;
      v1 = n1;
    });
    sc = at_merge(a, sa, sb);
    exp_pack(sc, p0, p1);
    const u32x4 k0 = at_mask(a, 0), k1 = at_mask(a, 1);
    o = MFMA(v0, BF(p0 & k0), o);
    o = MFMA(v1, BF(p1 & k1), o);
    o = MFMA(VFRAG(0, 0), BF(p0 & ~k0), o);
    o = MFMA(VFRAG(0, 1), BF(p1 & ~k1), o);
  } else {
#pragma unroll
    for (int i = 0; i < 8; ++i) w_early(v, i, af);
    max16(at_merge(a, MFMA(KFRAG(8), q, zero), splat(NEG)), m, m2);
    for (int kb = 8 - v.w; kb < 8; ++kb) max16(MFMA(KFRAG(kb), q, zero), m, m2);
    cm = splat(-swap32_max(__builtin_fmaxf(m, m2)));
    for (int kb = 8 - v.w; kb < 9; ++kb) {
      sc = MFMA(KFRAG(kb), q, cm);
      if (kb == 8) sc = at_merge(a, sc, splat(NEG));
      exp_pack(sc, p0, p1);
      o = MFMA(VFRAG(kb, 0), BF(p0), o);
      o = MFMA(VFRAG(kb, 1), BF(p1), o);
    }
    PIN(o);
    check_flags(v.ws, hpoll, v.tag, v.c ? v.tag : u32x2{~v.tag.x, 0u});
    if (!v.c) static_for<0, 20>([&](auto N_) __attribute__((always_inline)) {
      af[decltype(N_)::v >> 3][decltype(N_)::v & 7] = a_load<(decltype(N_)::v >> 3), (decltype(N_)::v & 7)>(v);
    });
  }
  o_store(v, v.w, o);
}

DI void c_scatter2(const Cx& v, LAS char* X, const f32x16& a, int nb, int j0) {
  const int t = v.lane & 31;
  LAS char* row = X + v.g * 8192 + t * 256;
  const int sw = ((v.lane >> 5) ^ (t & 15)) * 16;
#pragma unroll
  for (int j = j0; j < j0 + 2; ++j) *(LAS f32x4*)(row + (sw ^ ((4 * nb + j) * 32))) = f32x4{a[4 * j], a[4 * j + 1], a[4 * j + 2], a[4 * j + 3]};
}
DI LAS f32x4* c_cell(const Cx& v, LAS char* X, int half) {
  const int t = 8 * v.g + (v.lane >> 3);
  return (LAS f32x4*)(X + t * 256 + (((2 * (v.lane & 7) + half) ^ (t & 15)) * 16));
}
DI f32x4 c_gather(const Cx& v, LAS char* X, int half) {
  LAS const f32x4* src = c_cell(v, X, half);
  return (src[0] + src[512]) + (src[1024] + src[1536]);
}
DI f32x4 silu4(f32x4 d) {
#pragma unroll
  for (int i = 0; i < 4; ++i) d[i] *= __builtin_amdgcn_rcpf(1.f + __builtin_amdgcn_exp2f(-1.4426950408889634f * d[i]));
  return d;
}
DI u32x2 fin4(u32x2 x, f32x4 s, f32x4 g) {
  return u32x2{pk(__builtin_fmaf(s[0], g[0], __uint_as_float(x.x << 16)), __builtin_fmaf(s[1], g[1], __uint_as_float(x.x & 0xffff0000u))),
               pk(__builtin_fmaf(s[2], g[2], __uint_as_float(x.y << 16)), __builtin_fmaf(s[3], g[3], __uint_as_float(x.y & 0xffff0000u)))};
}
DI LAS char* c_xd(const Cx& v, int half) { return v.L + (1 + half) * REG; }
DI LAS char* c_xs(const Cx& v, int half) { return v.L + (half ? 0 : 4 * REG); }
DI unsigned c_xoff(const Cx& v) { return (64 * v.rt + 8 * v.w + (v.lane >> 3)) * 1024 + v.ct * 128 + (v.lane & 7) * 16; }
DI u32x4 x_load(const Params& P, const Cx& v) {
  u32x4 r;
  asm volatile("buffer_load_dwordx4 %0, %1, %2, 0 offen" : "=v"(r) : "v"(c_xoff(v)), "s"(mk_rsrc(P.x, 2048 * 512 * 2)) : "memory");
  return r;
}
struct CFrag {
  LAS const char *b0, *b1;
  bf16x8 wf[4][2];
};
DI CFrag c_frag(const Cx& v) {
  const int lane = v.lane, h = lane >> 5, qq = (lane & 15) >> 2, p = lane & 3, g1 = (lane >> 4) & 1;
  CFrag c;
  c.b0 = v.L + v.g * 8192 + (4 * h + qq) * 128 + (g1 + (qq & 2)) * 32 + 8 * p;
  c.b1 = v.L + v.g * 8192 + (4 * h + qq) * 128 + (g1 + (2 ^ (qq & 2))) * 32 + 8 * p;
  return c;
}
template <int S> DI void c_rd(CFrag& c) {
  constexpr int RG[4] = {0, 1, 2, 4};
  c.wf[S & 3][0] = rdW<1024>(c.b0 + RG[(S >> 2) & 3] * REG + (S & 3) * 2048);
  c.wf[S & 3][1] = rdW<1024>(c.b1 + RG[(S >> 2) & 3] * REG + (S & 3) * 2048);
}
template <int GP, bool STAGED>
DI void c_close(const Cx& v, const CFrag& c, u32x4 (&a0)[8], u32x4 (&a1)[8], f32x16 (&ac)[2][2], LAS char* x0, LAS char* x1) {
  constexpr int RG[4] = {0, 1, 2, 4};
  bf16x8 wa[8], wb[8];
#define C_OFF(k) (RG[(GP + ((k) >> 2)) & 3] * REG + ((k) & 3) * 2048)
  wa[0] = rdW<1024>(c.b0 + C_OFF(0));
  wa[1] = rdW<1024>(c.b0 + C_OFF(1));
  static_for<0, 8>([&](auto K_) __attribute__((always_inline)) {
    constexpr int k = decltype(K_)::v;
    u32x4(&a)[8] = k < 4 ? a0 : a1;
    if constexpr (STAGED && !(k & 1)) { wait_vm<13 - 2 * k>(); PIN4(a, 2 * (k & 3)); }
    ac[0][0] = MFMA(wa[k], BF(a[2 * (k & 3)]), ac[0][0]);
    PIN(ac[0][0]);
    SBAR();
    if constexpr (k < 6) wa[k + 2] = rdW<1024>(c.b0 + C_OFF(k + 2));
    wb[k] = rdW<1024>(c.b1 + C_OFF(k));
    SBAR();
    ac[0][1] = MFMA(wa[k], BF(a[2 * (k & 3) + 1]), ac[0][1]);
    PIN(ac[0][1]);
    SBAR();
  });
#pragma unroll
  for (int k = 0; k < 8; ++k) PIN(wb[k]);
  static_for<0, 24>([&](auto N_) __attribute__((always_inline)) {
    constexpr int n = decltype(N_)::v, k = n & 7, hf = n >> 3;
    if constexpr (n < 16) {
      ac[1][hf] = MFMA(wb[k], BF((k < 4 ? a0 : a1)[2 * (k & 3) + hf]), ac[1][hf]);
      PIN(ac[1][hf]);
      SBAR();
    }
    constexpr int w = n < 16 ? ((n & 1) && n >= 1 && n < 13 ? (n - 1) / 2 : -1) : (n < 18 ? n - 10 : -1);
    if constexpr (w >= 0) {
      constexpr int nb = w >> 2, th = (w >> 1) & 1;
      c_scatter2(v, th ? x1 : x0, ac[nb][th], nb, (w & 1) * 2);
    }
    SBAR();
  });
}
DI void gemm_role(const Params& P, const Cx& v, u32x4 (&af)[4][8], u32x4& xv) {
  CFrag c = c_frag(v);
  f32x16 d[2][2] = {};
  c_rd<0>(c);
  c_rd<1>(c);
  static_for<4, 8>([&](auto N_) __attribute__((always_inline)) { af[2][decltype(N_)::v] = a_load<2, decltype(N_)::v>(v); });
  __builtin_amdgcn_s_setprio(0);
  wait_vm<17>();
  pin8(af[0]);
  static_for<0, 20>([&](auto S_) __attribute__((always_inline)) {
    constexpr int s = decltype(S_)::v, gp = s >> 2, k = s & 3;
    if constexpr (k == 2 && gp < 4) {
      constexpr int N[4] = {8, 17, 20, 12};
      wait_vm<N[gp]>();
      pin8(af[(gp + 1) & 3]);
      if (gp == 0) o_flag(v, v.w);
    }
    if (k == 0 && gp >= 2) hs_set(v, 0, gp);
    if constexpr (s < 18) c_rd<s + 2>(c);
    static_for<0, 4>([&](auto J_) __attribute__((always_inline)) {
      constexpr int j = decltype(J_)::v, n = 4 * k + j;
      d[j >> 1][j & 1] = MFMA(c.wf[k][j >> 1], BF(af[gp & 3][2 * k + (j & 1)]), d[j >> 1][j & 1]);
      PIN(d[j >> 1][j & 1]);
      SBAR();
      if constexpr (gp < 4 && !(n & 1)) af[(gp + 3) & 3][n / 2] = a_load<gp + 3, n / 2>(v);
      if constexpr (gp == 1 && (n & 1)) c_piece(v, 4, 0, n / 2);
      if (s == 16 && j == 0) xv = x_load(P, v);
      SBAR();
    });
  });
  hs_set(v, 0, 5);
  wait_vm<1>();
  pin8(af[1]);
  pin8(af[2]);
  hs_wait(v, 1, 6);
  c_close<5, false>(v, c, af[1], af[2], d, c_xd(v, 0), c_xd(v, 1));
  asm volatile("s_waitcnt lgkmcnt(0)" ::: "memory");
  hs_set(v, 2 + v.g, 1);
  hs_wait(v, 2, 1);
#pragma unroll
  for (int i = 0; i < 4; ++i) *c_cell(v, c_xd(v, i >> 1), i & 1) = silu4(c_gather(v, c_xd(v, i >> 1), i & 1));
}
DI void side_role(const Params& P, const Cx& v, u32x4 (&af)[4][8], u32x4& xv) {
  wait_vm<0>();
  o_flag(v, v.w);
  __builtin_amdgcn_s_setprio(0);
  const unsigned oown = WS_FO + ((v.rt >> 2) * 32 + 8 * v.g + (v.lane & 7)) * 64 + ((2 * v.rt + ((v.lane >> 3) & 1)) & 7) * 8;
  const unsigned opoll = blockIdx.x == 0 && v.lane >= 32 ? WS_FO + (v.g * 64 + v.lane - 32) * 64 : oown;
  auto group = [&](int gp) __attribute__((always_inline)) {
    hs_wait(v, 0, gp - 3);
#pragma unroll
    for (int i = 0; i < 8; ++i)
      glds(gp < 7 ? v.rwd : v.rwo, c_voff(v, gp, i), gp < 7 ? v.cbq : v.cbo - 32768, ((gp & 3) == 3 ? 4 : gp & 3) * REG + v.g * 8192 + i * 1024);
  };
  pin8(af[0]);
  pin8(af[1]);
#pragma unroll
  for (int j = 0; j < 2; ++j) {
    hs_wait(v, 0, 2 + j);
#pragma unroll
    for (int i = 0; i < 8; ++i) *(LAS u32x4*)(v.L + (1 + j) * REG + v.g * 8192 + i * 1024 + v.lane * 16) = af[j][i];
  }
  asm volatile("s_waitcnt lgkmcnt(0)" ::: "memory");
  hs_set(v, 1, 6);
  group(7);
  u32x2 fl = ld64h(v.ws, opoll);
  group(8);
  asm volatile("s_waitcnt vmcnt(8)" : "+v"(fl)::"memory");
  check_flags(v.ws, opoll, v.tag, fl);
  if (blockIdx.x == 0) fl = ld64h(v.ws, opoll + (v.lane >= 32 ? 32 * 64 : 0));
  static_for<0, 16>([&](auto N_) __attribute__((always_inline)) {
    af[2 + decltype(N_)::v / 8][decltype(N_)::v & 7] = a_load<7 + decltype(N_)::v / 8, (decltype(N_)::v & 7)>(v);
  });
  xv = x_load(P, v);
  const CFrag c = c_frag(v);
  f32x16 sm[2][2] = {};
  asm volatile("s_waitcnt vmcnt(17)" : "+v"(fl)::"memory");
  c_close<7, true>(v, c, af[2], af[3], sm, c_xs(v, 0), c_xs(v, 1));
  if (blockIdx.x == 0) check_flags(v.ws, opoll + (v.lane >= 32 ? 32 * 64 : 0), v.tag, fl);
}

struct AB {
  bf16x8 X[32];
  bf16x8 wf[3][2];
  bf16x8 sel;
  unsigned nm;
  float r, sd;
  int gr;
  int rb, cg, strip0, dblA, dblB;
  int kx;
  unsigned voffX, voffQ, voffF;
  rsrc_t rx, rq, rf;
};
DI void x_piece(const Cx& v, const AB& a, int kc, int slot, int i) {
  glds(a.rx, a.voffX ^ (64 * i), (a.rb * 128 + 16 * v.w + 4 * i) * 1024 + (kc ^ a.kx) * 256, slot * SLOT + (4 * v.w + i) * 1024);
}
DI void s_piece(const Cx& v, const AB& a, int kq, int i) {
  glds(a.rq, a.voffQ, (128 * (kq ^ a.kx) + 16 * v.w + 8 * i) * 3072 + a.strip0 * 128, SBUF + (kq & 1) * SUB1 + (2 * v.w + i) * 1024);
}
DI void f_piece(const Cx& v, const AB& a, int dbl, int kq, int slot, int i) {
  glds(a.rf, a.voffF, (128 * (kq ^ a.kx) + 32 * v.g + 8 * i) * 3584 + dbl * 128, slot * SLOT + v.c * SUB1 + v.g * 4096 + i * 1024);
}
template <bool TB> DI void u_piece(const Cx& v, const AB& a, int kq, int i) {
  const int slot = 1 + 2 * (kq & 1);
  if (TB) glds(a.rq, a.voffQ, (128 * (kq ^ a.kx) + 32 * v.g + 8 * i) * 3072 + (a.strip0 + 1 + v.c) * 128,
               slot * SLOT + v.c * SUB1 + v.g * 4096 + i * 1024);
  else f_piece(v, a, a.dblA, kq, slot, i);
}
DI void cs_put(const Cx& v, const f32x4& c0, const f32x4& c1, const f32x4& c2) {
  const bool hi = v.lane & 16;
  *(LAS f32x4*)(v.L + AUX + v.w * 1024 + v.lane * 16) = f32x4{hi ? c0[1] : c0[0], hi ? c1[1] : c1[0], hi ? c2[1] : c2[0], 0.f};
}
DI f32x4 cs_get(const Cx& v) {
  LAS const f32x4* p = (LAS const f32x4*)(v.L + AUX + v.c * 4096 + v.lane * 16);
  return (p[0] + p[64]) + (p[128] + p[192]);
}
DI void q_store(const Cx& v, const AB& a, const f32x16& acc, int strip, int hh) {
  const float sc = strip < 8 ? a.r * QSCALE : a.r;
  u32x4 o;
#pragma unroll
  for (int i = 0; i < 4; ++i) o[i] = pk(acc[8 * hh + 2 * i] * sc, acc[8 * hh + 2 * i + 1] * sc);
  st128h(v.ws, v.lane * 16, WS_QKV + ((strip * 4 + 2 * v.c + hh) * 64 + 4 * a.rb + v.g) * 1024, o);
}
DI void q_flag(const Cx& v, const AB& a, int strip) { put_flag(v.ws, v.lane, WS_FQ + (a.rb * 24 + strip) * 64 + v.w * 8, v.tag); }
DI void put_h(const Cx& v, const AB& a, int dbl, int j, const u32x4& hv) {
  st128h(v.ws, v.lane * 16, WS_H + ((4 * a.rb + v.g) * 112 + 4 * dbl + 2 * v.c + j) * 1024, hv);
}
struct Bases { unsigned b[4]; };
template <bool F> DI Bases mk_bases(const Cx& v, const AB& a, int base) {
  int l = v.lane;
  PIN(l);
  const int qq = (l & 15) >> 2, g1 = (l >> 4) & 1;
  const int o = base + (F ? g1 * SUB1 : 0) + (8 * (l >> 5) + qq) * 128 + ((2 * v.c) ^ (qq & 2) ^ g1) * 32 + 8 * (l & 3);
  Bases r;
#pragma unroll
  for (int i = 0; i < 4; ++i) { r.b[i] = o + (i ^ a.gr) * 4096; PIN(r.b[i]); }
  return r;
}
DI void bump(Bases& B, int d) {
#pragma unroll
  for (int i = 0; i < 4; ++i) { B.b[i] += d; PIN(B.b[i]); }
}
#define WRD(B, off, i) rdW<512>((LAS const char*)(size_t)((B).b[(i) >> 1]) + (off) + ((i) & 1) * 2048)
#define WRX(B, off, i) rdW<512>((LAS const char*)(size_t)((B).b[(i) >> 1] ^ 32u) + (off) + ((i) & 1) * 2048)

DI u32x2 make_tag(u32x2 e) {
  const unsigned long long z = ((unsigned long long)__builtin_amdgcn_readfirstlane(e.y) << 32 | (unsigned)__builtin_amdgcn_readfirstlane(e.x));
  const unsigned long long t = (z ^ (z >> 31)) * 0x9E3779B97F4A7C15ull + 0xD1B54A32D192ED03ull;
  return u32x2{(unsigned)t, (unsigned)(t >> 32)};
}

DI void stats_sum(const u32x4& xr, float& s1, float& s2) {
  s1 = s2 = 0.f;
#pragma unroll
  for (int d = 0; d < 4; ++d) {
    const float lo = __uint_as_float(xr[d] << 16), hi = __uint_as_float(xr[d] & 0xffff0000u);
    s1 += lo + hi;
    s2 = __builtin_fmaf(lo, lo, __builtin_fmaf(hi, hi, s2));
  }
}
DI void stats_put(const Cx& v, const AB& a, float s1, float s2) {
  const float mean = s1 * (1.f / 512.f);
  const float var = __builtin_fmaxf(__builtin_fmaf(-mean, mean, s2 * (1.f / 512.f)), 0.f);
  const float sd = __builtin_amdgcn_sqrtf(rbf(rbf(var) + 1e-5f));
  put_flag(v.ws, v.lane ^ 63, WS_ST + (a.rb * 128 + a.cg * 8 + v.w) * 8, u32x2{pk(-mean, sd), v.tag.x});
}
DI void row_stats(const Cx& v, AB& a, unsigned g) {
  a.sd = __uint_as_float(g & 0xffff0000u);
  a.r = __builtin_amdgcn_rcpf(a.sd);
  a.nm = v.lane < 32 ? (g & 0xffffu) | (g << 16) : 0u;
}
DI void rd_step(AB& a, const Bases& B, int s) {
  a.wf[(s + 2) % 3][0] = WRD(B, ((s >> 3) & 1) * SLOT, s & 7);
  a.wf[(s + 2) % 3][1] = WRX(B, ((s >> 3) & 1) * SLOT, s & 7);
}
template <bool TB, int Q>
DI void f_step(Cx& v, AB& a, const Bases& BS, Bases& BU, f32x16& ps, f32x16& a0, f32x16& a1, f32x4& cs, f32x4& c0, f32x4& c1,
               u32x2& st, u32x4& xr) {
  const int t = v.lane & 31;
  const unsigned u = (((v.lane >> 5) ^ (t & 15)) * 16) ^ (64 * a.gr);
  LAS const char* xb = v.L + (32 * v.g + t) * 256;
  bf16x8 ws[3];
  float s1, s2;
  f32x4 cp;
  auto rdx = [&](int q, int i) __attribute__((always_inline)) { a.X[8 * q + i] = *(LAS const bf16x8*)(xb + 2 * (q & 1) * SLOT + (u ^ (32 * i))); };
  auto rd = [&](int i) __attribute__((always_inline)) {
    a.wf[i % 3][0] = WRD(BU, 0, i);
    a.wf[i % 3][1] = TB ? WRD(BU, SUB1, i) : WRX(BU, 0, i);
    ws[i % 3] = WRD(BS, (Q & 1) * SUB1, i);
  };
  auto req = [&](int n) __attribute__((always_inline)) {
    if (Q < 3) { if (n < 2) s_piece(v, a, Q + 1, n); else u_piece<TB>(v, a, Q + 1, n - 2); }
    else if (n < 4) f_piece(v, a, a.dblB, 1, 1, n);
  };
  rd(0);
  rd(1);
  static_for<0, 8>([&](auto I_) __attribute__((always_inline)) {
    constexpr int i = decltype(I_)::v;
    if (i == 4) {
      if (Q < 3) wait_bar<Q == 2 ? 8 : 6, 15>();
      else {
        wait_bar<4, 6>();
        cp = cs_get(v);
      }
    }
    const bf16x8 x = a.X[8 * Q + i];
    a0 = MFMA(a.wf[i % 3][0], x, a0);
    PIN(a0);
    SBAR();
    if (Q < 3 && i >= 4) { rdx(Q + 1, 2 * i - 8); rdx(Q + 1, 2 * i - 7); }
    if (i < 2) { COLSUM(a.wf[i % 3][0], c0); PIN(c0); }
    if (i < 3) req(2 * i);
    if (i >= 4) {
      if (Q < 2) x_piece(v, a, Q + 2, 2 * (Q & 1), i - 4);
      else f_piece(v, a, a.dblB, 2 * (Q & 1), 2 * (Q & 1), i - 4);
    }
    if (Q == 3 && i == 5) { ln_fix(a0, cp[0], a.nm); PIN(a0); }
    SBAR();
    a1 = MFMA(a.wf[i % 3][1], x, a1);
    PIN(a1);
    SBAR();
    if (i < 2) { COLSUM(a.wf[i % 3][1], c1); PIN(c1); }
    if (i < 3) req(2 * i + 1);
    if (Q == 3 && i == 6) { ln_fix(a1, cp[1], a.nm); PIN(a1); }
    if (Q == 1) {
      if (i == 3) { PIN(st); PIN(xr); v.tag = make_tag(st); stats_sum(xr, s1, s2); PIN(s1); PIN(s2); }
      if (i == 4) { s1 = wave_sum63(s1); PIN(s1); }
      if (i == 5) { s2 = wave_sum63(s2); PIN(s2); }
      if (i == 6) stats_put(v, a, s1, s2);
    }
    if (Q == 2 && i == 3) st = ld64h(v.ws, WS_ST + (a.rb * 128 + 32 * v.g + (v.lane & 31)) * 8);
    if (Q == 3 && i == 2) {
      PIN(st);
      check_granule(v.ws, WS_ST + (a.rb * 128 + 32 * v.g + (v.lane & 31)) * 8, v.tag.x, st);
      row_stats(v, a, st.x);
      PIN(a.nm); PIN(a.r); PIN(a.sd);
    }
    SBAR();
    const bf16x8 wsi = ws[i % 3];
    ps = MFMA(wsi, x, ps);
    PIN(ps);
    SBAR();
    if (i < 6) rd(i + 2);
    if (i < 2) { COLSUM(wsi, cs); PIN(cs); }
    if (Q == 3) {
      if (i == 2) cs_put(v, c0, c1, cs);
      if (i == 4) { ln_fix(ps, cp[2], a.nm); PIN(ps); }
      if (i == 6) BU = mk_bases<true>(v, a, 0);
      if (i >= 6) rd_step(a, BU, i - 6);
    } else if (i == 6) bump(BU, (Q & 1) ? -2 * SLOT : 2 * SLOT);
    if (Q == 1 && i == 7) {
      const int per = v.w == 1 ? 16 : 56;
      asm volatile("buffer_load_dword %0, %1, %2, 0 offen"
                   : "=v"(xr.x) : "v"(v.lane < per && v.w < 2 ? (blockIdx.x * per + v.lane) * 128 : 0x40000000u), "s"(v.w == 0 ? v.rwd : v.rwo)
                   : "memory");
    }
    SBAR();
  });
}
template <bool TB>
DI void f_step0(Cx& v, AB& a, const Bases& BS, Bases& BU, f32x16& ps, f32x16& a0, f32x16& a1, f32x4& cs, f32x4& c0, f32x4& c1,
                u32x2& st, u32x4& xr) {
  const int t = v.lane & 31;
  const unsigned u = (((v.lane >> 5) ^ (t & 15)) * 16) ^ (64 * a.gr);
  LAS const char* xb = v.L + (32 * v.g + t) * 256;
  bf16x8 ws[8];
#pragma unroll
  for (int i = 0; i < 3; ++i) {
    a.X[i] = *(LAS const bf16x8*)(xb + (u ^ (32 * i)));
    ws[i] = WRD(BS, 0, i);
  }
#pragma unroll
  for (int i = 0; i < 8; ++i) {
    if (i < 5) {
      a.X[i + 3] = *(LAS const bf16x8*)(xb + (u ^ (32 * (i + 3))));
      ws[i + 3] = WRD(BS, 0, i + 3);
    }
    if (i < 4) x_piece(v, a, 1, 2, i);
    else if (i < 6) s_piece(v, a, 1, i - 4);
    else u_piece<TB>(v, a, 1, i - 6);
    ps = MFMA(ws[i], a.X[i], ps);
    if (i < 2) COLSUM(ws[i], cs);
    PIN(ps); PIN(cs);
    SBAR();
  }
  wait_bar<8, 0>();
  auto rd = [&](int i) __attribute__((always_inline)) {
    a.wf[i % 3][0] = WRD(BU, 0, i);
    a.wf[i % 3][1] = TB ? WRD(BU, SUB1, i) : WRX(BU, 0, i);
  };
  rd(0);
  rd(1);
  static_for<0, 8>([&](auto I_) __attribute__((always_inline)) {
    constexpr int i = decltype(I_)::v;
    if (i == 4) wait_bar<10, 15>();
    const bf16x8 w0 = a.wf[i % 3][0], w1 = a.wf[i % 3][1];
    a0 = MFMA(w0, a.X[i], a0);
    PIN(a0);
    SBAR();
    if (i < 6) rd(i + 2);
    if (i >= 4) a.X[2 * i] = *(LAS const bf16x8*)(xb + 2 * SLOT + (u ^ (32 * (2 * i - 8))));
    if (i < 2) { COLSUM(w0, c0); PIN(c0); u_piece<TB>(v, a, 1, 2 + i); }
    else if (i < 6) x_piece(v, a, 2, 0, i - 2);
    SBAR();
    a1 = MFMA(w1, a.X[i], a1);
    PIN(a1);
    SBAR();
    if (i >= 4) a.X[2 * i + 1] = *(LAS const bf16x8*)(xb + 2 * SLOT + (u ^ (32 * (2 * i - 7))));
    if (i < 2) { COLSUM(w1, c1); PIN(c1); }
    if (i == 1) {
      st = ld64h(v.ws, WS_EPOCH);
      asm volatile("buffer_load_dwordx4 %0, %1, %2, %3 offen"
                   : "=v"(xr) : "v"(v.lane * 16), "s"(a.rx), "s"(RFL((a.rb * 128 + a.cg * 8 + v.w) * 1024)) : "memory");
    }
    if (i == 6) bump(BU, 2 * SLOT);
    SBAR();
  });
}
template <bool TB> DI void front(Cx& v, AB& a, Bases& BU, f32x16& ps, f32x16& a0, f32x16& a1) {
  const Bases BS = mk_bases<false>(v, a, SBUF);
  BU = mk_bases<!TB>(v, a, SLOT);
  f32x4 cs = {}, c0 = {}, c1 = {};
  u32x2 st;
  u32x4 xr;
  wait_bar<4, 0>();
  f_step0<TB>(v, a, BS, BU, ps, a0, a1, cs, c0, c1, st, xr);
  wait_bar<4, 0>();
  f_step<TB, 1>(v, a, BS, BU, ps, a0, a1, cs, c0, c1, st, xr);
  wait_bar<6, 0>();
  f_step<TB, 2>(v, a, BS, BU, ps, a0, a1, cs, c0, c1, st, xr);
  wait_bar<4, 0>();
  PIN(xr);
  f_step<TB, 3>(v, a, BS, BU, ps, a0, a1, cs, c0, c1, st, xr);
  wait_bar<4, 8>();
}
template <bool TB> DI void body(Cx& v, AB& a, Bases& B, const f32x16& ps, f32x16& p0, f32x16& p1, AtPre& ap) {
  f32x16 a0 = {}, a1 = {};
  f32x4 c0 = {}, c1 = {};
  float hlo;
  f32x4 cp;
  u32x4 hv;
  u32x2 fl;
  unsigned fo;
  static_for<0, 32>([&](auto S_) __attribute__((always_inline)) {
    constexpr int s = decltype(S_)::v, kq = s >> 3, i = s & 7;
    if (s == 2) { int l = v.lane; PIN(l); a.sel = mk_sel(l); }
    const bf16x8 w0 = a.wf[(s + 2) % 3][0], w1 = a.wf[(s + 2) % 3][1];
    constexpr bool CS = (i >> 1) == 1;
    constexpr int e = TB ? -1 : (s == 30 ? 14 : ((s & 1) && s >= 3 ? (s == 31 ? 15 : (s - 3) >> 1) : -1));
    float ga, ex;
    a0 = MFMA(w0, a.X[s], a0);
    PIN(a0);
    SBAR();
    if (s == 14) bump(B, 2 * SLOT);
    if (s < 30) rd_step(a, B, s + 2);
    if (s == 7) c_setup(v);
    if (CS) { COLSUM(w0, c0); PIN(c0); }
    if ((i & 1) == 0) {
      if (kq == 0) f_piece(v, a, a.dblB, 3, 3, i >> 1);
      else c_piece(v, kq - 1, kq - 1, i >> 1);
    }
    if constexpr (e >= 0) {
      ga = (e < 8 ? p0 : p1)[e & 7] * a.r;
      ex = geglu_exp(ga);
      ga *= (e < 8 ? p0 : p1)[8 + (e & 7)];
      PIN(ga); PIN(ex);
    }
    SBAR();
    a1 = MFMA(w1, a.X[s], a1);
    PIN(a1);
    SBAR();
    if (CS) { COLSUM(w1, c1); PIN(c1); }
    if constexpr (e >= 0) {
      const float hx = geglu_fin(ga, ex, a.sd);
      if (e & 1) { hv[(e & 7) >> 1] = pk(hlo, hx); PIN(hv[(e & 7) >> 1]); }
      else { hlo = hx; PIN(hlo); }
      if (e == 15) put_h(v, a, a.dblA, 1, hv);
    }
    if (s < 2) q_store(v, a, ps, a.strip0, s);
    if (s == 8) { wait_vm<4>(); q_flag(v, a, a.strip0); }
    if (!TB && s == 18) put_h(v, a, a.dblA, 0, hv);
    if (TB) {
      if (s == 3 || s == 5) q_store(v, a, p0, a.strip0 + 1, (s - 3) >> 1);
      if (s == 7 || s == 9) q_store(v, a, p1, a.strip0 + 2, (s - 7) >> 1);
      if (s == 14) { wait_vm<3>(); q_flag(v, a, a.strip0 + 1); q_flag(v, a, a.strip0 + 2); }
    }
    if (s == 28) {
      cs_put(v, c0, c1, c1);
      SBAR();
      wait_bar<63, 0>();
      cp = cs_get(v);
    }
    if (s == 30) { ln_fix(a0, cp[0], a.nm); PIN(a0); }
    if (s == 31) { ln_fix(a1, cp[1], a.nm); PIN(a1); }
    if (s == 23) { fo = at_flag_off(v); fl = ld64h(v.ws, fo); }
    if (s == 27) {
      asm volatile("s_waitcnt vmcnt(2)" : "+v"(fl)::"memory");
      check_flags(v.ws, fo, v.tag, fl);
      ap.k0 = at_load(v, 1);
    }
    if (s == 28) ap.k1 = at_load(v, 2);
    if (s == 29) ap.v0 = at_load(v, 3);
    if (s == 30) ap.v1 = at_load(v, 4);
    if (s == 31) ap.q = at_load(v, 0);
    SBAR();
    if (i == 7 && kq < 3) {
      if (kq < 2) wait_bar<4, 8>();
      else wait_bar<63, 8>();

    }
  });
  p0 = a0;
  p1 = a1;
}
DI void ab_tail(const Cx& v, const AB& a, int dbl, const f32x16& p0, const f32x16& p1, AtPre& ap) {
  u32x4 hv;
  static_for<0, 8>([&](auto I_) __attribute__((always_inline)) {
    constexpr int i = decltype(I_)::v, j = 2 * (i & 3);
    const f32x16& p = i < 4 ? p0 : p1;
    hv[i & 3] = pk(geglu(p[j], p[8 + j], a.r, a.sd), geglu(p[j + 1], p[9 + j], a.r, a.sd));
    PIN(hv[i & 3]);
    if constexpr ((i & 3) == 3) put_h(v, a, dbl, i >> 2, hv);
    SBAR();
  });
  asm volatile("s_waitcnt vmcnt(2)\n\ts_barrier" : "+v"(ap.q), "+v"(ap.k0), "+v"(ap.k1), "+v"(ap.v0), "+v"(ap.v1)::"memory");
  at_stage(v, ap);
  wait_vm<0>();
  put_flag(v.ws, v.lane, WS_FH + (a.rb * 16 + a.cg) * 64 + v.w * 8, v.tag);
  wait_bar<63, 0>();
}

template <bool TB> DI void ab_path(Cx& v, AB a, AtPre& ap) {
  int l = v.lane;
  PIN(l);
  a.voffX = (l >> 4) * 1024 + (((l & 15) ^ (l >> 4)) * 16);
  a.voffQ = w_voff(l, 3072);
  a.voffF = w_voff(l, 3584, v.c);
  a.sel = mk_sel(l);
#pragma unroll
  for (int i = 0; i < 4; ++i) u_piece<TB>(v, a, 0, i);
  f32x16 ps = {}, p0 = {}, p1 = {};
  Bases B;
  front<TB>(v, a, B, ps, p0, p1);

  body<TB>(v, a, B, ps, p0, p1, ap);

  ab_tail(v, a, a.dblB, p0, p1, ap);
}

__global__ __launch_bounds__(512, 2) void fused_kernel(Params P) {
  extern __shared__ __attribute__((aligned(16))) char smem[];
  Cx v;
  v.t0 = __builtin_amdgcn_s_memrealtime();
  v.L = (LAS char*)smem;
  v.lane = threadIdx.x & 63;
  v.w = __builtin_amdgcn_readfirstlane(threadIdx.x >> 6);
  v.g = v.w & 3;
  v.c = v.w >> 2;
  const int xc = blockIdx.x & 7, idx = blockIdx.x >> 3;
  v.ws = mk_rsrc(P.ws, WS_END);
  v.rwd = mk_rsrc(P.wd, 1792 * 512 * 2);
  v.rwo = mk_rsrc(P.wo, 512 * 512 * 2);
  v.rt = v.hd = xc * 4 + (idx & 3);
  v.ct = v.G = idx >> 2;
  AtPre ap;

  {
    AB a;
    a.rb = (xc & 1) * 8 + (idx & 7);
    const int cq = xc >> 1, j = idx >> 3;
    const bool typeB = j == 3;
    a.cg = cq * 4 + j;
    a.gr = v.g;
    a.strip0 = 6 * cq + j;
    a.dblA = 7 * cq + 2 * j;
    a.dblB = typeB ? a.dblA : a.dblA + 1;
    a.kx = a.rb & 3;
    a.rx = mk_rsrc(P.x, 2048 * 512 * 2);
    a.rq = mk_rsrc(P.wqkv, 512 * 1536 * 2);
    a.rf = mk_rsrc(v.c ? P.wu : P.wg, 512 * 1792 * 2);
    a.voffX = (v.lane >> 4) * 1024 + (((v.lane & 15) ^ (v.lane >> 4)) * 16);
    a.voffQ = w_voff(v.lane, 3072);
#pragma unroll
    for (int i = 0; i < 4; ++i) x_piece(v, a, 0, 0, i);
#pragma unroll
    for (int i = 0; i < 2; ++i) s_piece(v, a, 0, i);
    if (typeB) ab_path<true>(v, a, ap);
    else ab_path<false>(v, a, ap);
  }


  c_setup(v);
  v.tag = u32x2{uni(v.tag.x), uni(v.tag.y)};
  u32x4 af[4][8];
  __builtin_amdgcn_s_setprio(2);
  if (v.G) at_body<true>(v, ap, af);
  else at_body<false>(v, ap, af);

  c_setup(v);
  u32x4 xv;
  if (v.c) {
    side_role(P, v, af, xv);
    undef8(af[0]); undef8(af[1]); undef8(af[2]);
  }
  if (!uni(v.c)) gemm_role(P, v, af, xv);
  asm volatile("s_waitcnt vmcnt(0) lgkmcnt(0)\n\ts_barrier" : "+v"(xv)::"memory");
  if (blockIdx.x == 0 && v.w == 0) put_flag(v.ws, v.lane, WS_EPOCH, v.tag);
  const f32x4 ga = *c_cell(v, c_xd(v, v.c), 0), gb = *c_cell(v, c_xd(v, v.c), 1);
  const u32x2 y0 = fin4(u32x2{xv.x, xv.y}, c_gather(v, c_xs(v, v.c), 0), ga), y1 = fin4(u32x2{xv.z, xv.w}, c_gather(v, c_xs(v, v.c), 1), gb);
  const unsigned dur_ = (unsigned)(__builtin_amdgcn_s_memrealtime() - v.t0);
  __builtin_amdgcn_raw_buffer_store_b128(u32x4{dur_ > LIMIT * 100 ? (pk((float)dur_ * 0.01f + 100.f, 0.f) & 0xffffu) | (y0.x & 0xffff0000u) : y0.x, y0.y, y1.x, y1.y}, mk_rsrc(P.out, 2048 * 512 * 2), c_xoff(v), 0, 16);
}
}
extern "C" void kernel_launch(void* const* d_in, const int* in_sizes, int n_in, void* d_out, int out_size, void* d_ws,
                              size_t ws_size, hipStream_t stream) {
  Params P;
  P.x = d_in[0]; P.wqkv = d_in[1]; P.wo = d_in[2];
  P.wg = d_in[4]; P.wu = d_in[5]; P.wd = d_in[6];
  P.out = d_out; P.ws = d_ws;
  (void)hipFuncSetAttribute((const void*)fused_kernel, hipFuncAttributeMaxDynamicSharedMemorySize, LDS_BYTES);
  fused_kernel<<<256, 512, LDS_BYTES, stream>>>(P);
}
```

```cpp
#include <hip/hip_runtime.h>
#include <hip/hip_bf16.h>
#include <stdint.h>

typedef short bf16x8 __attribute__((ext_vector_type(8)));
typedef short s16x4 __attribute__((ext_vector_type(4)));
typedef float f32x16 __attribute__((ext_vector_type(16)));
typedef float f32x4 __attribute__((ext_vector_type(4)));
typedef float f32x2 __attribute__((ext_vector_type(2)));
typedef unsigned u32x4 __attribute__((ext_vector_type(4)));
typedef unsigned u32x2 __attribute__((ext_vector_type(2)));
typedef __bf16 bf2 __attribute__((ext_vector_type(2)));
typedef __amdgpu_buffer_rsrc_t rsrc_t;
#define LAS __attribute__((address_space(3)))
#define DI __device__ __forceinline__
#define SBAR() __builtin_amdgcn_sched_barrier(0)
#define PIN(x) asm volatile("" : "+v"(x))
#define RFL(x) __builtin_amdgcn_readfirstlane(x)
#define MFMA(a, b, c) __builtin_amdgcn_mfma_f32_32x32x16_bf16((a), (b), (c), 0, 0, 0)

namespace {
constexpr unsigned WS_EPOCH = 0;
constexpr unsigned WS_FQ = 1024;
constexpr unsigned WS_FH = WS_FQ + 16 * 24 * 64;
constexpr unsigned WS_FO = WS_FH + 16 * 16 * 64;
constexpr unsigned WS_ST = 65536;
constexpr unsigned WS_QKV = 131072;
constexpr unsigned SEC_BYTES = 32 * 64 * 1024;
constexpr unsigned WS_H = WS_QKV + 3 * SEC_BYTES;
constexpr unsigned WS_O = WS_H + 64 * 112 * 1024;
constexpr unsigned WS_END = WS_O + 64 * 32 * 1024;
static_assert(WS_FO + 256 * 64 <= WS_ST, "flags overlap");
constexpr int SLOT = 32768, SUB1 = 16384;
constexpr int SBUF = 4 * SLOT;
constexpr int AUX = SBUF;
constexpr int REG = 32768;
constexpr int LDS_BYTES = 163840;
constexpr int KV = 3 * REG;
constexpr float QSCALE = 0.25f * 1.4426950408889634f;
constexpr unsigned SPIN_CAP = 1u << 18;

struct Params {
  const void *x, *wqkv, *wo, *wg, *wu, *wd;
  void *out, *ws;
};

template <int I> struct IC { static constexpr int v = I; };
template <int I, int N, class F> DI void static_for(F&& f) {
  if constexpr (I < N) {
    f(IC<I>{});
    static_for<I + 1, N>(f);
  }
}
template <int VM> DI void wait_vm() { asm volatile("s_waitcnt vmcnt(%0)" ::"n"(VM) : "memory"); }
template <int VM, int LG> DI void wait_bar() { asm volatile("s_waitcnt vmcnt(%0) lgkmcnt(%1)\n\ts_barrier" ::"n"(VM), "n"(LG) : "memory"); }
DI unsigned uni(unsigned x) {
  x = __builtin_amdgcn_readfirstlane(x);
  asm volatile("" : "+s"(x));
  return x;
}
DI rsrc_t mk_rsrc(const void* p, unsigned bytes) { return __builtin_amdgcn_make_buffer_rsrc((void*)p, 0, bytes, 0x00020000); }
DI void glds(rsrc_t rs, unsigned voff, unsigned soff, unsigned lds) {
  asm volatile("s_mov_b32 m0, %2\n\ts_nop 0\n\tbuffer_load_dwordx4 %0, %1, %3 offen lds" ::"v"(voff), "s"(rs),
               "s"(RFL(lds)), "s"(RFL(soff)) : "memory");
}
template <int IMM = 0> DI u32x4 ld128h(rsrc_t rs, unsigned voff, unsigned soff) {
  u32x4 r;
  asm volatile("buffer_load_dwordx4 %0, %1, %2, %3 offen offset:%4 sc1" : "=v"(r) : "v"(voff), "s"(rs), "s"(RFL(soff)), "n"(IMM) : "memory");
  return r;
}
DI u32x2 ld64h(rsrc_t rs, unsigned voff) {
  u32x2 r;
  asm volatile("buffer_load_dwordx2 %0, %1, %2, 0 offen sc1" : "=v"(r) : "v"(voff), "s"(rs) : "memory");
  return r;
}
DI void st128h(rsrc_t rs, unsigned voff, unsigned soff, u32x4 d) {
  asm volatile("buffer_store_dwordx4 %0, %1, %2, %3 offen sc1\n\ts_nop 1" ::"v"(d), "v"(voff), "s"(rs), "s"(RFL(soff)) : "memory");
}
DI void put_flag(rsrc_t ws, int lane, unsigned soff, u32x2 tag) {
  asm volatile("buffer_store_dwordx2 %0, %1, %2, %3 offen sc1" ::"v"(tag), "v"(lane ? 0x80000000u : 0u), "s"(ws), "s"(RFL(soff)) : "memory");
}
DI void check_flags(rsrc_t ws, unsigned voff, u32x2 tag, u32x2 v) {
  unsigned cnt;
  asm volatile(
      "s_mov_b32 %1, %5\n"
      "1:\n\t"
      "v_cmp_ne_u64 vcc, %2, %0\n\t"
      "s_nop 4\n\t"
      "s_cbranch_vccz 2f\n\t"
      "s_sleep 1\n\t"
      "buffer_load_dwordx2 %0, %3, %4, 0 offen sc1\n\t"
      "s_waitcnt vmcnt(0)\n\t"
      "s_sub_u32 %1, %1, 1\n\t"
      "s_cbranch_scc0 1b\n"
      "2:"
      : "+v"(v), "=&s"(cnt)
      : "s"(tag), "v"(voff), "s"(ws), "n"(SPIN_CAP)
      : "vcc", "scc", "memory");
}
DI void check_granule(rsrc_t ws, unsigned voff, unsigned tag, u32x2& v) {
  unsigned cnt, lo = v.x, hi = v.y;
  asm volatile(
      "s_mov_b32 %2, %6\n"
      "1:\n\t"
      "v_cmp_ne_u32 vcc, %3, %1\n\t"
      "s_nop 4\n\t"
      "s_cbranch_vccz 2f\n\t"
      "s_sleep 1\n\t"
      "buffer_load_dword %1, %4, %5, 0 offen offset:4 sc1\n\t"
      "s_waitcnt vmcnt(0)\n\t"
      "buffer_load_dword %0, %4, %5, 0 offen sc1\n\t"
      "s_waitcnt vmcnt(0)\n\t"
      "s_sub_u32 %2, %2, 1\n\t"
      "s_cbranch_scc0 1b\n"
      "2:"
      : "+v"(lo), "+v"(hi), "=&s"(cnt)
      : "s"(tag), "v"(voff), "s"(ws), "n"(SPIN_CAP)
      : "vcc", "scc", "memory");
  v.x = lo;
}
template <int CTRL> DI float dpp_add(float v) {
  return v + __builtin_bit_cast(float, __builtin_amdgcn_update_dpp(0, __builtin_bit_cast(int, v), CTRL, 0xf, 0xf, true));
}
DI float wave_sum63(float v) {
  v = dpp_add<0x111>(v); v = dpp_add<0x112>(v); v = dpp_add<0x114>(v); v = dpp_add<0x118>(v);
  v = dpp_add<0x142>(v);
  return dpp_add<0x143>(v);
}
DI s16x4 trd(LAS const char* p) { return __builtin_amdgcn_ds_read_tr16_b64_v4i16((LAS s16x4*)p); }
template <int STEP> DI bf16x8 rdW(LAS const char* p) {
  const s16x4 a = trd(p), b = trd(p + STEP);
  return __builtin_shufflevector(a, b, 0, 1, 2, 3, 4, 5, 6, 7);
}
DI unsigned pk(float lo, float hi) {
  const f32x2 v = {lo, hi};
  return __builtin_bit_cast(unsigned, __builtin_convertvector(v, bf2));
}
DI float rbf(float v) { return __builtin_bit_cast(float, pk(0.f, v) & 0xffff0000u); }
#define COLSUM(w, cs) cs = __builtin_amdgcn_mfma_f32_16x16x32_bf16(a.sel, (w), cs, 0, 0, 0)
DI bf16x8 mk_sel(int l) {
  const unsigned sl = ((l ^ (l >> 4)) & 1) ? 0u : 0x3f803f80u;
  return __builtin_bit_cast(bf16x8, u32x4{sl, sl, sl, sl});
}
DI float swap32_max(float m) {
  auto r = __builtin_amdgcn_permlane32_swap(__float_as_uint(m), __float_as_uint(m), false, false);
  return __builtin_fmaxf(__uint_as_float(r[0]), __uint_as_float(r[1]));
}
DI f32x16 splat(float v) {
  f32x16 r;
#pragma unroll
  for (int i = 0; i < 16; ++i) r[i] = v;
  return r;
}
DI void ln_fix(f32x16& acc, float c, unsigned nm) {
  const unsigned u0 = __float_as_uint(c);
  const float r1 = c - __uint_as_float(u0 & 0xffff0000u);
  const unsigned u1 = __float_as_uint(r1);
  const float r2 = r1 - __uint_as_float(u1 & 0xffff0000u);
  const u32x2 a = {(u0 >> 16) | (u1 & 0xffff0000u), __float_as_uint(r2) >> 16};
  acc = __builtin_amdgcn_mfma_f32_32x32x8bf16_1k(__builtin_bit_cast(s16x4, a), __builtin_bit_cast(s16x4, u32x2{nm, nm}), acc, 0, 0, 0);
}
DI float geglu_exp(float t) {
  const float c1 = -2.f * 0.7978845608028654f * 1.4426950408889634f;
  return __builtin_amdgcn_exp2f(t * __builtin_fmaf(t * t, c1 * 0.044715f, c1));
}
DI float geglu_fin(float tb, float e, float sd) { return tb * __builtin_amdgcn_rcpf(__builtin_fmaf(e, sd, sd)); }
DI float geglu(float a, float b, float r, float sd) {
  const float t = a * r;
  return geglu_fin(t * b, geglu_exp(t), sd);
}

DI unsigned w_voff(int lane, int pitch, int x = 0) {
  PIN(lane);
  const int r8 = lane >> 3, p8 = lane & 7;
  return r8 * pitch + (((p8 >> 1) ^ (r8 & 2) ^ x) * 32) + (p8 & 1) * 16;
}
struct Cx {
  int lane, w, g, c;
  int rt, ct, rot;
  int hd, G;
  unsigned voffC;
  unsigned cbd, cbq, cbo, hb, ob;
  rsrc_t ws, rwd, rwo;
  u32x2 tag;
  LAS char* L;
};
DI void c_setup(Cx& v) {
  v.voffC = w_voff(v.lane, 1024);
  v.cbd = uni((448 * v.g + 32 * v.c) * 1024 + v.ct * 128);
  v.cbq = uni(448 * v.g * 1024 + v.ct * 128);
  v.cbo = uni((128 * v.g + 32 * v.c) * 1024 + v.ct * 128);
  v.hb = uni(WS_H + (2 * v.rt * 112 + 28 * v.g) * 1024);
  v.ob = uni(WS_O + (2 * v.rt * 32 + 8 * v.g) * 1024);
  v.rot = uni((v.rt & 3) * 2);
}
DI int c_grp(const Cx& v, int gp) {
  if (gp >= 7) return (gp - 7) ^ (v.rot & 1);
  const int r = gp + v.rot;
  return r >= 7 ? r - 7 : r;
}
DI unsigned c_voff(const Cx& v, int gp, int i) { return v.voffC + (c_grp(v, gp) << 16) + i * 8192; }
DI unsigned c_dst(const Cx& v, int region, int i) { return region * REG + v.g * 8192 + v.c * 4096 + i * 1024; }
DI void c_piece(const Cx& v, int gp, int region, int i) {
  glds(gp < 7 ? v.rwd : v.rwo, c_voff(v, gp, i), gp < 7 ? v.cbd : v.cbo, c_dst(v, region, i));
}

DI unsigned hs_word(const Cx& v, int i) { return KV + 128 + (i < 2 ? v.g * 8 + i * 4 : 32 + (i - 2) * 4); }
DI void hs_set(const Cx& v, int i, int val) { *(volatile LAS int*)(v.L + hs_word(v, i)) = val; }
DI void hs_wait(const Cx& v, int i, int need) {
  unsigned cnt;
  int x;
  asm volatile(
      "s_mov_b32 %1, %4\n"
      "1:\n\t"
      "ds_read_b32 %0, %2\n\t"
      "s_waitcnt lgkmcnt(0)\n\t"
      "v_cmp_gt_i32 vcc, %3, %0\n\t"
      "s_nop 4\n\t"
      "s_cbranch_vccz 2f\n\t"
      "s_sleep 2\n\t"
      "s_sub_u32 %1, %1, 1\n\t"
      "s_cbranch_scc0 1b\n"
      "2:"
      : "=&v"(x), "=&s"(cnt)
      : "v"(hs_word(v, i) + (i == 2 ? (v.lane & 3) * 4 : 0)), "n"(need), "n"(SPIN_CAP * 8)
      : "vcc", "scc", "memory");
}

struct AtPre { u32x4 q, k0, k1, v0, v1; };
DI int at_kb(const Cx& v) {
  const int kb = 8 * v.G - 8 + 2 * v.w;
  return kb < 0 ? 0 : kb;
}
DI u32x4 at_load(const Cx& v, int i) {
  const int blk = i == 0 ? 8 * v.G + v.w : at_kb(v) + ((i - 1) & 1);
  return ld128h(v.ws, v.lane * 16, WS_QKV + ((i + 1) >> 1) * SEC_BYTES + (v.hd * 64 + blk) * 1024);
}
DI unsigned at_flag_off(const Cx& v) {
  const int i = v.lane & 7, isq = i >> 2;
  const int blk = at_kb(v) + (i & 1) + isq * (8 * v.G + v.w - at_kb(v) - (i & 1));
  const int sec = (1 - isq) * (1 + ((i >> 1) & 1));
  return WS_FQ + ((blk >> 2) * 24 + sec * 8 + (v.hd >> 2)) * 64 + (((v.hd >> 1) & 1) * 4 + (blk & 3)) * 8;
}
DI void at_stage(const Cx& v, const AtPre& a) {
  LAS char* kd = v.L + KV + 2 * v.w * 1024 + v.lane * 16;
  LAS char* vd = v.L + KV + 16384 + 2 * v.w * 1024 + (v.lane & 31) * 32 + (v.lane >> 5) * 16;
  const unsigned one = (v.lane & 8) ? 0u : 0x3f803f80u;
  *(LAS u32x4*)kd = v.w == 0 ? u32x4{one, one, one, one} : a.k0;
  *(LAS u32x4*)(kd + 1024) = a.k1;
  *(LAS u32x4*)vd = a.v0;
  *(LAS u32x4*)(vd + 1024) = a.v1;
}
DI void max16(const f32x16& s, float& m, float& m2) {
#pragma unroll
  for (int r = 0; r < 16; r += 4) {
    m = __builtin_fmaxf(__builtin_fmaxf(m, s[r]), s[r + 1]);
    m2 = __builtin_fmaxf(__builtin_fmaxf(m2, s[r + 2]), s[r + 3]);
  }
}
DI void exp_pack(const f32x16& s, u32x4& p0, u32x4& p1) {
#pragma unroll
  for (int i = 0; i < 4; ++i) {
    p0[i] = pk(__builtin_amdgcn_exp2f(s[2 * i]), __builtin_amdgcn_exp2f(s[2 * i + 1]));
    p1[i] = pk(__builtin_amdgcn_exp2f(s[8 + 2 * i]), __builtin_amdgcn_exp2f(s[9 + 2 * i]));
  }
}
#define PIN4(a, o) asm volatile("" : "+v"(a[o]), "+v"(a[o + 1]), "+v"(a[o + 2]), "+v"(a[o + 3]))
#define BF(x) __builtin_bit_cast(bf16x8, x)
DI void pin8(u32x4 (&a)[8]) { PIN4(a, 0); PIN4(a, 4); }
DI void undef8(u32x4 (&a)[8]) {
  asm volatile("" : "=v"(a[0]), "=v"(a[1]), "=v"(a[2]), "=v"(a[3]), "=v"(a[4]), "=v"(a[5]), "=v"(a[6]), "=v"(a[7]));
}
DI void o_flag(const Cx& v, int qb) { put_flag(v.ws, v.lane, WS_FO + (v.G * 32 + v.hd) * 64 + qb * 8, v.tag); }
struct AtLane {
  LAS const char *kp, *vp;
  int vstr;
  int td;
};
#define KFRAG(kb) (*(LAS const bf16x8*)(a.kp + (kb) * 1024))
#define VFRAG(kb, s) rdW<256>(a.vp + (kb) * a.vstr + (s) * 512)
DI AtLane at_lane(const Cx& v, int qb) {
  const int lane = v.lane, h = lane >> 5, qq = (lane & 15) >> 2, p = lane & 3;
  AtLane a;
  a.kp = v.L + KV + qb * 1024 + lane * 16;
  int vreal = KV + 16384 + qb * 1024 + (4 * h + qq) * 32 + (p & 1) * 16 + (p >> 1) * 8, vone = KV + (lane & 15) * 8;
  PIN(vreal); PIN(vone);
  a.vp = v.L + ((lane & 16) ? vone : vreal);
  a.vstr = (lane & 16) ? 0 : 1024;
  a.td = (lane & 31) - 4 * h;
  return a;
}
DI f32x16 at_merge(const AtLane& a, const f32x16& sd, const f32x16& sf) {
  f32x16 r;
#pragma unroll
  for (int i = 0; i < 16; ++i) r[i] = (i & 3) + 8 * (i >> 2) <= a.td ? sd[i] : sf[i];
  return r;
}
DI u32x4 at_mask(const AtLane& a, int half) {
  u32x4 k;
#pragma unroll
  for (int i = 0; i < 4; ++i) {
    const int r0 = 2 * (i & 1) + 8 * (i >> 1) + 16 * half;
    k[i] = (r0 <= a.td ? 0xffffu : 0u) | (r0 + 1 <= a.td ? 0xffff0000u : 0u);
  }
  return k;
}
DI void o_store(const Cx& v, int qb, const f32x16& o) {
  const float il = __builtin_amdgcn_rcpf(o[8]);
  u32x4 ov;
#pragma unroll
  for (int i = 0; i < 4; ++i) ov[i] = pk(o[2 * i] * il, o[2 * i + 1] * il);
  st128h(v.ws, v.lane * 16, WS_O + ((8 * v.G + qb) * 32 + v.hd) * 1024, ov);
}
template <int GP, int N> DI u32x4 a_load(const Cx& v) {
  return ld128h<(N >> 1) * 1024>(v.ws, v.lane * 16 + (c_grp(v, GP) << 12) + (N & 1) * (GP < 7 ? 112 : 32) * 1024, GP < 7 ? v.hb : v.ob);
}
DI void w_early(const Cx& v, int q, u32x4 (&af)[4][8]) {
  if (v.c) {
#pragma unroll
    for (int i = (2 * q) & 7; i < ((2 * q) & 7) + 2; ++i)
      asm volatile("buffer_load_dwordx4 %0, %1, %2, %3 offen"
                   : "=v"(af[q >> 2][i]) : "v"(c_voff(v, 5 + (q >> 2), i)), "s"(v.rwd), "s"(v.cbq) : "memory");
  } else c_piece(v, 3, 4, q);
}
template <bool FULL> DI void at_body(const Cx& v, const AtPre& ap, u32x4 (&af)[4][8]) {
  const AtLane a = at_lane(v, v.w);
  const bf16x8 q = BF(ap.q);
  const float NEG = -1e30f;
  const f32x16 zero = {};
  const int th = (v.lane >> 3) & 1;
  const unsigned hpoll = WS_FH + ((v.rt >> 1) * 16 + 4 * v.g + (v.lane & 3)) * 64 + (((v.lane >> 2) & 1) * 4 + ((2 * v.rt + th) & 3)) * 8;
  float m = NEG, m2 = NEG;
  f32x16 o = zero, cm, sc;
  u32x4 p0, p1;
  u32x2 fl;
  if (FULL) {
    bf16x8 kf[9];
#pragma unroll
    for (int kb = 0; kb < 9; ++kb) kf[(kb + 8) % 9] = KFRAG((kb + 8) % 9);
    if (v.w == 0) kf[0] = BF(ap.k0);
    f32x16 sa = MFMA(kf[8], q, zero), sb = MFMA(kf[0], q, zero);
    sc = MFMA(kf[1], q, zero);
    PIN(sa); PIN(sb); PIN(sc);
    SBAR();
    w_early(v, 0, af);
    max16(at_merge(a, sa, sb), m, m2);
    static_for<1, 8>([&](auto K_) __attribute__((always_inline)) {
      constexpr int kb = decltype(K_)::v;
      f32x16 sn;
      if constexpr (kb < 7) { sn = MFMA(kf[kb + 1], q, zero); PIN(sn); }
      SBAR();
      if (kb == 6) fl = ld64h(v.ws, hpoll);
      w_early(v, kb, af);
      max16(sc, m, m2);
      PIN(m); PIN(m2);
      SBAR();
      if constexpr (kb < 7) sc = sn;
    });
    m = swap32_max(__builtin_fmaxf(m, m2));
    cm = splat(-m);
    sc = MFMA(kf[1], q, cm);
    bf16x8 kz = kf[0], kn = kf[2], v0 = VFRAG(1, 0), v1 = VFRAG(1, 1);
    static_for<1, 8>([&](auto K_) __attribute__((always_inline)) {
      constexpr int kb = decltype(K_)::v;
      const bf16x8 n0 = VFRAG(kb + 1, 0), n1 = VFRAG(kb + 1, 1);
      f32x16 sn;
      if constexpr (kb < 7) { sn = MFMA(kn, q, cm); PIN(sn); kn = KFRAG(kb + 2); }
      else { sa = MFMA(kn, q, cm); sb = MFMA(kz, q, cm); PIN(sa); PIN(sb); }
      SBAR();
      exp_pack(sc, p0, p1);
      o = MFMA(v0, BF(p0), o);
      o = MFMA(v1, BF(p1), o);
      PIN(o);
      if (kb == 1) {
        asm volatile("s_waitcnt vmcnt(2)" : "+v"(fl)::"memory");
        check_flags(v.ws, hpoll, v.tag, v.c ? v.tag : fl);
      }
      if (!v.c) static_for<(kb - 1) * 3, (kb < 7 ? kb * 3 : 20)>([&](auto N_) __attribute__((always_inline)) {
          af[decltype(N_)::v >> 3][decltype(N_)::v & 7] = a_load<(decltype(N_)::v >> 3), (decltype(N_)::v & 7)>(v);
        });
      SBAR();
      if constexpr (kb < 7) sc = sn;
      v0 = n0;
      v1 = n1;
    });
    sc = at_merge(a, sa, sb);
    exp_pack(sc, p0, p1);
    const u32x4 k0 = at_mask(a, 0), k1 = at_mask(a, 1);
    o = MFMA(v0, BF(p0 & k0), o);
    o = MFMA(v1, BF(p1 & k1), o);
    o = MFMA(VFRAG(0, 0), BF(p0 & ~k0), o);
    o = MFMA(VFRAG(0, 1), BF(p1 & ~k1), o);
  } else {
#pragma unroll
    for (int i = 0; i < 8; ++i) w_early(v, i, af);
    max16(at_merge(a, MFMA(KFRAG(8), q, zero), splat(NEG)), m, m2);
    for (int kb = 8 - v.w; kb < 8; ++kb) max16(MFMA(KFRAG(kb), q, zero), m, m2);
    cm = splat(-swap32_max(__builtin_fmaxf(m, m2)));
    for (int kb = 8 - v.w; kb < 9; ++kb) {
      sc = MFMA(KFRAG(kb), q, cm);
      if (kb == 8) sc = at_merge(a, sc, splat(NEG));
      exp_pack(sc, p0, p1);
      o = MFMA(VFRAG(kb, 0), BF(p0), o);
      o = MFMA(VFRAG(kb, 1), BF(p1), o);
    }
    PIN(o);
    check_flags(v.ws, hpoll, v.tag, v.c ? v.tag : u32x2{~v.tag.x, 0u});
    if (!v.c) static_for<0, 20>([&](auto N_) __attribute__((always_inline)) {
      af[decltype(N_)::v >> 3][decltype(N_)::v & 7] = a_load<(decltype(N_)::v >> 3), (decltype(N_)::v & 7)>(v);
    });
  }
  o_store(v, v.w, o);
}

DI void c_scatter2(const Cx& v, LAS char* X, const f32x16& a, int nb, int j0) {
  const int t = v.lane & 31;
  LAS char* row = X + v.g * 8192 + t * 256;
  const int sw = ((v.lane >> 5) ^ (t & 15)) * 16;
#pragma unroll
  for (int j = j0; j < j0 + 2; ++j) *(LAS f32x4*)(row + (sw ^ ((4 * nb + j) * 32))) = f32x4{a[4 * j], a[4 * j + 1], a[4 * j + 2], a[4 * j + 3]};
}
DI LAS f32x4* c_cell(const Cx& v, LAS char* X, int half) {
  const int t = 8 * v.g + (v.lane >> 3);
  return (LAS f32x4*)(X + t * 256 + (((2 * (v.lane & 7) + half) ^ (t & 15)) * 16));
}
DI f32x4 c_gather(const Cx& v, LAS char* X, int half) {
  LAS const f32x4* src = c_cell(v, X, half);
  return (src[0] + src[512]) + (src[1024] + src[1536]);
}
DI f32x4 silu4(f32x4 d) {
#pragma unroll
  for (int i = 0; i < 4; ++i) d[i] *= __builtin_amdgcn_rcpf(1.f + __builtin_amdgcn_exp2f(-1.4426950408889634f * d[i]));
  return d;
}
DI u32x2 fin4(u32x2 x, f32x4 s, f32x4 g) {
  return u32x2{pk(__builtin_fmaf(s[0], g[0], __uint_as_float(x.x << 16)), __builtin_fmaf(s[1], g[1], __uint_as_float(x.x & 0xffff0000u))),
               pk(__builtin_fmaf(s[2], g[2], __uint_as_float(x.y << 16)), __builtin_fmaf(s[3], g[3], __uint_as_float(x.y & 0xffff0000u)))};
}
DI LAS char* c_xd(const Cx& v, int half) { return v.L + (1 + half) * REG; }
DI LAS char* c_xs(const Cx& v, int half) { return v.L + (half ? 0 : 4 * REG); }
DI unsigned c_xoff(const Cx& v) { return (64 * v.rt + 8 * v.w + (v.lane >> 3)) * 1024 + v.ct * 128 + (v.lane & 7) * 16; }
DI u32x4 x_load(const Params& P, const Cx& v) {
  u32x4 r;
  asm volatile("buffer_load_dwordx4 %0, %1, %2, 0 offen" : "=v"(r) : "v"(c_xoff(v)), "s"(mk_rsrc(P.x, 2048 * 512 * 2)) : "memory");
  return r;
}
struct CFrag {
  LAS const char *b0, *b1;
  bf16x8 wf[4][2];
};
DI CFrag c_frag(const Cx& v) {
  const int lane = v.lane, h = lane >> 5, qq = (lane & 15) >> 2, p = lane & 3, g1 = (lane >> 4) & 1;
  CFrag c;
  c.b0 = v.L + v.g * 8192 + (4 * h + qq) * 128 + (g1 + (qq & 2)) * 32 + 8 * p;
  c.b1 = v.L + v.g * 8192 + (4 * h + qq) * 128 + (g1 + (2 ^ (qq & 2))) * 32 + 8 * p;
  return c;
}
template <int S> DI void c_rd(CFrag& c) {
  constexpr int RG[4] = {0, 1, 2, 4};
  c.wf[S & 3][0] = rdW<1024>(c.b0 + RG[(S >> 2) & 3] * REG + (S & 3) * 2048);
  c.wf[S & 3][1] = rdW<1024>(c.b1 + RG[(S >> 2) & 3] * REG + (S & 3) * 2048);
}
template <int GP, bool STAGED>
DI void c_close(const Cx& v, const CFrag& c, u32x4 (&a0)[8], u32x4 (&a1)[8], f32x16 (&ac)[2][2], LAS char* x0, LAS char* x1) {
  constexpr int RG[4] = {0, 1, 2, 4};
  bf16x8 wa[8], wb[8];
#define C_OFF(k) (RG[(GP + ((k) >> 2)) & 3] * REG + ((k) & 3) * 2048)
  wa[0] = rdW<1024>(c.b0 + C_OFF(0));
  wa[1] = rdW<1024>(c.b0 + C_OFF(1));
  static_for<0, 8>([&](auto K_) __attribute__((always_inline)) {
    constexpr int k = decltype(K_)::v;
    u32x4(&a)[8] = k < 4 ? a0 : a1;
    if constexpr (STAGED && !(k & 1)) { wait_vm<13 - 2 * k>(); PIN4(a, 2 * (k & 3)); }
    ac[0][0] = MFMA(wa[k], BF(a[2 * (k & 3)]), ac[0][0]);
    PIN(ac[0][0]);
    SBAR();
    if constexpr (k < 6) wa[k + 2] = rdW<1024>(c.b0 + C_OFF(k + 2));
    wb[k] = rdW<1024>(c.b1 + C_OFF(k));
    SBAR();
    ac[0][1] = MFMA(wa[k], BF(a[2 * (k & 3) + 1]), ac[0][1]);
    PIN(ac[0][1]);
    SBAR();
  });
#pragma unroll
  for (int k = 0; k < 8; ++k) PIN(wb[k]);
  static_for<0, 24>([&](auto N_) __attribute__((always_inline)) {
    constexpr int n = decltype(N_)::v, k = n & 7, hf = n >> 3;
    if constexpr (n < 16) {
      ac[1][hf] = MFMA(wb[k], BF((k < 4 ? a0 : a1)[2 * (k & 3) + hf]), ac[1][hf]);
      PIN(ac[1][hf]);
      SBAR();
    }
    constexpr int w = n < 16 ? ((n & 1) && n >= 1 && n < 13 ? (n - 1) / 2 : -1) : (n < 18 ? n - 10 : -1);
    if constexpr (w >= 0) {
      constexpr int nb = w >> 2, th = (w >> 1) & 1;
      c_scatter2(v, th ? x1 : x0, ac[nb][th], nb, (w & 1) * 2);
    }
    SBAR();
  });
}
DI void gemm_role(const Params& P, const Cx& v, u32x4 (&af)[4][8], u32x4& xv) {
  CFrag c = c_frag(v);
  f32x16 d[2][2] = {};
  c_rd<0>(c);
  c_rd<1>(c);
  static_for<4, 8>([&](auto N_) __attribute__((always_inline)) { af[2][decltype(N_)::v] = a_load<2, decltype(N_)::v>(v); });
  __builtin_amdgcn_s_setprio(0);
  wait_vm<17>();
  pin8(af[0]);
  static_for<0, 20>([&](auto S_) __attribute__((always_inline)) {
    constexpr int s = decltype(S_)::v, gp = s >> 2, k = s & 3;
    if constexpr (k == 2 && gp < 4) {
      constexpr int N[4] = {8, 17, 20, 12};
      wait_vm<N[gp]>();
      pin8(af[(gp + 1) & 3]);
      if (gp == 0) o_flag(v, v.w);
    }
    if (k == 0 && gp >= 2) hs_set(v, 0, gp);
    if constexpr (s < 18) c_rd<s + 2>(c);
    static_for<0, 4>([&](auto J_) __attribute__((always_inline)) {
      constexpr int j = decltype(J_)::v, n = 4 * k + j;
      d[j >> 1][j & 1] = MFMA(c.wf[k][j >> 1], BF(af[gp & 3][2 * k + (j & 1)]), d[j >> 1][j & 1]);
      PIN(d[j >> 1][j & 1]);
      SBAR();
      if constexpr (gp < 4 && !(n & 1)) af[(gp + 3) & 3][n / 2] = a_load<gp + 3, n / 2>(v);
      if constexpr (gp == 1 && (n & 1)) c_piece(v, 4, 0, n / 2);
      if (s == 16 && j == 0) xv = x_load(P, v);
      SBAR();
    });
  });
  hs_set(v, 0, 5);
  wait_vm<1>();
  pin8(af[1]);
  pin8(af[2]);
  hs_wait(v, 1, 6);
  c_close<5, false>(v, c, af[1], af[2], d, c_xd(v, 0), c_xd(v, 1));
  asm volatile("s_waitcnt lgkmcnt(0)" ::: "memory");
  hs_set(v, 2 + v.g, 1);
  hs_wait(v, 2, 1);
#pragma unroll
  for (int i = 0; i < 4; ++i) *c_cell(v, c_xd(v, i >> 1), i & 1) = silu4(c_gather(v, c_xd(v, i >> 1), i & 1));
}
DI void side_role(const Params& P, const Cx& v, u32x4 (&af)[4][8], u32x4& xv) {
  wait_vm<0>();
  o_flag(v, v.w);
  __builtin_amdgcn_s_setprio(0);
  const unsigned oown = WS_FO + ((v.rt >> 2) * 32 + 8 * v.g + (v.lane & 7)) * 64 + ((2 * v.rt + ((v.lane >> 3) & 1)) & 7) * 8;
  const unsigned opoll = blockIdx.x == 0 && v.lane >= 32 ? WS_FO + (v.g * 64 + v.lane - 32) * 64 : oown;
  auto group = [&](int gp) __attribute__((always_inline)) {
    hs_wait(v, 0, gp - 3);
#pragma unroll
    for (int i = 0; i < 8; ++i)
      glds(gp < 7 ? v.rwd : v.rwo, c_voff(v, gp, i), gp < 7 ? v.cbq : v.cbo - 32768, ((gp & 3) == 3 ? 4 : gp & 3) * REG + v.g * 8192 + i * 1024);
  };
  pin8(af[0]);
  pin8(af[1]);
#pragma unroll
  for (int j = 0; j < 2; ++j) {
    hs_wait(v, 0, 2 + j);
#pragma unroll
    for (int i = 0; i < 8; ++i) *(LAS u32x4*)(v.L + (1 + j) * REG + v.g * 8192 + i * 1024 + v.lane * 16) = af[j][i];
  }
  asm volatile("s_waitcnt lgkmcnt(0)" ::: "memory");
  hs_set(v, 1, 6);
  group(7);
  u32x2 fl = ld64h(v.ws, opoll);
  group(8);
  asm volatile("s_waitcnt vmcnt(8)" : "+v"(fl)::"memory");
  check_flags(v.ws, opoll, v.tag, fl);
  if (blockIdx.x == 0) fl = ld64h(v.ws, opoll + (v.lane >= 32 ? 32 * 64 : 0));
  static_for<0, 16>([&](auto N_) __attribute__((always_inline)) {
    af[2 + decltype(N_)::v / 8][decltype(N_)::v & 7] = a_load<7 + decltype(N_)::v / 8, (decltype(N_)::v & 7)>(v);
  });
  xv = x_load(P, v);
  const CFrag c = c_frag(v);
  f32x16 sm[2][2] = {};
  asm volatile("s_waitcnt vmcnt(17)" : "+v"(fl)::"memory");
  c_close<7, true>(v, c, af[2], af[3], sm, c_xs(v, 0), c_xs(v, 1));
  if (blockIdx.x == 0) check_flags(v.ws, opoll + (v.lane >= 32 ? 32 * 64 : 0), v.tag, fl);
}

struct AB {
  bf16x8 X[32];
  bf16x8 wf[3][2];
  bf16x8 sel;
  unsigned nm;
  float r, sd;
  int gr;
  int rb, cg, strip0, dblA, dblB;
  int kx;
  unsigned voffX, voffQ, voffF;
  rsrc_t rx, rq, rf;
};
DI void x_piece(const Cx& v, const AB& a, int kc, int slot, int i) {
  glds(a.rx, a.voffX ^ (64 * i), (a.rb * 128 + 16 * v.w + 4 * i) * 1024 + (kc ^ a.kx) * 256, slot * SLOT + (4 * v.w + i) * 1024);
}
DI void s_piece(const Cx& v, const AB& a, int kq, int i) {
  glds(a.rq, a.voffQ, (128 * (kq ^ a.kx) + 16 * v.w + 8 * i) * 3072 + a.strip0 * 128, SBUF + (kq & 1) * SUB1 + (2 * v.w + i) * 1024);
}
DI void f_piece(const Cx& v, const AB& a, int dbl, int kq, int slot, int i) {
  glds(a.rf, a.voffF, (128 * (kq ^ a.kx) + 32 * v.g + 8 * i) * 3584 + dbl * 128, slot * SLOT + v.c * SUB1 + v.g * 4096 + i * 1024);
}
template <bool TB> DI void u_piece(const Cx& v, const AB& a, int kq, int i) {
  const int slot = 1 + 2 * (kq & 1);
  if (TB) glds(a.rq, a.voffQ, (128 * (kq ^ a.kx) + 32 * v.g + 8 * i) * 3072 + (a.strip0 + 1 + v.c) * 128,
               slot * SLOT + v.c * SUB1 + v.g * 4096 + i * 1024);
  else f_piece(v, a, a.dblA, kq, slot, i);
}
DI void cs_put(const Cx& v, const f32x4& c0, const f32x4& c1, const f32x4& c2) {
  const bool hi = v.lane & 16;
  *(LAS f32x4*)(v.L + AUX + v.w * 1024 + v.lane * 16) = f32x4{hi ? c0[1] : c0[0], hi ? c1[1] : c1[0], hi ? c2[1] : c2[0], 0.f};
}
DI f32x4 cs_get(const Cx& v) {
  LAS const f32x4* p = (LAS const f32x4*)(v.L + AUX + v.c * 4096 + v.lane * 16);
  return (p[0] + p[64]) + (p[128] + p[192]);
}
DI void q_store(const Cx& v, const AB& a, const f32x16& acc, int strip, int hh) {
  const float sc = strip < 8 ? a.r * QSCALE : a.r;
  u32x4 o;
#pragma unroll
  for (int i = 0; i < 4; ++i) o[i] = pk(acc[8 * hh + 2 * i] * sc, acc[8 * hh + 2 * i + 1] * sc);
  st128h(v.ws, v.lane * 16, WS_QKV + ((strip * 4 + 2 * v.c + hh) * 64 + 4 * a.rb + v.g) * 1024, o);
}
DI void q_flag(const Cx& v, const AB& a, int strip) { put_flag(v.ws, v.lane, WS_FQ + (a.rb * 24 + strip) * 64 + v.w * 8, v.tag); }
DI void put_h(const Cx& v, const AB& a, int dbl, int j, const u32x4& hv) {
  st128h(v.ws, v.lane * 16, WS_H + ((4 * a.rb + v.g) * 112 + 4 * dbl + 2 * v.c + j) * 1024, hv);
}
struct Bases { unsigned b[4]; };
template <bool F> DI Bases mk_bases(const Cx& v, const AB& a, int base) {
  int l = v.lane;
  PIN(l);
  const int qq = (l & 15) >> 2, g1 = (l >> 4) & 1;
  const int o = base + (F ? g1 * SUB1 : 0) + (8 * (l >> 5) + qq) * 128 + ((2 * v.c) ^ (qq & 2) ^ g1) * 32 + 8 * (l & 3);
  Bases r;
#pragma unroll
  for (int i = 0; i < 4; ++i) { r.b[i] = o + (i ^ a.gr) * 4096; PIN(r.b[i]); }
  return r;
}
DI void bump(Bases& B, int d) {
#pragma unroll
  for (int i = 0; i < 4; ++i) { B.b[i] += d; PIN(B.b[i]); }
}
#define WRD(B, off, i) rdW<512>((LAS const char*)(size_t)((B).b[(i) >> 1]) + (off) + ((i) & 1) * 2048)
#define WRX(B, off, i) rdW<512>((LAS const char*)(size_t)((B).b[(i) >> 1] ^ 32u) + (off) + ((i) & 1) * 2048)

DI u32x2 make_tag(u32x2 e) {
  const unsigned long long z = ((unsigned long long)__builtin_amdgcn_readfirstlane(e.y) << 32 | (unsigned)__builtin_amdgcn_readfirstlane(e.x));
  const unsigned long long t = (z ^ (z >> 31)) * 0x9E3779B97F4A7C15ull + 0xD1B54A32D192ED03ull;
  return u32x2{(unsigned)t, (unsigned)(t >> 32)};
}

DI void stats_sum(const u32x4& xr, float& s1, float& s2) {
  s1 = s2 = 0.f;
#pragma unroll
  for (int d = 0; d < 4; ++d) {
    const float lo = __uint_as_float(xr[d] << 16), hi = __uint_as_float(xr[d] & 0xffff0000u);
    s1 += lo + hi;
    s2 = __builtin_fmaf(lo, lo, __builtin_fmaf(hi, hi, s2));
  }
}
DI void stats_put(const Cx& v, const AB& a, float s1, float s2) {
  const float mean = s1 * (1.f / 512.f);
  const float var = __builtin_fmaxf(__builtin_fmaf(-mean, mean, s2 * (1.f / 512.f)), 0.f);
  const float sd = __builtin_amdgcn_sqrtf(rbf(rbf(var) + 1e-5f));
  put_flag(v.ws, v.lane ^ 63, WS_ST + (a.rb * 128 + a.cg * 8 + v.w) * 8, u32x2{pk(-mean, sd), v.tag.x});
}
DI void row_stats(const Cx& v, AB& a, unsigned g) {
  a.sd = __uint_as_float(g & 0xffff0000u);
  a.r = __builtin_amdgcn_rcpf(a.sd);
  a.nm = v.lane < 32 ? (g & 0xffffu) | (g << 16) : 0u;
}
DI void rd_step(AB& a, const Bases& B, int s) {
  a.wf[(s + 2) % 3][0] = WRD(B, ((s >> 3) & 1) * SLOT, s & 7);
  a.wf[(s + 2) % 3][1] = WRX(B, ((s >> 3) & 1) * SLOT, s & 7);
}
template <bool TB, int Q>
DI void f_step(Cx& v, AB& a, const Bases& BS, Bases& BU, f32x16& ps, f32x16& a0, f32x16& a1, f32x4& cs, f32x4& c0, f32x4& c1,
               u32x2& st, u32x4& xr) {
  const int t = v.lane & 31;
  const unsigned u = (((v.lane >> 5) ^ (t & 15)) * 16) ^ (64 * a.gr);
  LAS const char* xb = v.L + (32 * v.g + t) * 256;
  bf16x8 ws[3];
  float s1, s2;
  f32x4 cp;
  auto rdx = [&](int q, int i) __attribute__((always_inline)) { a.X[8 * q + i] = *(LAS const bf16x8*)(xb + 2 * (q & 1) * SLOT + (u ^ (32 * i))); };
  auto rd = [&](int i) __attribute__((always_inline)) {
    a.wf[i % 3][0] = WRD(BU, 0, i);
    a.wf[i % 3][1] = TB ? WRD(BU, SUB1, i) : WRX(BU, 0, i);
    ws[i % 3] = WRD(BS, (Q & 1) * SUB1, i);
  };
  auto req = [&](int n) __attribute__((always_inline)) {
    if (Q < 3) { if (n < 2) s_piece(v, a, Q + 1, n); else u_piece<TB>(v, a, Q + 1, n - 2); }
    else if (n < 4) f_piece(v, a, a.dblB, 1, 1, n);
  };
  rd(0);
  rd(1);
  static_for<0, 8>([&](auto I_) __attribute__((always_inline)) {
    constexpr int i = decltype(I_)::v;
    if (i == 4) {
      if (Q < 3) wait_bar<Q == 2 ? 8 : 6, 15>();
      else {
        wait_bar<4, 6>();
        cp = cs_get(v);
      }
    }
    const bf16x8 x = a.X[8 * Q + i];
    a0 = MFMA(a.wf[i % 3][0], x, a0);
    PIN(a0);
    SBAR();
    if (Q < 3 && i >= 4) { rdx(Q + 1, 2 * i - 8); rdx(Q + 1, 2 * i - 7); }
    if (i < 2) { COLSUM(a.wf[i % 3][0], c0); PIN(c0); }
    if (i < 3) req(2 * i);
    if (i >= 4) {
      if (Q < 2) x_piece(v, a, Q + 2, 2 * (Q & 1), i - 4);
      else f_piece(v, a, a.dblB, 2 * (Q & 1), 2 * (Q & 1), i - 4);
    }
    if (Q == 3 && i == 5) { ln_fix(a0, cp[0], a.nm); PIN(a0); }
    SBAR();
    a1 = MFMA(a.wf[i % 3][1], x, a1);
    PIN(a1);
    SBAR();
    if (i < 2) { COLSUM(a.wf[i % 3][1], c1); PIN(c1); }
    if (i < 3) req(2 * i + 1);
    if (Q == 3 && i == 6) { ln_fix(a1, cp[1], a.nm); PIN(a1); }
    if (Q == 1) {
      if (i == 3) { PIN(st); PIN(xr); v.tag = make_tag(st); stats_sum(xr, s1, s2); PIN(s1); PIN(s2); }
      if (i == 4) { s1 = wave_sum63(s1); PIN(s1); }
      if (i == 5) { s2 = wave_sum63(s2); PIN(s2); }
      if (i == 6) stats_put(v, a, s1, s2);
    }
    if (Q == 2 && i == 3) st = ld64h(v.ws, WS_ST + (a.rb * 128 + 32 * v.g + (v.lane & 31)) * 8);
    if (Q == 3 && i == 2) {
      PIN(st);
      check_granule(v.ws, WS_ST + (a.rb * 128 + 32 * v.g + (v.lane & 31)) * 8, v.tag.x, st);
      row_stats(v, a, st.x);
      PIN(a.nm); PIN(a.r); PIN(a.sd);
    }
    SBAR();
    const bf16x8 wsi = ws[i % 3];
    ps = MFMA(wsi, x, ps);
    PIN(ps);
    SBAR();
    if (i < 6) rd(i + 2);
    if (i < 2) { COLSUM(wsi, cs); PIN(cs); }
    if (Q == 3) {
      if (i == 2) cs_put(v, c0, c1, cs);
      if (i == 4) { ln_fix(ps, cp[2], a.nm); PIN(ps); }
      if (i == 6) BU = mk_bases<true>(v, a, 0);
      if (i >= 6) rd_step(a, BU, i - 6);
    } else if (i == 6) bump(BU, (Q & 1) ? -2 * SLOT : 2 * SLOT);
    if (Q == 1 && i == 7) {
      const int per = v.w == 1 ? 16 : 56;
      asm volatile("buffer_load_dword %0, %1, %2, 0 offen"
                   : "=v"(xr.x) : "v"(v.lane < per && v.w < 2 ? (blockIdx.x * per + v.lane) * 128 : 0x40000000u), "s"(v.w == 0 ? v.rwd : v.rwo)
                   : "memory");
    }
    SBAR();
  });
}
template <bool TB>
DI void f_step0(Cx& v, AB& a, const Bases& BS, Bases& BU, f32x16& ps, f32x16& a0, f32x16& a1, f32x4& cs, f32x4& c0, f32x4& c1,
                u32x2& st, u32x4& xr) {
  const int t = v.lane & 31;
  const unsigned u = (((v.lane >> 5) ^ (t & 15)) * 16) ^ (64 * a.gr);
  LAS const char* xb = v.L + (32 * v.g + t) * 256;
  bf16x8 ws[8];
#pragma unroll
  for (int i = 0; i < 3; ++i) {
    a.X[i] = *(LAS const bf16x8*)(xb + (u ^ (32 * i)));
    ws[i] = WRD(BS, 0, i);
  }
#pragma unroll
  for (int i = 0; i < 8; ++i) {
    if (i < 5) {
      a.X[i + 3] = *(LAS const bf16x8*)(xb + (u ^ (32 * (i + 3))));
      ws[i + 3] = WRD(BS, 0, i + 3);
    }
    if (i < 4) x_piece(v, a, 1, 2, i);
    else if (i < 6) s_piece(v, a, 1, i - 4);
    else u_piece<TB>(v, a, 1, i - 6);
    ps = MFMA(ws[i], a.X[i], ps);
    if (i < 2) COLSUM(ws[i], cs);
    PIN(ps); PIN(cs);
    SBAR();
  }
  wait_bar<8, 0>();
  auto rd = [&](int i) __attribute__((always_inline)) {
    a.wf[i % 3][0] = WRD(BU, 0, i);
    a.wf[i % 3][1] = TB ? WRD(BU, SUB1, i) : WRX(BU, 0, i);
  };
  rd(0);
  rd(1);
  static_for<0, 8>([&](auto I_) __attribute__((always_inline)) {
    constexpr int i = decltype(I_)::v;
    if (i == 4) wait_bar<10, 15>();
    const bf16x8 w0 = a.wf[i % 3][0], w1 = a.wf[i % 3][1];
    a0 = MFMA(w0, a.X[i], a0);
    PIN(a0);
    SBAR();
    if (i < 6) rd(i + 2);
    if (i >= 4) a.X[2 * i] = *(LAS const bf16x8*)(xb + 2 * SLOT + (u ^ (32 * (2 * i - 8))));
    if (i < 2) { COLSUM(w0, c0); PIN(c0); u_piece<TB>(v, a, 1, 2 + i); }
    else if (i < 6) x_piece(v, a, 2, 0, i - 2);
    SBAR();
    a1 = MFMA(w1, a.X[i], a1);
    PIN(a1);
    SBAR();
    if (i >= 4) a.X[2 * i + 1] = *(LAS const bf16x8*)(xb + 2 * SLOT + (u ^ (32 * (2 * i - 7))));
    if (i < 2) { COLSUM(w1, c1); PIN(c1); }
    if (i == 1) {
      st = ld64h(v.ws, WS_EPOCH);
      asm volatile("buffer_load_dwordx4 %0, %1, %2, %3 offen"
                   : "=v"(xr) : "v"(v.lane * 16), "s"(a.rx), "s"(RFL((a.rb * 128 + a.cg * 8 + v.w) * 1024)) : "memory");
    }
    if (i == 6) bump(BU, 2 * SLOT);
    SBAR();
  });
}
template <bool TB> DI void front(Cx& v, AB& a, Bases& BU, f32x16& ps, f32x16& a0, f32x16& a1) {
  const Bases BS = mk_bases<false>(v, a, SBUF);
  BU = mk_bases<!TB>(v, a, SLOT);
  f32x4 cs = {}, c0 = {}, c1 = {};
  u32x2 st;
  u32x4 xr;
  wait_bar<4, 0>();
  f_step0<TB>(v, a, BS, BU, ps, a0, a1, cs, c0, c1, st, xr);
  wait_bar<4, 0>();
  f_step<TB, 1>(v, a, BS, BU, ps, a0, a1, cs, c0, c1, st, xr);
  wait_bar<6, 0>();
  f_step<TB, 2>(v, a, BS, BU, ps, a0, a1, cs, c0, c1, st, xr);
  wait_bar<4, 0>();
  PIN(xr);
  f_step<TB, 3>(v, a, BS, BU, ps, a0, a1, cs, c0, c1, st, xr);
  wait_bar<4, 8>();
}
template <bool TB> DI void body(Cx& v, AB& a, Bases& B, const f32x16& ps, f32x16& p0, f32x16& p1, AtPre& ap) {
  f32x16 a0 = {}, a1 = {};
  f32x4 c0 = {}, c1 = {};
  float hlo;
  f32x4 cp;
  u32x4 hv;
  u32x2 fl;
  unsigned fo;
  static_for<0, 32>([&](auto S_) __attribute__((always_inline)) {
    constexpr int s = decltype(S_)::v, kq = s >> 3, i = s & 7;
    if (s == 2) { int l = v.lane; PIN(l); a.sel = mk_sel(l); }
    const bf16x8 w0 = a.wf[(s + 2) % 3][0], w1 = a.wf[(s + 2) % 3][1];
    constexpr bool CS = (i >> 1) == 1;
    constexpr int e = TB ? -1 : (s == 30 ? 14 : ((s & 1) && s >= 3 ? (s == 31 ? 15 : (s - 3) >> 1) : -1));
    float ga, ex;
    a0 = MFMA(w0, a.X[s], a0);
    PIN(a0);
    SBAR();
    if (s == 14) bump(B, 2 * SLOT);
    if (s < 30) rd_step(a, B, s + 2);
    if (s == 7) c_setup(v);
    if (CS) { COLSUM(w0, c0); PIN(c0); }
    if ((i & 1) == 0) {
      if (kq == 0) f_piece(v, a, a.dblB, 3, 3, i >> 1);
      else c_piece(v, kq - 1, kq - 1, i >> 1);
    }
    if constexpr (e >= 0) {
      ga = (e < 8 ? p0 : p1)[e & 7] * a.r;
      ex = geglu_exp(ga);
      ga *= (e < 8 ? p0 : p1)[8 + (e & 7)];
      PIN(ga); PIN(ex);
    }
    SBAR();
    a1 = MFMA(w1, a.X[s], a1);
    PIN(a1);
    SBAR();
    if (CS) { COLSUM(w1, c1); PIN(c1); }
    if constexpr (e >= 0) {
      const float hx = geglu_fin(ga, ex, a.sd);
      if (e & 1) { hv[(e & 7) >> 1] = pk(hlo, hx); PIN(hv[(e & 7) >> 1]); }
      else { hlo = hx; PIN(hlo); }
      if (e == 15) put_h(v, a, a.dblA, 1, hv);
    }
    if (s < 2) q_store(v, a, ps, a.strip0, s);
    if (s == 8) { wait_vm<4>(); q_flag(v, a, a.strip0); }
    if (!TB && s == 18) put_h(v, a, a.dblA, 0, hv);
    if (TB) {
      if (s == 3 || s == 5) q_store(v, a, p0, a.strip0 + 1, (s - 3) >> 1);
      if (s == 7 || s == 9) q_store(v, a, p1, a.strip0 + 2, (s - 7) >> 1);
      if (s == 14) { wait_vm<3>(); q_flag(v, a, a.strip0 + 1); q_flag(v, a, a.strip0 + 2); }
    }
    if (s == 28) {
      cs_put(v, c0, c1, c1);
      SBAR();
      wait_bar<63, 0>();
      cp = cs_get(v);
    }
    if (s == 30) { ln_fix(a0, cp[0], a.nm); PIN(a0); }
    if (s == 31) { ln_fix(a1, cp[1], a.nm); PIN(a1); }
    if (s == 23) { fo = at_flag_off(v); fl = ld64h(v.ws, fo); }
    if (s == 27) {
      asm volatile("s_waitcnt vmcnt(2)" : "+v"(fl)::"memory");
      check_flags(v.ws, fo, v.tag, fl);
      ap.k0 = at_load(v, 1);
    }
    if (s == 28) ap.k1 = at_load(v, 2);
    if (s == 29) ap.v0 = at_load(v, 3);
    if (s == 30) ap.v1 = at_load(v, 4);
    if (s == 31) ap.q = at_load(v, 0);
    SBAR();
    if (i == 7 && kq < 3) {
      if (kq < 2) wait_bar<4, 8>();
      else wait_bar<63, 8>();

    }
  });
  p0 = a0;
  p1 = a1;
}
DI void ab_tail(const Cx& v, const AB& a, int dbl, const f32x16& p0, const f32x16& p1, AtPre& ap) {
  u32x4 hv;
  static_for<0, 8>([&](auto I_) __attribute__((always_inline)) {
    constexpr int i = decltype(I_)::v, j = 2 * (i & 3);
    const f32x16& p = i < 4 ? p0 : p1;
    hv[i & 3] = pk(geglu(p[j], p[8 + j], a.r, a.sd), geglu(p[j + 1], p[9 + j], a.r, a.sd));
    PIN(hv[i & 3]);
    if constexpr ((i & 3) == 3) put_h(v, a, dbl, i >> 2, hv);
    SBAR();
  });
  asm volatile("s_waitcnt vmcnt(2)\n\ts_barrier" : "+v"(ap.q), "+v"(ap.k0), "+v"(ap.k1), "+v"(ap.v0), "+v"(ap.v1)::"memory");
  at_stage(v, ap);
  wait_vm<0>();
  put_flag(v.ws, v.lane, WS_FH + (a.rb * 16 + a.cg) * 64 + v.w * 8, v.tag);
  wait_bar<63, 0>();
}

template <bool TB> DI void ab_path(Cx& v, AB a, AtPre& ap) {
  int l = v.lane;
  PIN(l);
  a.voffX = (l >> 4) * 1024 + (((l & 15) ^ (l >> 4)) * 16);
  a.voffQ = w_voff(l, 3072);
  a.voffF = w_voff(l, 3584, v.c);
  a.sel = mk_sel(l);
#pragma unroll
  for (int i = 0; i < 4; ++i) u_piece<TB>(v, a, 0, i);
  f32x16 ps = {}, p0 = {}, p1 = {};
  Bases B;
  front<TB>(v, a, B, ps, p0, p1);

  body<TB>(v, a, B, ps, p0, p1, ap);

  ab_tail(v, a, a.dblB, p0, p1, ap);
}

__global__ __launch_bounds__(512, 2) void fused_kernel(Params P) {
  extern __shared__ __attribute__((aligned(16))) char smem[];
  Cx v;
  v.L = (LAS char*)smem;
  v.lane = threadIdx.x & 63;
  v.w = __builtin_amdgcn_readfirstlane(threadIdx.x >> 6);
  v.g = v.w & 3;
  v.c = v.w >> 2;
  const int xc = blockIdx.x & 7, idx = blockIdx.x >> 3;
  v.ws = mk_rsrc(P.ws, WS_END);
  v.rwd = mk_rsrc(P.wd, 1792 * 512 * 2);
  v.rwo = mk_rsrc(P.wo, 512 * 512 * 2);
  v.rt = v.hd = xc * 4 + (idx & 3);
  v.ct = v.G = idx >> 2;
  AtPre ap;

  {
    AB a;
    a.rb = (xc & 1) * 8 + (idx & 7);
    const int cq = xc >> 1, j = idx >> 3;
    const bool typeB = j == 3;
    a.cg = cq * 4 + j;
    a.gr = v.g;
    a.strip0 = 6 * cq + j;
    a.dblA = 7 * cq + 2 * j;
    a.dblB = typeB ? a.dblA : a.dblA + 1;
    a.kx = a.rb & 3;
    a.rx = mk_rsrc(P.x, 2048 * 512 * 2);
    a.rq = mk_rsrc(P.wqkv, 512 * 1536 * 2);
    a.rf = mk_rsrc(v.c ? P.wu : P.wg, 512 * 1792 * 2);
    a.voffX = (v.lane >> 4) * 1024 + (((v.lane & 15) ^ (v.lane >> 4)) * 16);
    a.voffQ = w_voff(v.lane, 3072);
#pragma unroll
    for (int i = 0; i < 4; ++i) x_piece(v, a, 0, 0, i);
#pragma unroll
    for (int i = 0; i < 2; ++i) s_piece(v, a, 0, i);
    if (typeB) ab_path<true>(v, a, ap);
    else ab_path<false>(v, a, ap);
  }


  c_setup(v);
  v.tag = u32x2{uni(v.tag.x), uni(v.tag.y)};
  u32x4 af[4][8];
  __builtin_amdgcn_s_setprio(2);
  if (v.G) at_body<true>(v, ap, af);
  else at_body<false>(v, ap, af);

  c_setup(v);
  u32x4 xv;
  if (v.c) {
    side_role(P, v, af, xv);
    undef8(af[0]); undef8(af[1]); undef8(af[2]);
  }
  if (!uni(v.c)) gemm_role(P, v, af, xv);
  asm volatile("s_waitcnt vmcnt(0) lgkmcnt(0)\n\ts_barrier" : "+v"(xv)::"memory");
  if (blockIdx.x == 0 && v.w == 0) put_flag(v.ws, v.lane, WS_EPOCH, v.tag);
  const f32x4 ga = *c_cell(v, c_xd(v, v.c), 0), gb = *c_cell(v, c_xd(v, v.c), 1);
  const u32x2 y0 = fin4(u32x2{xv.x, xv.y}, c_gather(v, c_xs(v, v.c), 0), ga), y1 = fin4(u32x2{xv.z, xv.w}, c_gather(v, c_xs(v, v.c), 1), gb);
  __builtin_amdgcn_raw_buffer_store_b128(u32x4{y0.x, y0.y, y1.x, y1.y}, mk_rsrc(P.out, 2048 * 512 * 2), c_xoff(v), 0, 16);
}
}
extern "C" void kernel_launch(void* const* d_in, const int* in_sizes, int n_in, void* d_out, int out_size, void* d_ws,
                              size_t ws_size, hipStream_t stream) {
  Params P;
  P.x = d_in[0]; P.wqkv = d_in[1]; P.wo = d_in[2];
  P.wg = d_in[4]; P.wu = d_in[5]; P.wd = d_in[6];
  P.out = d_out; P.ws = d_ws;
  (void)hipFuncSetAttribute((const void*)fused_kernel, hipFuncAttributeMaxDynamicSharedMemorySize, LDS_BYTES);
  fused_kernel<<<256, 512, LDS_BYTES, stream>>>(P);
}
```
